```python
import torch

def _softmax(x, axis=-1):
    return torch.softmax(x, dim=axis)
def _gelu(x):
    return torch.nn.functional.gelu(x, approximate="tanh")
def _silu(x):
    return torch.nn.functional.silu(x)
def _sigmoid(x):
    return torch.sigmoid(x)
def _layernorm(x, axis=-1, eps=1e-5):
    m = x.mean(dim=axis, keepdim=True)
    v = x.var(dim=axis, keepdim=True, unbiased=False)
    return (x - m) / torch.sqrt(v + eps)
def _scatter_add(tgt, idx, src, axis=0):
    return tgt.clone().index_add_(axis, idx, src)
def _segment_sum(src, seg, n):
    out = torch.zeros((n,) + src.shape[1:], dtype=src.dtype)
    return out.index_add_(0, seg, src)
def _pad(x, pw):
    flat = tuple(v for pair in reversed(pw) for v in pair)
    return torch.nn.functional.pad(x, flat)

kernel_name = 'gated_mix__sliding_block__mha_block__prompt'

def setup_inputs():

    _w = (lambda a: (lambda t: t.to(torch.bfloat16) if t.is_floating_point() else t)(torch.as_tensor(a)))
    def _A_setup():
        B, S, D = 1, 2048, 1024
        return {
            "x": _w((lambda *s: torch.randn(*s, dtype=torch.bfloat16) * 0.05)(B, S, D)),
            "wqkv": _w((lambda *s: torch.randn(*s, dtype=torch.bfloat16) * 0.8)(D, 3 * D)),
            "wo": _w((lambda *s: torch.randn(*s, dtype=torch.bfloat16) * 0.05)(D, D)),
        }
    _A_in = _A_setup()

    def _B_setup():
        B, S, D, H = 1, 2048, 1024, 32
        return {
            "x": _w((lambda *s: torch.randn(*s, dtype=torch.bfloat16) * 0.05)(B, S, D)),
            "wqkv": _w((lambda *s: torch.randn(*s, dtype=torch.bfloat16) * 0.8)(D, 3 * D)),
            "wo": _w((lambda *s: torch.randn(*s, dtype=torch.bfloat16) * 0.1)(D, D)),
        }
    _B_in = _B_setup()

    return {
        "A_x": _A_in["x"],
        "A_wqkv": _A_in["wqkv"],
        "A_wo": _A_in["wo"],
        "B_x": _B_in["x"],
        "B_wqkv": _B_in["wqkv"],
        "B_wo": _B_in["wo"],
        "gate_w": _w((lambda *s: torch.randn(*s, dtype=torch.bfloat16) * 0.05)(1024) * 16.0),
    }

def reference(A_x, A_wqkv, A_wo, B_x, B_wqkv, B_wo, gate_w):
    def _A_ref(x, wqkv, wo):
        B, S, D = x.shape
        H = 32; Dh = D // H; W = 256
        qkv = (lambda x, *d: x.permute(*d))((x @ wqkv).reshape(B, S, 3, H, Dh), 2, 0, 3, 1, 4)
        q, k, v = qkv[0], qkv[1], qkv[2]
        scores = torch.einsum("bhqd,bhkd->bhqk", q, k) * (Dh ** -0.5)
        idx = torch.arange(S)
        mask = (idx[:, None] - idx[None, :] >= 0) & (idx[:, None] - idx[None, :] < W)
        scores = torch.where(mask[None, None], scores, -1e9)
        p = _softmax(scores, axis=-1)
        o = torch.einsum("bhqk,bhkd->bhqd", p, v)
        return (lambda x, *d: x.permute(*d))(o, 0, 2, 1, 3).reshape(B, S, D) @ wo

    def _B_ref(x, wqkv, wo):
        B, S, D = x.shape
        H = 32
        Dh = D // H
        qkv = (lambda x, *d: x.permute(*d))((x @ wqkv).reshape(B, S, 3, H, Dh), 2, 0, 3, 1, 4)
        q, k, v = qkv[0], qkv[1], qkv[2]
        scores = torch.einsum("bhqd,bhkd->bhqk", q, k) * (Dh ** -0.5)
        p = _softmax(scores, axis=-1)
        o = torch.einsum("bhqk,bhkd->bhqd", p, v)
        return (lambda x, *d: x.permute(*d))(o, 0, 2, 1, 3).reshape(B, S, D) @ wo

    x = A_x
    g = _sigmoid((x * gate_w).sum(-1, keepdims=True))
    a = _A_ref(x, A_wqkv, A_wo)
    b = _B_ref(x, B_wqkv, B_wo)
    return g * a + (1 - g) * b
```

```cpp
#include <hip/hip_runtime.h>
#include <hip/hip_bf16.h>
#include <stdint.h>

#define LAS __attribute__((address_space(3)))
typedef short bf16x8 __attribute__((ext_vector_type(8)));
typedef short s16x4 __attribute__((ext_vector_type(4)));
typedef float f32x16 __attribute__((ext_vector_type(16)));
typedef float f32x4 __attribute__((ext_vector_type(4)));
typedef float f32x2 __attribute__((ext_vector_type(2)));
typedef unsigned u32x4 __attribute__((ext_vector_type(4)));
typedef unsigned u32x2 __attribute__((ext_vector_type(2)));
typedef __bf16 bf16x2_t __attribute__((ext_vector_type(2)));
typedef LAS const char* lds_cptr;
typedef LAS char* lds_ptr;

#define MFMA32(a, b, c) __builtin_amdgcn_mfma_f32_32x32x16_bf16(a, b, c, 0, 0, 0)
#define MFMA16(a, b, c) __builtin_amdgcn_mfma_f32_16x16x32_bf16(a, b, c, 0, 0, 0)
#define SBAR() __builtin_amdgcn_sched_barrier(0)
#define PIN(x) asm volatile("" : "+v"(x))
#define WAIT_BAR(N) asm volatile("s_waitcnt vmcnt(" #N ")\n\ts_barrier" ::: "memory")
#define WAIT_BAR_L(N) asm volatile("s_waitcnt vmcnt(" #N ") lgkmcnt(0)\n\ts_barrier" ::: "memory")

constexpr int SEQ = 2048, DM = 1024, NH = 32;
constexpr float QSCALE = 0.17677669529663687f * 1.4426950408889634f;

constexpr size_t WS_KV = 0;
constexpr size_t WS_Q = 16u << 20;
constexpr size_t WS_O = 24u << 20;
constexpr size_t WS_G = 32u << 20;
constexpr int LDS_BYTES = 147456;

__device__ __forceinline__ unsigned cvtpk(float lo, float hi) {
    f32x2 v = {lo, hi}; bf16x2_t b = __builtin_convertvector(v, bf16x2_t); return __builtin_bit_cast(unsigned, b);
}
__device__ __forceinline__ void glds16(const void* sbase, unsigned voff, unsigned lds_dst) {
    asm volatile("s_mov_b32 m0, %2\n\ts_nop 0\n\tglobal_load_lds_dwordx4 %0, %1" : : "v"(voff), "s"(sbase), "s"(lds_dst) : "memory");
}
__device__ __forceinline__ s16x4 trread(lds_cptr p) {
    typedef short v4i16_t __attribute__((ext_vector_type(4)));
    return __builtin_bit_cast(s16x4, __builtin_amdgcn_ds_read_tr16_b64_v4i16((LAS v4i16_t*)p));
}
__device__ __forceinline__ bf16x8 trfrag(lds_cptr p, int second_off) {
    const s16x4 lo = trread(p), hi = trread(p + second_off);
    return (bf16x8){lo[0], lo[1], lo[2], lo[3], hi[0], hi[1], hi[2], hi[3]};
}
__device__ __forceinline__ u32x4 pack8(const f32x16& a, int s, float sc) {
    u32x4 r;
    r[0] = cvtpk(a[8 * s + 0] * sc, a[8 * s + 1] * sc); r[1] = cvtpk(a[8 * s + 2] * sc, a[8 * s + 3] * sc);
    r[2] = cvtpk(a[8 * s + 4] * sc, a[8 * s + 5] * sc); r[3] = cvtpk(a[8 * s + 6] * sc, a[8 * s + 7] * sc);
    return r;
}
__device__ __forceinline__ u32x4 pack8n(const f32x16& a, int s) {
    u32x4 r;
    r[0] = cvtpk(a[8 * s + 0], a[8 * s + 1]); r[1] = cvtpk(a[8 * s + 2], a[8 * s + 3]);
    r[2] = cvtpk(a[8 * s + 4], a[8 * s + 5]); r[3] = cvtpk(a[8 * s + 6], a[8 * s + 7]);
    return r;
}
__device__ __forceinline__ float bf2f(unsigned short b) { return __uint_as_float((unsigned)b << 16); }

constexpr int P1_XR = 0, P1_WR = 49152, P1_WSLOT = 24576;

struct P1Frags { bf16x8 x[2]; bf16x8 w[3]; };

__device__ __forceinline__ void phase1(const char* __restrict__ x, const char* __restrict__ wA, const char* __restrict__ wB,
                                       const unsigned short* __restrict__ gate_w, char* __restrict__ ws, char* lds, int hp, int qb) {
    const int tid = threadIdx.x, lane = tid & 63;
    const int wid = __builtin_amdgcn_readfirstlane(tid >> 6);
    const int wm = wid >> 2, wn = wid & 3, br = wn >> 1, hh = wn & 1;
    const int r32 = lane & 31, hi = lane >> 5;
    const unsigned lds0 = (unsigned)(uintptr_t)lds;
    const lds_cptr L = (lds_cptr)lds;

    const char* xs = x + (size_t)qb * 128 * 2048;
    const char* wsrc = (wid < 4 ? wA : wB);
    unsigned vx[2], vw[3];
#pragma unroll
    for (int p = 0; p < 2; ++p) {
        const int row = 16 * wid + 8 * p + (lane >> 3);
        vx[p] = (unsigned)(row * 2048 + (((lane & 7) ^ ((row >> 1) & 7)) * 16));
    }
#pragma unroll
    for (int i = 0; i < 3; ++i) {
        const int id = wid * 3 + i, s = id >> 2, rg = id & 3, part = s % 3;
        const int row = rg * 8 + (lane >> 3);
        vw[i] = (unsigned)(row * 6144 + part * 2048 + hp * 128 + (((lane & 7) ^ (((row >> 1) & 1) << 2)) * 16));
    }
    const unsigned xdst = lds0 + P1_XR + wid * 2048, wdst = lds0 + P1_WR + wid * 3072;
#define P1_STAGE_X(T, slot, p) glds16(xs + (T) * 128, vx[p], xdst + (slot) * 16384 + (p) * 1024)
#define P1_STAGE_W(j, i) glds16(wsrc + (size_t)(j) * (32 * 6144), vw[i], wdst + ((j) & 3) * P1_WSLOT + (i) * 1024)

    int xa[4];
#pragma unroll
    for (int k = 0; k < 4; ++k) xa[k] = P1_XR + (wm * 64 + r32) * 128 + (((2 * k + hi) ^ ((r32 >> 1) & 7)) * 16);
    const int q4 = (lane >> 2) & 3, p4 = lane & 3, nsub = (lane >> 4) & 1;
    const int wa = P1_WR + br * 3 * 4096 + hi * 1024 + q4 * 128 + ((hh ^ (q4 >> 1)) * 64) + nsub * 32 + p4 * 8;

    P1_STAGE_X(0, 0, 0); P1_STAGE_X(0, 0, 1);
    P1_STAGE_W(0, 0); P1_STAGE_W(0, 1); P1_STAGE_W(0, 2);
    P1_STAGE_W(1, 0); P1_STAGE_W(1, 1); P1_STAGE_W(1, 2);
    P1_STAGE_X(1, 1, 0);
    P1_STAGE_W(2, 0); P1_STAGE_W(2, 1); P1_STAGE_W(2, 2);
    P1_STAGE_X(1, 1, 1);

    {
        const int tok = qb * 128 + hp * 8 + wid;
        const u32x4* xr = (const u32x4*)(x + (size_t)tok * 2048) + lane * 2;
        const u32x4* gr = (const u32x4*)gate_w + lane * 2;
        const u32x4 a0 = xr[0], a1 = xr[1], g0 = gr[0], g1 = gr[1];
        float s = 0.f;
#pragma unroll
        for (int i = 0; i < 4; ++i) {
            s += __uint_as_float(a0[i] << 16) * __uint_as_float(g0[i] << 16) + __uint_as_float(a0[i] & 0xffff0000u) * __uint_as_float(g0[i] & 0xffff0000u);
            s += __uint_as_float(a1[i] << 16) * __uint_as_float(g1[i] << 16) + __uint_as_float(a1[i] & 0xffff0000u) * __uint_as_float(g1[i] & 0xffff0000u);
        }
#pragma unroll
        for (int o = 32; o > 0; o >>= 1) s += __shfl_xor(s, o);
        if (lane == 0) ((float*)(ws + WS_G))[tok] = 1.0f / (1.0f + __expf(-s));
    }

    f32x16 acc[2][3];
#pragma unroll
    for (int m = 0; m < 2; ++m)
#pragma unroll
        for (int p = 0; p < 3; ++p) acc[m][p] = f32x16{};

#define P1_LOAD(F, xoff, k4, woff, ks) do { \
        F.x[0] = *(const LAS bf16x8*)(L + (xoff) + xa[k4]); F.x[1] = *(const LAS bf16x8*)(L + (xoff) + xa[k4] + 4096); \
        F.w[0] = trfrag(L + wa + (woff) + (ks) * 2048, 512); F.w[1] = trfrag(L + wa + (woff) + (ks) * 2048 + 4096, 512); \
        F.w[2] = trfrag(L + wa + (woff) + (ks) * 2048 + 8192, 512); } while (0)
#define P1_MMA(F) do { \
        acc[0][0] = MFMA32(F.w[0], F.x[0], acc[0][0]); acc[1][0] = MFMA32(F.w[0], F.x[1], acc[1][0]); \
        acc[0][1] = MFMA32(F.w[1], F.x[0], acc[0][1]); acc[1][1] = MFMA32(F.w[1], F.x[1], acc[1][1]); \
        acc[0][2] = MFMA32(F.x[0], F.w[2], acc[0][2]); acc[1][2] = MFMA32(F.x[1], F.w[2], acc[1][2]); } while (0)

    WAIT_BAR(4);
    P1Frags FA, FB;
    P1_LOAD(FA, 0, 0, 0, 0);
    int xcur = 0, xnxt = 16384, xfree = 32768;
#define P1_TILE(t, GW0, GX0, GW1, GX1, N0, N1, LASTT) do { \
        const int w0 = ((t) & 1) * 2 * P1_WSLOT, w1 = w0 + P1_WSLOT, wn0 = (((t) + 1) & 1) * 2 * P1_WSLOT; \
          \
        P1_LOAD(FB, xcur, 1, w0, 1); \
        if (GW0) { P1_STAGE_W(2 * (t) + 3, 0); P1_STAGE_W(2 * (t) + 3, 1); P1_STAGE_W(2 * (t) + 3, 2); } \
        if (GX0) { glds16(xs + ((t) + 2) * 128, vx[0], xdst + xfree); } \
        SBAR(); P1_MMA(FA); SBAR(); \
        P1_LOAD(FA, xcur, 2, w1, 0); SBAR(); \
        P1_MMA(FB); SBAR(); \
        WAIT_BAR(N0); \
          \
        P1_LOAD(FB, xcur, 3, w1, 1); \
        if (GW1) { P1_STAGE_W(2 * (t) + 4, 0); P1_STAGE_W(2 * (t) + 4, 1); P1_STAGE_W(2 * (t) + 4, 2); } \
        if (GX1) { glds16(xs + ((t) + 2) * 128, vx[1], xdst + xfree + 1024); } \
        SBAR(); P1_MMA(FA); SBAR(); \
        if (!(LASTT)) { P1_LOAD(FA, xnxt, 0, wn0, 0); } SBAR(); \
        P1_MMA(FB); SBAR(); \
        if (!(LASTT)) { WAIT_BAR(N1); } \
        { const int tmp = xcur; xcur = xnxt; xnxt = xfree; xfree = tmp; } \
    } while (0)
    for (int t = 0; t < 14; ++t) P1_TILE(t, true, true, true, true, 4, 4, false);
    P1_TILE(14, true, false, false, false, 3, 0, false);
    P1_TILE(15, false, false, false, false, 0, 0, true);
#undef P1_TILE
#undef P1_LOAD
#undef P1_MMA
#undef P1_STAGE_X
#undef P1_STAGE_W

    const int h = hp * 2 + hh;
#pragma unroll
    for (int m = 0; m < 2; ++m) {
        const int tile = qb * 4 + wm * 2 + m;
        char* qp = ws + WS_Q + ((size_t)((br * 32 + h) * 64 + tile)) * 2048 + lane * 16;
        char* kvp = ws + WS_KV + ((size_t)((br * 32 + h) * 64 + tile)) * 4096 + lane * 16;
        *(u32x4*)(qp) = pack8(acc[m][0], 0, QSCALE); *(u32x4*)(qp + 1024) = pack8(acc[m][0], 1, QSCALE);
        *(u32x4*)(kvp) = pack8n(acc[m][1], 0); *(u32x4*)(kvp + 1024) = pack8n(acc[m][1], 1);
        *(u32x4*)(kvp + 2048) = pack8n(acc[m][2], 0); *(u32x4*)(kvp + 3072) = pack8n(acc[m][2], 1);
    }
}

__device__ __forceinline__ int crow(int r, int hi) { return (r & 3) + 8 * (r >> 2) + 4 * hi; }
__device__ __forceinline__ float halfmax(float m) {
    auto rr = __builtin_amdgcn_permlane32_swap(__float_as_uint(m), __float_as_uint(m), false, false);
    return __builtin_fmaxf(__uint_as_float(rr[0]), __uint_as_float(rr[1]));
}
__device__ __forceinline__ float rowmax16(const f32x16& p) {
    float a = __builtin_fmaxf(p[0], p[1]);
#pragma unroll
    for (int r = 2; r < 16; ++r) a = __builtin_fmaxf(a, p[r]);
    return halfmax(a);
}
template <int MASK> __device__ __forceinline__ void apply_mask(f32x16& s, int r32, int hi) {
    if (MASK == 0) return;
#pragma unroll
    for (int r = 0; r < 16; ++r) {
        const int kl = (r & 3) + 8 * (r >> 2) + 4 * hi;
        const bool keep = (MASK == 1) ? (kl <= r32) : (kl > r32);
        if (!keep) s[r] = -INFINITY;
    }
}
struct AttState { f32x16 o; f32x4 l; f32x16 negm; };

__device__ __forceinline__ void attn_slow(const char* kvh, int t_first, int t_last, int t_diag, int t_far, bf16x8 q0, bf16x8 q1, char* odst, int lane) {
    const int r32 = lane & 31, hi = lane >> 5;
    f32x16 o = f32x16{}; float m = -INFINITY, l = 0.f;
    for (int t = t_last; t >= t_first; --t) {
        const char* p = kvh + (size_t)t * 4096 + lane * 16;
        const bf16x8 k0 = *(const bf16x8*)(p), k1 = *(const bf16x8*)(p + 1024), v0 = *(const bf16x8*)(p + 2048), v1 = *(const bf16x8*)(p + 3072);
        f32x16 s = MFMA32(k0, q0, f32x16{}); s = MFMA32(k1, q1, s);
        if (t == t_diag) apply_mask<1>(s, r32, hi);
        if (t == t_far) apply_mask<2>(s, r32, hi);
        const float mn = __builtin_fmaxf(m, rowmax16(s));
        const float ms = (mn == -INFINITY) ? 0.f : mn;
        const float alpha = __builtin_amdgcn_exp2f(m - ms);
        float rs = 0.f;
#pragma unroll
        for (int r = 0; r < 16; ++r) { s[r] = __builtin_amdgcn_exp2f(s[r] - ms); o[r] *= alpha; }
        const u32x4 p0 = pack8n(s, 0), p1 = pack8n(s, 1);
#pragma unroll
        for (int i = 0; i < 4; ++i) rs += __uint_as_float(p0[i] << 16) + __uint_as_float(p0[i] & 0xffff0000u) + __uint_as_float(p1[i] << 16) + __uint_as_float(p1[i] & 0xffff0000u);
        l = l * alpha + rs; m = mn;
        o = MFMA32(v0, __builtin_bit_cast(bf16x8, p0), o); o = MFMA32(v1, __builtin_bit_cast(bf16x8, p1), o);
    }
    auto rr = __builtin_amdgcn_permlane32_swap(__float_as_uint(l), __float_as_uint(l), false, false);
    l = __uint_as_float(rr[0]) + __uint_as_float(rr[1]);
    const float inv = 1.0f / l;
    *(u32x4*)(odst) = pack8(o, 0, inv); *(u32x4*)(odst + 1024) = pack8(o, 1, inv);
}

template <int MASK, bool FIRST>
__device__ __forceinline__ void attn_tile(lds_cptr kp, const bf16x8& q0, const bf16x8& q1, const bf16x8& sel, AttState& st, int r32, int hi) {
    const bf16x8 k0 = *(const LAS bf16x8*)(kp), k1 = *(const LAS bf16x8*)(kp + 1024);
    const bf16x8 v0 = *(const LAS bf16x8*)(kp + 2048), v1 = *(const LAS bf16x8*)(kp + 3072);
    f32x16 s;
    if (FIRST) { s = MFMA32(k0, q0, f32x16{}); } else { s = MFMA32(k0, q0, st.negm); }
    s = MFMA32(k1, q1, s);
    apply_mask<MASK>(s, r32, hi);
    if (FIRST) {
        const float m = rowmax16(s);
#pragma unroll
        for (int r = 0; r < 16; ++r) { s[r] -= m; st.negm[r] = -m; }
    }
#pragma unroll
    for (int r = 0; r < 16; ++r) s[r] = __builtin_amdgcn_exp2f(s[r]);
    const bf16x8 p0 = __builtin_bit_cast(bf16x8, pack8n(s, 0)), p1 = __builtin_bit_cast(bf16x8, pack8n(s, 1));
    st.o = MFMA32(v0, p0, st.o); st.o = MFMA32(v1, p1, st.o);
    st.l = MFMA16(sel, p0, st.l); st.l = MFMA16(sel, p1, st.l);
}

__device__ __forceinline__ bool finite_state(const AttState& st) {
    float a = __builtin_fabsf(st.l[0]);
#pragma unroll
    for (int r = 0; r < 16; ++r) a = __builtin_fmaxf(a, __builtin_fabsf(st.o[r]));
    bool ok = (st.l[0] < 1.0e30f) && (a < 3.0e38f);
#pragma unroll
    for (int r = 0; r < 16; ++r) ok = ok && (st.o[r] == st.o[r]);
    return ok;
}

__device__ __forceinline__ void phase2(char* __restrict__ ws, char* lds, int hp, int qb) {
    const int tid = threadIdx.x, lane = tid & 63;
    const int wid = __builtin_amdgcn_readfirstlane(tid >> 6);
    const int hh = wid >> 2, tt = wid & 3, h = hp * 2 + hh;
    const int r32 = lane & 31, hi = lane >> 5;
    const unsigned lds0 = (unsigned)(uintptr_t)lds;
    const lds_cptr L = (lds_cptr)lds;
    const int qtile = qb * 4 + tt;

    const int nA = qb >= 2 ? 3 : qb + 1, nS = nA + 16;
    const char* kvw = ws + WS_KV + (size_t)(hp * 2 + (wid >> 2)) * (64 * 4096) + (wid & 3) * 4096;
    const unsigned voff = lane * 16;
    auto stage = [&](int si) {
        const int br = si < nA ? 0 : 1, blk = si < nA ? qb - si : si - nA;
        const char* src = kvw + (size_t)br * (32 * 64 * 4096) + (size_t)blk * 16384;
        const unsigned dst = lds0 + (si & 3) * 32768 + wid * 4096;
        glds16(src, voff, dst); glds16(src + 1024, voff, dst + 1024); glds16(src + 2048, voff, dst + 2048); glds16(src + 3072, voff, dst + 3072);
    };
    stage(0); stage(1); stage(2);
    bf16x8 qa0, qa1, qb0, qb1;
    {
        const char* qp = ws + WS_Q + ((size_t)(h * 64 + qtile)) * 2048 + lane * 16;
        qa0 = *(const bf16x8*)(qp); qa1 = *(const bf16x8*)(qp + 1024);
        qb0 = *(const bf16x8*)(qp + (size_t)32 * 64 * 2048); qb1 = *(const bf16x8*)(qp + (size_t)32 * 64 * 2048 + 1024);
    }
    bf16x8 sel;
    { const short one = (((lane >> 4) & 1) == ((lane >> 2) & 1)) ? (short)0x3F80 : (short)0;
#pragma unroll
      for (int i = 0; i < 8; ++i) sel[i] = one; }
    AttState sa, sb;
    sa.o = f32x16{}; sa.l = f32x4{}; sa.negm = f32x16{}; sb.o = f32x16{}; sb.l = f32x4{}; sb.negm = f32x16{};
    const lds_cptr kbase = L + hh * 16384 + lane * 16;

    for (int si = 0; si < nS; ++si) {
        if (si + 2 < nS) { WAIT_BAR_L(8); } else if (si + 1 < nS) { WAIT_BAR_L(4); } else { WAIT_BAR_L(0); }
        if (si + 3 < nS) stage(si + 3);
        const lds_cptr kp = kbase + (si & 3) * 32768;
        if (si < nA) {
            if (si == 0) {
                attn_tile<1, true>(kp + tt * 4096, qa0, qa1, sel, sa, r32, hi);
                for (int t = tt - 1; t >= 0; --t) attn_tile<0, false>(kp + t * 4096, qa0, qa1, sel, sa, r32, hi);
            } else if (si == 1) {
                for (int t = 3; t >= 0; --t) attn_tile<0, false>(kp + t * 4096, qa0, qa1, sel, sa, r32, hi);
            } else {
                for (int t = 3; t > tt; --t) attn_tile<0, false>(kp + t * 4096, qa0, qa1, sel, sa, r32, hi);
                attn_tile<2, false>(kp + tt * 4096, qa0, qa1, sel, sa, r32, hi);
            }
        } else if (si == nA) {
            attn_tile<0, true>(kp, qb0, qb1, sel, sb, r32, hi);
#pragma unroll
            for (int t = 1; t < 4; ++t) attn_tile<0, false>(kp + t * 4096, qb0, qb1, sel, sb, r32, hi);
        } else {
#pragma unroll
            for (int t = 0; t < 4; ++t) attn_tile<0, false>(kp + t * 4096, qb0, qb1, sel, sb, r32, hi);
        }
    }
    char* oa = ws + WS_O + ((size_t)(qtile * 64 + h)) * 2048 + lane * 16;
    char* ob = oa + 32 * 2048;
    if (__all(finite_state(sa))) {
        const float inv = 1.0f / sa.l[0];
        *(u32x4*)(oa) = pack8(sa.o, 0, inv); *(u32x4*)(oa + 1024) = pack8(sa.o, 1, inv);
    } else {
        attn_slow(ws + WS_KV + (size_t)h * (64 * 4096), qtile >= 8 ? qtile - 8 : 0, qtile, qtile, qtile - 8, qa0, qa1, oa, lane);
    }
    if (__all(finite_state(sb))) {
        const float inv = 1.0f / sb.l[0];
        *(u32x4*)(ob) = pack8(sb.o, 0, inv); *(u32x4*)(ob + 1024) = pack8(sb.o, 1, inv);
    } else {
        attn_slow(ws + WS_KV + (size_t)(32 + h) * (64 * 4096), 0, 63, -1, -1, qb0, qb1, ob, lane);
    }
}

__device__ __forceinline__ void phase3(const char* __restrict__ woA, const char* __restrict__ woB, const char* __restrict__ ws,
                                       unsigned short* __restrict__ out, char* lds, int rb, int cb) {
    const int tid = threadIdx.x, lane = tid & 63;
    const int wid = __builtin_amdgcn_readfirstlane(tid >> 6);
    const int wt = wid & 3, br = wid >> 2;
    const int r32 = lane & 31, hi = lane >> 5;
    const unsigned lds0 = (unsigned)(uintptr_t)lds;
    const lds_cptr L = (lds_cptr)lds;
    const char* wsrc = (br ? woB : woA) + (size_t)(wt * 32) * 2048 + cb * 128;
    unsigned vw;
    { const int row = lane >> 3; vw = (unsigned)(row * 2048 + (((lane & 7) ^ (((row >> 1) & 1) << 2)) * 16)); }
    auto stage = [&](int c) {
        const char* src = wsrc + (size_t)c * (128 * 2048);
        const unsigned dst = lds0 + (c & 3) * 32768 + wid * 4096;
        glds16(src, vw, dst); glds16(src + 8 * 2048, vw, dst + 1024); glds16(src + 16 * 2048, vw, dst + 2048); glds16(src + 24 * 2048, vw, dst + 3072);
    };
    stage(0); stage(1); stage(2);
    const int q4 = (lane >> 2) & 3, p4 = lane & 3, nsub = (lane >> 4) & 1;
    const int wa0 = br * 16384 + (4 * hi + q4) * 128 + ((0 ^ (q4 >> 1)) * 64) + nsub * 32 + p4 * 8;
    const int wa1 = br * 16384 + (4 * hi + q4) * 128 + ((1 ^ (q4 >> 1)) * 64) + nsub * 32 + p4 * 8;
    const char* osrc = ws + WS_O + ((size_t)((rb * 4 + wt) * 64 + br * 32)) * 2048 + lane * 16;
    f32x16 acc0 = f32x16{}, acc1 = f32x16{};
    for (int c = 0; c < 8; ++c) {
        bf16x8 of[8];
#pragma unroll
        for (int i = 0; i < 8; ++i) of[i] = *(const bf16x8*)(osrc + (size_t)c * 8192 + i * 1024);
        if (c + 2 < 8) { WAIT_BAR_L(16); } else if (c + 1 < 8) { WAIT_BAR_L(12); } else { WAIT_BAR_L(8); }
        if (c + 3 < 8) stage(c + 3);
        const lds_cptr wp = L + (c & 3) * 32768;
#pragma unroll
        for (int i = 0; i < 8; ++i) {
            const bf16x8 w0 = trfrag(wp + wa0 + i * 2048, 1024), w1 = trfrag(wp + wa1 + i * 2048, 1024);
            acc0 = MFMA32(w0, of[i], acc0); acc1 = MFMA32(w1, of[i], acc1);
        }
    }
    const float g = ((const float*)(ws + WS_G))[rb * 128 + wt * 32 + r32];
    const float sc = br ? (1.0f - g) : g;
    WAIT_BAR_L(0);
    LAS f32x4* ex = (LAS f32x4*)((lds_ptr)lds + wid * 4096) + lane;
    {
        const f32x16& give = br ? acc0 : acc1;
#pragma unroll
        for (int i = 0; i < 4; ++i) ex[i * 64] = (f32x4){give[4 * i] * sc, give[4 * i + 1] * sc, give[4 * i + 2] * sc, give[4 * i + 3] * sc};
    }
    WAIT_BAR_L(0);
    {
        const LAS f32x4* ey = (const LAS f32x4*)((lds_ptr)lds + (wid ^ 4) * 4096) + lane;
        const f32x16& keep = br ? acc1 : acc0;
        unsigned short* op = out + (size_t)(rb * 128 + wt * 32 + r32) * DM + cb * 64 + br * 32 + 4 * hi;
#pragma unroll
        for (int i = 0; i < 4; ++i) {
            const f32x4 y = ey[i * 64];
            u32x2 w;
            w[0] = cvtpk(keep[4 * i] * sc + y[0], keep[4 * i + 1] * sc + y[1]);
            w[1] = cvtpk(keep[4 * i + 2] * sc + y[2], keep[4 * i + 3] * sc + y[3]);
            *(u32x2*)(op + 8 * i) = w;
        }
    }
}

__global__ __launch_bounds__(512, 2) void k_phase1(const char* x, const char* wA, const char* wB, const unsigned short* gw, char* ws) {
    extern __shared__ __attribute__((aligned(16))) char lds[];
    const int b = blockIdx.x, xcd = b & 7, j = b >> 3;
    phase1(x, wA, wB, gw, ws, lds, xcd * 2 + (j & 1), j >> 1);
}
__global__ __launch_bounds__(512, 2) void k_phase2(char* ws) {
    extern __shared__ __attribute__((aligned(16))) char lds[];
    const int b = blockIdx.x, xcd = b & 7, j = b >> 3;
    phase2(ws, lds, xcd * 2 + (j & 1), j >> 1);
}
__global__ __launch_bounds__(512, 2) void k_phase3(const char* woA, const char* woB, const char* ws, unsigned short* out) {
    extern __shared__ __attribute__((aligned(16))) char lds[];
    const int b = blockIdx.x, xcd = b & 7, j = b >> 3;
    phase3(woA, woB, ws, out, lds, 4 * (xcd >> 1) + (j & 3), 8 * (xcd & 1) + (j >> 2));
}

extern "C" void kernel_launch(void* const* d_in, const int* in_sizes, int n_in, void* d_out, int out_size, void* d_ws, size_t ws_size,
                              hipStream_t stream) {
    const char* x = (const char*)d_in[0];
    const char* wA = (const char*)d_in[1];
    const char* woA = (const char*)d_in[2];
    const char* wB = (const char*)d_in[4];
    const char* woB = (const char*)d_in[5];
    const unsigned short* gw = (const unsigned short*)d_in[6];
    (void)hipFuncSetAttribute((const void*)k_phase1, hipFuncAttributeMaxDynamicSharedMemorySize, LDS_BYTES);
    (void)hipFuncSetAttribute((const void*)k_phase2, hipFuncAttributeMaxDynamicSharedMemorySize, LDS_BYTES);
    (void)hipFuncSetAttribute((const void*)k_phase3, hipFuncAttributeMaxDynamicSharedMemorySize, LDS_BYTES);
    k_phase1<<<256, 512, LDS_BYTES, stream>>>(x, wA, wB, gw, (char*)d_ws);
    k_phase2<<<256, 512, LDS_BYTES, stream>>>((char*)d_ws);
    k_phase3<<<256, 512, LDS_BYTES, stream>>>(woA, woB, (const char*)d_ws, (unsigned short*)d_out);
}
```

```cpp
#include <hip/hip_runtime.h>
#include <hip/hip_bf16.h>
#include <stdint.h>

#define LAS __attribute__((address_space(3)))
typedef short bf16x8 __attribute__((ext_vector_type(8)));
typedef short s16x4 __attribute__((ext_vector_type(4)));
typedef float f32x16 __attribute__((ext_vector_type(16)));
typedef float f32x4 __attribute__((ext_vector_type(4)));
typedef float f32x2 __attribute__((ext_vector_type(2)));
typedef unsigned u32x4 __attribute__((ext_vector_type(4)));
typedef unsigned u32x2 __attribute__((ext_vector_type(2)));
typedef __bf16 bf16x2_t __attribute__((ext_vector_type(2)));
typedef LAS const char* lds_cptr;
typedef LAS char* lds_ptr;
typedef __attribute__((address_space(1))) unsigned long long gu64;
typedef __attribute__((address_space(1))) unsigned gu32;

#define MFMA32(a, b, c) __builtin_amdgcn_mfma_f32_32x32x16_bf16(a, b, c, 0, 0, 0)
#define MFMA16(a, b, c) __builtin_amdgcn_mfma_f32_16x16x32_bf16(a, b, c, 0, 0, 0)
#define SBAR() __builtin_amdgcn_sched_barrier(0)
#define PIN(x) asm volatile("" : "+v"(x))
#define WAIT_BAR(N) asm volatile("s_waitcnt vmcnt(" #N ")\n\ts_barrier" ::: "memory")
#define WAIT_BAR_L(N) asm volatile("s_waitcnt vmcnt(" #N ") lgkmcnt(0)\n\ts_barrier" ::: "memory")
#define LBAR() asm volatile("s_waitcnt lgkmcnt(0)\n\ts_barrier" ::: "memory")
#define BAR() asm volatile("s_barrier" ::: "memory")
#define RLX_AGENT __ATOMIC_RELAXED, __HIP_MEMORY_SCOPE_AGENT

constexpr int DM = 1024;
constexpr float QSCALE = 0.17677669529663687f * 1.4426950408889634f;

constexpr unsigned WS_KV = 0;
constexpr unsigned WS_O = 16u << 20;
constexpr unsigned WS_G = 24u << 20;
constexpr unsigned WS_EPOCH = WS_G + 65536;
constexpr unsigned WS_FLAG1 = WS_EPOCH + 4096;
constexpr unsigned WS_FLAG2 = WS_FLAG1 + 32768;
constexpr unsigned WS_USED = 32u << 20;
constexpr int LDS_BYTES = 163840;

#define STAMP(i) do {} while (0)

__device__ __forceinline__ unsigned cvtpk(float lo, float hi) {
    f32x2 v = {lo, hi}; bf16x2_t b = __builtin_convertvector(v, bf16x2_t); return __builtin_bit_cast(unsigned, b);
}
__device__ __forceinline__ void glds16(const void* sbase, unsigned voff, unsigned lds_dst) {
    asm volatile("s_mov_b32 m0, %2\n\ts_nop 0\n\tglobal_load_lds_dwordx4 %0, %1" : : "v"(voff), "s"(sbase), "s"(lds_dst) : "memory");
}
__device__ __forceinline__ void touch_lines64(const void* vaddr, unsigned lds_dst) {
    asm volatile("s_mov_b32 m0, %1\n\ts_nop 0\n\tglobal_load_lds_dword %0, off" : : "v"(vaddr), "s"(lds_dst) : "memory");
}
__device__ __forceinline__ void st_sc1(__amdgpu_buffer_rsrc_t rs, unsigned off, u32x4 v) { __builtin_amdgcn_raw_buffer_store_b128(v, rs, (int)off, 0, 16); }
__device__ __forceinline__ u32x4 ld_sc1(__amdgpu_buffer_rsrc_t rs, unsigned off) { return __builtin_amdgcn_raw_buffer_load_b128(rs, (int)off, 0, 16); }
__device__ __forceinline__ void wait16(const char* base, unsigned long long tag, int lane) {
    gu64* f = (gu64*)(base + (lane & 15) * 128);
    for (unsigned spins = 0; spins < (1u << 22); ++spins) {
        const unsigned long long v = __hip_atomic_load(f, RLX_AGENT);
        if (__all(v == tag)) break;
        __builtin_amdgcn_s_sleep(1);
    }
}
__device__ __forceinline__ s16x4 trread(lds_cptr p) {
    typedef short v4i16_t __attribute__((ext_vector_type(4)));
    return __builtin_bit_cast(s16x4, __builtin_amdgcn_ds_read_tr16_b64_v4i16((LAS v4i16_t*)p));
}
__device__ __forceinline__ bf16x8 trfrag(lds_cptr p, int second_off) {
    const s16x4 lo = trread(p), hi = trread(p + second_off);
    return (bf16x8){lo[0], lo[1], lo[2], lo[3], hi[0], hi[1], hi[2], hi[3]};
}
__device__ __forceinline__ u32x4 pack8(const f32x16& a, int s, float sc) {
    u32x4 r;
    r[0] = cvtpk(a[8 * s + 0] * sc, a[8 * s + 1] * sc); r[1] = cvtpk(a[8 * s + 2] * sc, a[8 * s + 3] * sc);
    r[2] = cvtpk(a[8 * s + 4] * sc, a[8 * s + 5] * sc); r[3] = cvtpk(a[8 * s + 6] * sc, a[8 * s + 7] * sc);
    return r;
}
__device__ __forceinline__ u32x4 pack8n(const f32x16& a, int s) {
    u32x4 r;
    r[0] = cvtpk(a[8 * s + 0], a[8 * s + 1]); r[1] = cvtpk(a[8 * s + 2], a[8 * s + 3]);
    r[2] = cvtpk(a[8 * s + 4], a[8 * s + 5]); r[3] = cvtpk(a[8 * s + 6], a[8 * s + 7]);
    return r;
}
__device__ __forceinline__ u32x4 pack44(const f32x4& a, const f32x4& b, float sc) {
    return (u32x4){cvtpk(a[0] * sc, a[1] * sc), cvtpk(a[2] * sc, a[3] * sc), cvtpk(b[0] * sc, b[1] * sc), cvtpk(b[2] * sc, b[3] * sc)};
}
__device__ __forceinline__ u32x4 pack44n(const f32x4& a, const f32x4& b) {
    return (u32x4){cvtpk(a[0], a[1]), cvtpk(a[2], a[3]), cvtpk(b[0], b[1]), cvtpk(b[2], b[3])};
}
#define EXP2(v) __builtin_amdgcn_exp2f(v)
#define LDSV(p) (*(const LAS bf16x8*)(p))
#define LDSW(p) (*(LAS u32x4*)(p))
#define BC8(v) __builtin_bit_cast(bf16x8, v)

__device__ __forceinline__ float halfmax(float m) {
    auto rr = __builtin_amdgcn_permlane32_swap(__float_as_uint(m), __float_as_uint(m), false, false);
    return __builtin_fmaxf(__uint_as_float(rr[0]), __uint_as_float(rr[1]));
}
__device__ __forceinline__ float rowmax16(const f32x16& p) {
    float a = __builtin_fmaxf(p[0], p[1]);
#pragma unroll
    for (int r = 2; r < 16; ++r) a = __builtin_fmaxf(a, p[r]);
    return halfmax(a);
}
template <int MASK> __device__ __forceinline__ void apply_mask(f32x16& s, int r32, int hi) {
#pragma unroll
    for (int r = 0; r < 16; ++r) {
        const int kl = (r & 3) + 8 * (r >> 2) + 4 * hi;
        const bool keep = (MASK == 1) ? (kl <= r32) : (kl > r32);
        if (!keep) s[r] = -INFINITY;
    }
}
__device__ __forceinline__ void attn_slow(__amdgpu_buffer_rsrc_t rs, unsigned kvh, int t_first, int t_last, int t_diag, int t_far, bf16x8 q0, bf16x8 q1, unsigned odst, int lane) {
    const int r32 = lane & 31, hi = lane >> 5;
    f32x16 o = f32x16{}; float m = -INFINITY, l = 0.f;
    for (int t = t_last; t >= t_first; --t) {
        const unsigned p = kvh + (unsigned)t * 4096 + lane * 16;
        const bf16x8 k0 = BC8(ld_sc1(rs, p)), k1 = BC8(ld_sc1(rs, p + 1024)), v0 = BC8(ld_sc1(rs, p + 2048)), v1 = BC8(ld_sc1(rs, p + 3072));
        f32x16 s = MFMA32(k0, q0, f32x16{}); s = MFMA32(k1, q1, s);
        if (t == t_diag) apply_mask<1>(s, r32, hi);
        if (t == t_far) apply_mask<2>(s, r32, hi);
        const float mn = __builtin_fmaxf(m, rowmax16(s));
        const float ms = (mn == -INFINITY) ? 0.f : mn;
        const float alpha = EXP2(m - ms);
        float rs_ = 0.f;
#pragma unroll
        for (int r = 0; r < 16; ++r) { s[r] = EXP2(s[r] - ms); o[r] *= alpha; }
        const u32x4 p0 = pack8n(s, 0), p1 = pack8n(s, 1);
#pragma unroll
        for (int i = 0; i < 4; ++i) rs_ += __uint_as_float(p0[i] << 16) + __uint_as_float(p0[i] & 0xffff0000u) + __uint_as_float(p1[i] << 16) + __uint_as_float(p1[i] & 0xffff0000u);
        l = l * alpha + rs_; m = mn;
        o = MFMA32(v0, BC8(p0), o); o = MFMA32(v1, BC8(p1), o);
    }
    auto rr = __builtin_amdgcn_permlane32_swap(__float_as_uint(l), __float_as_uint(l), false, false);
    l = __uint_as_float(rr[0]) + __uint_as_float(rr[1]);
    const float inv = 1.0f / l;
    st_sc1(rs, odst, pack8(o, 0, inv)); st_sc1(rs, odst + 1024, pack8(o, 1, inv));
}
__device__ __forceinline__ bool finite_state(const f32x16& o, float l) {
    float a = 0.f;
#pragma unroll
    for (int r = 0; r < 16; ++r) a = __builtin_fmaxf(a, __builtin_fabsf(o[r]));
    bool ok = (l < 1.0e30f) && (a < 3.0e38f);
#pragma unroll
    for (int r = 0; r < 16; ++r) ok = ok && (o[r] == o[r]);
    return ok;
}
struct Fr2 { bf16x8 a, b; };
#define EX4(S, i) do { S[i] = EXP2(S[i]); S[(i) + 1] = EXP2(S[(i) + 1]); S[(i) + 2] = EXP2(S[(i) + 2]); S[(i) + 3] = EXP2(S[(i) + 3]); PIN(S); } while (0)
#define PK4(W, S, i) do { W[0] = cvtpk(S[i], S[(i) + 1]); W[1] = cvtpk(S[(i) + 2], S[(i) + 3]); W[2] = cvtpk(S[(i) + 4], S[(i) + 5]); W[3] = cvtpk(S[(i) + 6], S[(i) + 7]); PIN(W); } while (0)
#define ATT_STEP(SC, SN, CN, Q0, Q1, O, LS, KC, KN, VC, VN, KP, VP, G0) do { \
        SBAR(); \
        SN = MFMA32(KC.a, Q0, CN); SBAR(); \
        KN.a = LDSV(KP); KN.b = LDSV((KP) + 1024); EX4(SC, 0); SBAR(); \
        O = MFMA32(VC.a, BC8(pw0), O); SBAR(); \
        VN.a = LDSV((VP) + 2048); VN.b = LDSV((VP) + 3072); EX4(SC, 4); SBAR(); \
        SN = MFMA32(KC.b, Q1, SN); SBAR(); \
        EX4(SC, 8); SBAR(); \
        LS = MFMA16(sel, BC8(pw0), LS); SBAR(); \
        PK4(pw0, SC, 0); SBAR(); \
        O = MFMA32(VC.b, BC8(pw1), O); SBAR(); \
        EX4(SC, 12); SBAR(); \
        LS = MFMA16(sel, BC8(pw1), LS); SBAR(); \
        G0; PK4(pw1, SC, 8); SBAR(); \
    } while (0)
#define ATT_DRAIN(O, LS, VC) do { \
        O = MFMA32(VC.a, BC8(pw0), O); O = MFMA32(VC.b, BC8(pw1), O); \
        LS = MFMA16(sel, BC8(pw0), LS); LS = MFMA16(sel, BC8(pw1), LS); \
    } while (0)
#define ZERO_P() do { pw0 = u32x4{}; pw1 = u32x4{}; } while (0)

constexpr int P1_XR = 0, P1_WR = 49152, P1_WSLOT = 24576, P1_DUMMY = 147456;
constexpr int P2_QX = 0;
constexpr int P2_BS0 = 98304, P2_BS1 = 131072, P2_BS2 = 0, P2_DUMMY = 65536;
struct P1Frags { bf16x8 x[4]; bf16x8 w[6]; };

__global__ __launch_bounds__(512, 2) void fused_kernel(const char* __restrict__ x, const char* __restrict__ wA, const char* __restrict__ woA,
                                                       const char* __restrict__ wB, const char* __restrict__ woB,
                                                       const unsigned short* __restrict__ gate_w, char* __restrict__ ws, unsigned short* __restrict__ out) {
    extern __shared__ __attribute__((aligned(16))) char lds[];
    const int tid = threadIdx.x, lane = tid & 63;
    const int wid = __builtin_amdgcn_readfirstlane(tid >> 6);
    const int xcd = blockIdx.x & 7, jj = blockIdx.x >> 3;
    const int hp = xcd * 2 + (jj & 1), qb = jj >> 1;
    const unsigned lds0 = (unsigned)(uintptr_t)lds;
    const lds_cptr L = (lds_cptr)lds;
    const lds_ptr LW = (lds_ptr)lds;
    const __amdgpu_buffer_rsrc_t rs = __builtin_amdgcn_make_buffer_rsrc((void*)ws, 0, (int)WS_USED, 0x00020000);
    const int i16 = lane & 15, g4 = lane >> 4, q4 = (lane >> 2) & 3, p4 = lane & 3;
    const int r32 = lane & 31, hi = lane >> 5;
    STAMP(0);
    unsigned long long tag = 0;
    if (wid == 0) tag = __hip_atomic_load((gu64*)(ws + WS_EPOCH), RLX_AGENT) + 1;

    {
        const int wm = wid >> 2, wn = wid & 3, br = wn >> 1, hh = wn & 1;
        const char* xs = x + (size_t)qb * 128 * 2048;
        const char* wsrc = (wid < 4 ? wA : wB);
        unsigned vx[2], vw[3];
#pragma unroll
        for (int p = 0; p < 2; ++p) {
            const int row = 16 * wid + 8 * p + (lane >> 3);
            const int sw = ((row >> 1) & 3) | (((row >> 4) & 1) << 2);
            vx[p] = (unsigned)(row * 2048 + (((lane & 7) ^ sw) * 16));
        }
#pragma unroll
        for (int i = 0; i < 3; ++i) {
            const int id = wid * 3 + i, s = id >> 2, rg = id & 3, part = s % 3;
            const int row = rg * 8 + (lane >> 3);
            const int f = (((row >> 1) & 1) << 1) | (((row >> 3) & 1) << 2);
            vw[i] = (unsigned)(row * 6144 + part * 2048 + hp * 128 + (((lane & 7) ^ f) * 16));
        }
        const unsigned xdst = lds0 + P1_XR + wid * 2048, wdst = lds0 + P1_WR + wid * 3072;
#define P1_STAGE_X(T, slotoff, p) glds16(xs + (T) * 128, vx[p], xdst + (slotoff) + (p) * 1024)
#define P1_STAGE_W(j, i) glds16(wsrc + (size_t)(j) * (32 * 6144), vw[i], wdst + ((j) & 3) * P1_WSLOT + (i) * 1024)
        const char* pfb; unsigned pfstep;
        { const int id = (jj * 8 + wid) * 11 + (lane < 11 ? lane : 0);
          if (id < 2048) { pfb = x + (size_t)id * 2048; pfstep = 128; }
          else { const int iw = id - 2048, bq = iw / 384, idx = iw % 384;
                 pfb = (bq ? wB : wA) + (size_t)((idx / 6) * 6144 + ((idx % 6) >> 1) * 2048 + xcd * 256 + (idx & 1) * 128); pfstep = 64 * 6144; } }
#define P1_PREF(T) touch_lines64(pfb + (size_t)(T) * pfstep, lds0 + P1_DUMMY)

        P1_STAGE_X(0, 0, 0); P1_STAGE_X(0, 0, 1);
        P1_STAGE_W(0, 0); P1_STAGE_W(0, 1); P1_STAGE_W(0, 2);
        P1_STAGE_W(1, 0); P1_STAGE_W(1, 1); P1_STAGE_W(1, 2);
        P1_STAGE_X(1, 16384, 0);
        P1_STAGE_W(2, 0); P1_STAGE_W(2, 1); P1_STAGE_W(2, 2);
        P1_STAGE_X(1, 16384, 1);
        P1_PREF(2); P1_PREF(3);
        int xa[2];
        { const int row = wm * 64 + 16 * (i16 >> 3) + (i16 & 7); const int sw = ((row >> 1) & 3) | (((row >> 4) & 1) << 2);
          xa[0] = P1_XR + row * 128 + ((g4 ^ sw) * 16); xa[1] = P1_XR + row * 128 + (((4 + g4) ^ sw) * 16); }
        int wa[2];
        { const int row = 8 * g4 + q4; const int f = (((row >> 1) & 1) << 1) | (((row >> 3) & 1) << 2);
#pragma unroll
          for (int np = 0; np < 2; ++np) wa[np] = P1_WR + br * 3 * 4096 + row * 128 + (((hh * 4 + np * 2 + (p4 >> 1)) ^ f) * 16) + (p4 & 1) * 8; }

        f32x4 acc[4][6];
#pragma unroll
        for (int m = 0; m < 4; ++m)
#pragma unroll
            for (int n = 0; n < 6; ++n) acc[m][n] = f32x4{};

#define P1_LDX(F, m, xoff, kh) F.x[m] = LDSV(L + (xoff) + xa[kh] + ((m) >> 1) * 4096 + ((m) & 1) * 1024)
#define P1_LDW(F, n, woff) F.w[n] = trfrag(L + wa[(n) & 1] + (woff) + ((n) >> 1) * 4096, 512)
#define P1_M(F, m, n) acc[m][n] = ((n) < 4) ? MFMA16(F.w[n], F.x[m], acc[m][n]) : MFMA16(F.x[m], F.w[n], acc[m][n])
#define P1_STEP(FC, FN, LD, xoffn, khn, woffn, G0, G1, G2, G3) do { \
            if (LD) { P1_LDX(FN, 0, xoffn, khn); P1_LDW(FN, 0, woffn); } P1_M(FC, 0, 0); P1_M(FC, 1, 0); P1_M(FC, 2, 0); SBAR(); \
            G0; if (LD) { P1_LDW(FN, 1, woffn); } P1_M(FC, 3, 0); P1_M(FC, 0, 1); P1_M(FC, 1, 1); SBAR(); \
            if (LD) { P1_LDX(FN, 1, xoffn, khn); P1_LDW(FN, 2, woffn); } P1_M(FC, 2, 1); P1_M(FC, 3, 1); P1_M(FC, 0, 2); SBAR(); \
            G1; if (LD) { P1_LDW(FN, 3, woffn); } P1_M(FC, 1, 2); P1_M(FC, 2, 2); P1_M(FC, 3, 2); SBAR(); \
            if (LD) { P1_LDX(FN, 2, xoffn, khn); P1_LDW(FN, 4, woffn); } P1_M(FC, 0, 3); P1_M(FC, 1, 3); P1_M(FC, 2, 3); SBAR(); \
            G2; if (LD) { P1_LDW(FN, 5, woffn); } P1_M(FC, 3, 3); P1_M(FC, 0, 4); P1_M(FC, 1, 4); SBAR(); \
            if (LD) { P1_LDX(FN, 3, xoffn, khn); } P1_M(FC, 2, 4); P1_M(FC, 3, 4); P1_M(FC, 0, 5); SBAR(); \
            G3; P1_M(FC, 1, 5); P1_M(FC, 2, 5); P1_M(FC, 3, 5); SBAR(); \
        } while (0)

        WAIT_BAR(6);
        STAMP(1);
        P1Frags FA, FB;
#pragma unroll
        for (int m = 0; m < 4; ++m) P1_LDX(FA, m, 0, 0);
#pragma unroll
        for (int n = 0; n < 6; ++n) P1_LDW(FA, n, 0);
        int xcur = 0, xnxt = 16384, xfree = 32768;
#define P1_TILE(t, GW0, GX0, GW1, GX1, N0, N1, LASTT, PF) do { \
            const int w1 = ((t) & 1) * 2 * P1_WSLOT + P1_WSLOT, wn0 = (((t) + 1) & 1) * 2 * P1_WSLOT; \
            P1_STEP(FA, FB, true, xcur, 1, w1, \
                    if (GW0) P1_STAGE_W(2 * (t) + 3, 0), if (GW0) P1_STAGE_W(2 * (t) + 3, 1), if (GW0) P1_STAGE_W(2 * (t) + 3, 2), if (GX0) P1_STAGE_X((t) + 2, xfree, 0)); \
            WAIT_BAR(N0); \
            P1_STEP(FB, FA, !(LASTT), xnxt, 0, wn0, \
                    if (GW1) P1_STAGE_W(2 * (t) + 4, 0), if (GW1) P1_STAGE_W(2 * (t) + 4, 1), if (GW1) P1_STAGE_W(2 * (t) + 4, 2), if (GX1) P1_STAGE_X((t) + 2, xfree, 1)); \
            if (PF) P1_PREF((t) + 4); \
            if (!(LASTT)) { WAIT_BAR(N1); } \
            { const int tmp = xcur; xcur = xnxt; xnxt = xfree; xfree = tmp; } \
        } while (0)
        for (int t = 0; t < 12; ++t) P1_TILE(t, true, true, true, true, 4, 5, false, true);
        P1_TILE(12, true, true, true, true, 4, 4, false, false);
        P1_TILE(13, true, true, true, true, 4, 4, false, false);
        P1_TILE(14, true, false, false, false, 3, 0, false, false);
        const int gtok = qb * 128 + hp * 8 + wid;
        u32x4 ga0, ga1, gg0, gg1;
        { const u32x4* xr = (const u32x4*)(x + (size_t)gtok * 2048) + lane * 2; const u32x4* gr = (const u32x4*)gate_w + lane * 2;
          ga0 = xr[0]; ga1 = xr[1]; gg0 = gr[0]; gg1 = gr[1]; }
        P1_TILE(15, false, false, false, false, 0, 0, true, false);
#undef P1_TILE
#undef P1_STEP
#undef P1_M
#undef P1_LDX
#undef P1_LDW
#undef P1_STAGE_X
#undef P1_STAGE_W
        STAMP(2);
        LBAR();
        const int h = hp * 2 + hh;
        const int so = (g4 >> 1) * 1024 + (g4 & 1) * 512;
#pragma unroll
        for (int T = 0; T < 2; ++T) {
            const int tl = wm * 2 + T;
            const unsigned kvg = WS_KV + (unsigned)((br * 32 + h) * 64 + qb * 4 + tl) * 4096;
            const lds_ptr kvl = LW + (br ? P2_BS0 + hh * 16384 + tl * 4096 : hh * 49152 + (8 + tl) * 4096);
            const lds_ptr qxl = LW + P2_QX + ((br * 2 + hh) * 4 + tl) * 2048;
#pragma unroll
            for (int mp = 0; mp < 2; ++mp) {
                const int off = so + (16 * (i16 >> 3) + 8 * mp + (i16 & 7)) * 16;
                const u32x4 kf = pack44n(acc[2 * T + mp][2], acc[2 * T + mp][3]);
                st_sc1(rs, kvg + off, kf); LDSW(kvl + off) = kf;
                LDSW(qxl + off) = pack44(acc[2 * T + mp][0], acc[2 * T + mp][1], QSCALE);
            }
#pragma unroll
            for (int np = 0; np < 2; ++np) {
                const int off = 2048 + so + (16 * np + i16) * 16;
                const u32x4 vf = pack44n(acc[2 * T][4 + np], acc[2 * T + 1][4 + np]);
                st_sc1(rs, kvg + off, vf); LDSW(kvl + off) = vf;
            }
        }
        {
            float s = 0.f;
#pragma unroll
            for (int i = 0; i < 4; ++i) {
                s += __uint_as_float(ga0[i] << 16) * __uint_as_float(gg0[i] << 16) + __uint_as_float(ga0[i] & 0xffff0000u) * __uint_as_float(gg0[i] & 0xffff0000u);
                s += __uint_as_float(ga1[i] << 16) * __uint_as_float(gg1[i] << 16) + __uint_as_float(ga1[i] & 0xffff0000u) * __uint_as_float(gg1[i] & 0xffff0000u);
            }
#pragma unroll
            for (int o = 32; o > 0; o >>= 1) s += __shfl_xor(s, o);
            if (lane == 0) __hip_atomic_store((gu32*)(ws + WS_G) + gtok, __float_as_uint(1.0f / (1.0f + __expf(-s))), RLX_AGENT);
        }
        LBAR();
        STAMP(3);
    }

    {
        const int hh = wid >> 2, tt = wid & 3, h = hp * 2 + hh;
        const int qtile = qb * 4 + tt;
        const bf16x8 qa0 = LDSV(L + P2_QX + (hh * 4 + tt) * 2048 + lane * 16), qa1 = LDSV(L + P2_QX + (hh * 4 + tt) * 2048 + 1024 + lane * 16);
        const bf16x8 qb0 = LDSV(L + P2_QX + ((2 + hh) * 4 + tt) * 2048 + lane * 16), qb1 = LDSV(L + P2_QX + ((2 + hh) * 4 + tt) * 2048 + 1024 + lane * 16);
        bf16x8 sel;
        { const short one = (((lane >> 4) & 1) == ((lane >> 2) & 1)) ? (short)0x3F80 : (short)0;
#pragma unroll
          for (int i = 0; i < 8; ++i) sel[i] = one; }
        f32x16 SA, SB, negm, oa = f32x16{}, ob = f32x16{};
        f32x4 la = f32x4{}, lb = f32x4{};
        Fr2 kA, kB, vA, vB; u32x4 pw0, pw1;
        const lds_cptr hb = L + hh * 16384 + lane * 16;
        float mB;
        {
            const lds_cptr cur = hb + P2_BS0;
            kA.a = LDSV(cur); kA.b = LDSV(cur + 1024);
            SA = MFMA32(kA.a, qb0, f32x16{}); SA = MFMA32(kA.b, qb1, SA);
            mB = rowmax16(SA);
#pragma unroll
            for (int r = 0; r < 16; ++r) { SA[r] -= mB; negm[r] = -mB; }
            kA.a = LDSV(cur + 4096); kA.b = LDSV(cur + 4096 + 1024);
            ZERO_P(); vA = kA;
            ATT_STEP(SA, SB, negm, qb0, qb1, ob, lb, kA, kB, vA, vB, cur + 2 * 4096, cur, (void)0);
            ATT_STEP(SB, SA, negm, qb0, qb1, ob, lb, kB, kA, vB, vA, cur + 3 * 4096, cur + 1 * 4096, (void)0);
            ATT_STEP(SA, SB, negm, qb0, qb1, ob, lb, kA, kB, vA, vB, cur, cur + 2 * 4096, (void)0);
            ATT_STEP(SB, SA, negm, qb0, qb1, ob, lb, kB, kA, vB, vA, cur, cur + 3 * 4096, (void)0);
            ATT_DRAIN(ob, lb, vA);
        }
        WAIT_BAR(0);
        if (tid == 0) __hip_atomic_store((gu64*)(ws + WS_FLAG1 + (hp * 16 + qb) * 128), tag, RLX_AGENT);
        STAMP(4);
        if (wid == 0) wait16(ws + WS_FLAG1 + hp * 2048, tag, lane);
        BAR();
        STAMP(5);
        const unsigned kvA = WS_KV + (unsigned)(h * 64 + tt) * 4096 + lane * 16, kvB = kvA + 32u * 64 * 4096;
        u32x4 st[4];
        {
            u32x4 a2[4], a1[4];
            const int b2 = qb >= 2 ? qb - 2 : qb, b1 = qb >= 1 ? qb - 1 : qb;
#pragma unroll
            for (int i = 0; i < 4; ++i) a2[i] = ld_sc1(rs, kvA + b2 * 16384 + i * 1024);
#pragma unroll
            for (int i = 0; i < 4; ++i) a1[i] = ld_sc1(rs, kvA + b1 * 16384 + i * 1024);
#pragma unroll
            for (int i = 0; i < 4; ++i) st[i] = ld_sc1(rs, kvB + ((qb + 1) & 15) * 16384 + i * 1024);
            const lds_ptr ad = LW + hh * 49152 + tt * 4096 + lane * 16;
#pragma unroll
            for (int i = 0; i < 4; ++i) LDSW(ad + i * 1024) = a2[i];
#pragma unroll
            for (int i = 0; i < 4; ++i) LDSW(ad + 4 * 4096 + i * 1024) = a1[i];
#pragma unroll
            for (int i = 0; i < 4; ++i) LDSW(LW + P2_BS1 + wid * 4096 + i * 1024 + lane * 16) = st[i];
#pragma unroll
            for (int i = 0; i < 4; ++i) st[i] = ld_sc1(rs, kvB + ((qb + 2) & 15) * 16384 + i * 1024);
        }
        LBAR();
        STAMP(6);
        {
            const lds_cptr ab = L + hh * 49152 + (8 + tt) * 4096 + lane * 16;
            kA.a = LDSV(ab); kA.b = LDSV(ab + 1024);
            SA = MFMA32(kA.a, qa0, f32x16{}); SA = MFMA32(kA.b, qa1, SA);
            apply_mask<1>(SA, r32, hi);
            const float m = rowmax16(SA);
#pragma unroll
            for (int r = 0; r < 16; ++r) { SA[r] -= m; negm[r] = -m; }
            ZERO_P(); vA = kA;
            const int nfull = qtile < 7 ? qtile : 7;
            const bool far = qtile >= 8;
            int n = nfull + (far ? 1 : 0);
            f32x16 c1 = negm;
            if (far) apply_mask<2>(c1, r32, hi);
            lds_cptr p1 = n == 0 ? ab : (far ? ab - 8 * 4096 : ab - 4096);
            lds_cptr p2 = n <= 1 ? p1 : (far ? ab - 4096 : ab - 2 * 4096);
            kA.a = LDSV(p1); kA.b = LDSV(p1 + 1024);
            ATT_STEP(SA, SB, c1, qa0, qa1, oa, la, kA, kB, vA, vB, p2, ab, (void)0);
            while (n >= 2) {
                { const lds_cptr p3 = n > 2 ? p2 - 4096 : p2;
                  ATT_STEP(SB, SA, negm, qa0, qa1, oa, la, kB, kA, vB, vA, p3, p1, (void)0);
                  const lds_cptr p4 = n > 3 ? p3 - 4096 : p3;
                  ATT_STEP(SA, SB, negm, qa0, qa1, oa, la, kA, kB, vA, vB, p4, p2, (void)0);
                  p1 = p3; p2 = p4; }
                n -= 2;
            }
            if (n == 1) { ATT_STEP(SB, SA, negm, qa0, qa1, oa, la, kB, kA, vB, vA, p1, p1, (void)0); ATT_DRAIN(oa, la, vA); }
            else { ATT_DRAIN(oa, la, vB); }
        }
        LBAR();
        STAMP(7);
#pragma unroll
        for (int i = 0; i < 4; ++i) LDSW(LW + P2_BS2 + wid * 4096 + i * 1024 + lane * 16) = st[i];
        LBAR();
        {
            int so0 = P2_BS1, so1 = P2_BS2, so2 = P2_BS0;
            lds_cptr cur = hb + so0;
#pragma unroll
            for (int r = 0; r < 16; ++r) negm[r] = -mB;
            kA.a = LDSV(cur); kA.b = LDSV(cur + 1024);
            SA = MFMA32(kA.a, qb0, negm); SA = MFMA32(kA.b, qb1, SA);
            kA.a = LDSV(cur + 4096); kA.b = LDSV(cur + 4096 + 1024);
            ZERO_P(); vA = kA;
            { const int id = (jj * 8 + wid) * 64 + lane, row = id >> 3;
              touch_lines64((row < 1024 ? woA : woB) + (size_t)((row & 1023) * 2048 + (xcd & 1) * 1024 + (id & 7) * 128), lds0 + P2_DUMMY); }
#define P2_STAGE(s, LD) do { \
                const lds_cptr nxt = hb + so1; \
                if (LD) { const unsigned sb_ = kvB + ((qb + (s) + 2) & 15) * 16384; _Pragma("unroll") for (int i = 0; i < 4; ++i) st[i] = ld_sc1(rs, sb_ + i * 1024); } \
                ATT_STEP(SA, SB, negm, qb0, qb1, ob, lb, kA, kB, vA, vB, cur + 2 * 4096, cur, (void)0); \
                ATT_STEP(SB, SA, negm, qb0, qb1, ob, lb, kB, kA, vB, vA, cur + 3 * 4096, cur + 1 * 4096, (void)0); \
                ATT_STEP(SA, SB, negm, qb0, qb1, ob, lb, kA, kB, vA, vB, nxt, cur + 2 * 4096, (void)0); \
                ATT_STEP(SB, SA, negm, qb0, qb1, ob, lb, kB, kA, vB, vA, nxt + 4096, cur + 3 * 4096, \
                         if (LD) { _Pragma("unroll") for (int i = 0; i < 4; ++i) LDSW(LW + so2 + wid * 4096 + i * 1024 + lane * 16) = st[i]; }); \
                cur = nxt; { const int t_ = so0; so0 = so1; so1 = so2; so2 = t_; } \
            } while (0)
            for (int s = 1; s < 14; ++s) { P2_STAGE(s, true); LBAR(); }
            P2_STAGE(14, false);
            P2_STAGE(15, false);
            ATT_DRAIN(ob, lb, vA);
#undef P2_STAGE
        }
        STAMP(8);
        const unsigned oap = WS_O + (unsigned)(qtile * 64 + h) * 2048 + lane * 16, obp = oap + 32 * 2048;
        if (__all(finite_state(oa, la[0]))) {
            const float inv = 1.0f / la[0];
            st_sc1(rs, oap, pack8(oa, 0, inv)); st_sc1(rs, oap + 1024, pack8(oa, 1, inv));
        } else {
            attn_slow(rs, WS_KV + (unsigned)h * (64 * 4096), qtile >= 8 ? qtile - 8 : 0, qtile, qtile, qtile - 8, qa0, qa1, oap, lane);
        }
        if (__all(finite_state(ob, lb[0]))) {
            const float inv = 1.0f / lb[0];
            st_sc1(rs, obp, pack8(ob, 0, inv)); st_sc1(rs, obp + 1024, pack8(ob, 1, inv));
        } else {
            attn_slow(rs, WS_KV + (unsigned)(32 + h) * (64 * 4096), 0, 63, -1, -1, qb0, qb1, obp, lane);
        }
    }

    {
        const int rb = 4 * (xcd >> 1) + (jj & 3), cb = 8 * (xcd & 1) + (jj >> 2);
        const int wt = wid & 3, br = wid >> 2;
        const char* wsrc = (br ? woB : woA) + (size_t)(wt * 32) * 2048 + cb * 128;
        const unsigned vw = (unsigned)((lane >> 3) * 2048 + (((lane & 7) ^ (((lane >> 4) & 3) << 1)) * 16));
#define P3_STAGE(c) do { const char* src_ = wsrc + (size_t)(c) * (128 * 2048); const unsigned dst_ = lds0 + ((c) & 3) * 32768 + wid * 4096; \
            glds16(src_, vw, dst_); glds16(src_ + 8 * 2048, vw, dst_ + 1024); glds16(src_ + 16 * 2048, vw, dst_ + 2048); glds16(src_ + 24 * 2048, vw, dst_ + 3072); } while (0)
        LBAR();
        P3_STAGE(0); P3_STAGE(1); P3_STAGE(2); P3_STAGE(3);
        WAIT_BAR(16);
        if (tid == 0) __hip_atomic_store((gu64*)(ws + WS_FLAG2 + (qb * 16 + hp) * 128), tag, RLX_AGENT);
        STAMP(9);
        if (wid == 0) wait16(ws + WS_FLAG2 + rb * 2048, tag, lane);
        WAIT_BAR(0);
        STAMP(10);
        if (blockIdx.x == 0 && tid == 0) __hip_atomic_store((gu64*)(ws + WS_EPOCH), tag, RLX_AGENT);
        int wa[4];
        { const int f = (2 * (g4 & 1) + (q4 >> 1)) << 1; const int row = 16 * (g4 >> 1) + 4 * (g4 & 1) + q4;
#pragma unroll
          for (int n = 0; n < 4; ++n) wa[n] = br * 16384 + row * 128 + (((2 * n + (p4 >> 1)) ^ f) * 16) + (p4 & 1) * 8; }
        const unsigned osrc = WS_O + (unsigned)((rb * 4 + wt) * 64 + br * 32) * 2048 + (g4 >> 1) * 1024 + ((g4 & 1) * 32 + i16) * 16;
        f32x4 acc[2][4];
#pragma unroll
        for (int t = 0; t < 2; ++t)
#pragma unroll
            for (int n = 0; n < 4; ++n) acc[t][n] = f32x4{};
        u32x4 ofA[8], ofB[8];
#define P3_LDO(OF, c) do { _Pragma("unroll") for (int i = 0; i < 8; ++i) OF[i] = ld_sc1(rs, osrc + ((c) * 4 + (i >> 1)) * 2048 + (i & 1) * 256); } while (0)
#define P3_COMP(OF, c) do { const lds_cptr wp_ = L + ((c) & 3) * 32768; \
            _Pragma("unroll") for (int hq = 0; hq < 4; ++hq) { \
                bf16x8 wf[4]; \
                _Pragma("unroll") for (int n = 0; n < 4; ++n) wf[n] = trfrag(wp_ + wa[n] + hq * 4096, 1024); \
                _Pragma("unroll") for (int n = 0; n < 4; ++n) { acc[0][n] = MFMA16(wf[n], BC8(OF[2 * hq]), acc[0][n]); acc[1][n] = MFMA16(wf[n], BC8(OF[2 * hq + 1]), acc[1][n]); } \
            } } while (0)
        P3_LDO(ofA, 0); P3_LDO(ofB, 1);
        const float g = __uint_as_float(__hip_atomic_load((gu32*)(ws + WS_G) + rb * 128 + wt * 32 + i16, RLX_AGENT));
        const float g2 = __uint_as_float(__hip_atomic_load((gu32*)(ws + WS_G) + rb * 128 + wt * 32 + 16 + i16, RLX_AGENT));
        P3_COMP(ofA, 0); LBAR(); P3_STAGE(4); P3_LDO(ofA, 2);
        P3_COMP(ofB, 1); LBAR(); P3_STAGE(5); P3_LDO(ofB, 3);
        P3_COMP(ofA, 2); LBAR(); P3_STAGE(6); P3_LDO(ofA, 4);
        P3_COMP(ofB, 3); LBAR(); P3_STAGE(7); P3_LDO(ofB, 5);
        P3_COMP(ofA, 4); LBAR(); P3_LDO(ofA, 6);
        P3_COMP(ofB, 5); LBAR(); P3_LDO(ofB, 7);
        P3_COMP(ofA, 6); WAIT_BAR_L(0);
        P3_COMP(ofB, 7);
#undef P3_STAGE
#undef P3_LDO
#undef P3_COMP
        STAMP(11);
        const float sc0 = br ? (1.0f - g) : g, sc1 = br ? (1.0f - g2) : g2;
        LBAR();
        LAS f32x4* ex = (LAS f32x4*)(LW + wid * 4096) + lane;
#pragma unroll
        for (int t = 0; t < 2; ++t)
#pragma unroll
            for (int n = 0; n < 2; ++n) { const f32x4 a = br ? acc[t][n] : acc[t][2 + n]; ex[(t * 2 + n) * 64] = a * (t ? sc1 : sc0); }
        LBAR();
        const LAS f32x4* ey = (const LAS f32x4*)(LW + (wid ^ 4) * 4096) + lane;
#pragma unroll
        for (int t = 0; t < 2; ++t)
#pragma unroll
            for (int n = 0; n < 2; ++n) {
                const f32x4 a = br ? acc[t][2 + n] : acc[t][n];
                const f32x4 y = ey[(t * 2 + n) * 64] + a * (t ? sc1 : sc0);
                unsigned short* op = out + (size_t)(rb * 128 + wt * 32 + t * 16 + i16) * DM + cb * 64 + (2 * br + n) * 16 + 4 * g4;
                *(u32x2*)op = (u32x2){cvtpk(y[0], y[1]), cvtpk(y[2], y[3])};
            }
        STAMP(12);
    }
}

extern "C" void kernel_launch(void* const* d_in, const int* in_sizes, int n_in, void* d_out, int out_size, void* d_ws, size_t ws_size,
                              hipStream_t stream) {
    (void)hipFuncSetAttribute((const void*)fused_kernel, hipFuncAttributeMaxDynamicSharedMemorySize, LDS_BYTES);
    fused_kernel<<<256, 512, LDS_BYTES, stream>>>((const char*)d_in[0], (const char*)d_in[1], (const char*)d_in[2], (const char*)d_in[4], (const char*)d_in[5],
                                                  (const unsigned short*)d_in[6], (char*)d_ws, (unsigned short*)d_out);
}
```

```cpp
#include <hip/hip_runtime.h>
#include <hip/hip_bf16.h>
#include <stdint.h>

#define LAS __attribute__((address_space(3)))
typedef short bf16x8 __attribute__((ext_vector_type(8)));
typedef short s16x4 __attribute__((ext_vector_type(4)));
typedef float f32x16 __attribute__((ext_vector_type(16)));
typedef float f32x4 __attribute__((ext_vector_type(4)));
typedef float f32x2 __attribute__((ext_vector_type(2)));
typedef unsigned u32x4 __attribute__((ext_vector_type(4)));
typedef unsigned u32x2 __attribute__((ext_vector_type(2)));
typedef __bf16 bf16x2_t __attribute__((ext_vector_type(2)));
typedef LAS const char* lds_cptr;
typedef LAS char* lds_ptr;
typedef __attribute__((address_space(1))) unsigned long long gu64;
typedef __attribute__((address_space(1))) unsigned gu32;

#define MFMA32(a, b, c) __builtin_amdgcn_mfma_f32_32x32x16_bf16(a, b, c, 0, 0, 0)
#define MFMA16(a, b, c) __builtin_amdgcn_mfma_f32_16x16x32_bf16(a, b, c, 0, 0, 0)
#define SBAR() __builtin_amdgcn_sched_barrier(0)
#define PIN(x) asm volatile("" : "+v"(x))
#define WAIT_BAR(N) asm volatile("s_waitcnt vmcnt(" #N ")\n\ts_barrier" ::: "memory")
#define WAIT_BAR_L(N) asm volatile("s_waitcnt vmcnt(" #N ") lgkmcnt(0)\n\ts_barrier" ::: "memory")
#define LBAR() asm volatile("s_waitcnt lgkmcnt(0)\n\ts_barrier" ::: "memory")
#define BAR() asm volatile("s_barrier" ::: "memory")
#define RLX_AGENT __ATOMIC_RELAXED, __HIP_MEMORY_SCOPE_AGENT

constexpr int DM = 1024;
constexpr float QSCALE = 0.17677669529663687f * 1.4426950408889634f;

constexpr unsigned WS_KV = 0;
constexpr unsigned WS_O = 16u << 20;
constexpr unsigned WS_G = 24u << 20;
constexpr unsigned WS_EPOCH = WS_G + 65536;
constexpr unsigned WS_FLAG1 = WS_EPOCH + 4096;
constexpr unsigned WS_FLAG2 = WS_FLAG1 + 32768;
constexpr unsigned WS_FLAG3 = WS_FLAG2 + 32768;
constexpr unsigned WS_USED = 32u << 20;
constexpr int LDS_BYTES = 163840;

#define STAMP(i) do {} while (0)

__device__ __forceinline__ unsigned cvtpk(float lo, float hi) {
    f32x2 v = {lo, hi}; bf16x2_t b = __builtin_convertvector(v, bf16x2_t); return __builtin_bit_cast(unsigned, b);
}
__device__ __forceinline__ void glds16(const void* sbase, unsigned voff, unsigned lds_dst) {
    asm volatile("s_mov_b32 m0, %2\n\ts_nop 0\n\tglobal_load_lds_dwordx4 %0, %1" : : "v"(voff), "s"(sbase), "s"(lds_dst) : "memory");
}
__device__ __forceinline__ void touch_lines64(const void* vaddr, unsigned lds_dst) {
    asm volatile("s_mov_b32 m0, %1\n\ts_nop 0\n\tglobal_load_lds_dword %0, off" : : "v"(vaddr), "s"(lds_dst) : "memory");
}
__device__ __forceinline__ void st_sc1(__amdgpu_buffer_rsrc_t rs, unsigned off, u32x4 v) { __builtin_amdgcn_raw_buffer_store_b128(v, rs, (int)off, 0, 16); }
__device__ __forceinline__ u32x4 ld_sc1(__amdgpu_buffer_rsrc_t rs, unsigned off) { return __builtin_amdgcn_raw_buffer_load_b128(rs, (int)off, 0, 16); }
__device__ __forceinline__ void wait16(const char* base, unsigned long long tag, int lane) {
    gu64* f = (gu64*)(base + (lane & 15) * 128);
    for (unsigned spins = 0; spins < (1u << 18); ++spins) {
        const unsigned long long v = __hip_atomic_load(f, RLX_AGENT);
        if (__all(v == tag)) break;
        __builtin_amdgcn_s_sleep(1);
    }
}
__device__ __forceinline__ s16x4 trread(lds_cptr p) {
    typedef short v4i16_t __attribute__((ext_vector_type(4)));
    return __builtin_bit_cast(s16x4, __builtin_amdgcn_ds_read_tr16_b64_v4i16((LAS v4i16_t*)p));
}
__device__ __forceinline__ bf16x8 trfrag(lds_cptr p, int second_off) {
    const s16x4 lo = trread(p), hi = trread(p + second_off);
    return (bf16x8){lo[0], lo[1], lo[2], lo[3], hi[0], hi[1], hi[2], hi[3]};
}
__device__ __forceinline__ u32x4 pack8(const f32x16& a, int s, float sc) {
    u32x4 r;
    r[0] = cvtpk(a[8 * s + 0] * sc, a[8 * s + 1] * sc); r[1] = cvtpk(a[8 * s + 2] * sc, a[8 * s + 3] * sc);
    r[2] = cvtpk(a[8 * s + 4] * sc, a[8 * s + 5] * sc); r[3] = cvtpk(a[8 * s + 6] * sc, a[8 * s + 7] * sc);
    return r;
}
__device__ __forceinline__ u32x4 pack8n(const f32x16& a, int s) {
    u32x4 r;
    r[0] = cvtpk(a[8 * s + 0], a[8 * s + 1]); r[1] = cvtpk(a[8 * s + 2], a[8 * s + 3]);
    r[2] = cvtpk(a[8 * s + 4], a[8 * s + 5]); r[3] = cvtpk(a[8 * s + 6], a[8 * s + 7]);
    return r;
}
__device__ __forceinline__ u32x4 pack44(const f32x4& a, const f32x4& b, float sc) {
    return (u32x4){cvtpk(a[0] * sc, a[1] * sc), cvtpk(a[2] * sc, a[3] * sc), cvtpk(b[0] * sc, b[1] * sc), cvtpk(b[2] * sc, b[3] * sc)};
}
__device__ __forceinline__ u32x4 pack44n(const f32x4& a, const f32x4& b) {
    return (u32x4){cvtpk(a[0], a[1]), cvtpk(a[2], a[3]), cvtpk(b[0], b[1]), cvtpk(b[2], b[3])};
}
#define EXP2(v) __builtin_amdgcn_exp2f(v)
#define LDSV(p) (*(const LAS bf16x8*)(p))
#define LDSW(p) (*(LAS u32x4*)(p))
#define BC8(v) __builtin_bit_cast(bf16x8, v)

__device__ __forceinline__ float halfmax(float m) {
    auto rr = __builtin_amdgcn_permlane32_swap(__float_as_uint(m), __float_as_uint(m), false, false);
    return __builtin_fmaxf(__uint_as_float(rr[0]), __uint_as_float(rr[1]));
}
__device__ __forceinline__ float rowmax16(const f32x16& p) {
    float a = __builtin_fmaxf(p[0], p[1]);
#pragma unroll
    for (int r = 2; r < 16; ++r) a = __builtin_fmaxf(a, p[r]);
    return halfmax(a);
}
template <int MASK> __device__ __forceinline__ void apply_mask(f32x16& s, int r32, int hi) {
#pragma unroll
    for (int r = 0; r < 16; ++r) {
        const int kl = (r & 3) + 8 * (r >> 2) + 4 * hi;
        const bool keep = (MASK == 1) ? (kl <= r32) : (kl > r32);
        if (!keep) s[r] = -INFINITY;
    }
}
__device__ __forceinline__ void attn_slow(__amdgpu_buffer_rsrc_t rs, unsigned kvh, int t_first, int t_last, int t_diag, int t_far, bf16x8 q0, bf16x8 q1, unsigned odst, int lane) {
    const int r32 = lane & 31, hi = lane >> 5;
    f32x16 o = f32x16{}; float m = -INFINITY, l = 0.f;
    for (int t = t_last; t >= t_first; --t) {
        const unsigned p = kvh + (unsigned)t * 4096 + lane * 16;
        const bf16x8 k0 = BC8(ld_sc1(rs, p)), k1 = BC8(ld_sc1(rs, p + 1024)), v0 = BC8(ld_sc1(rs, p + 2048)), v1 = BC8(ld_sc1(rs, p + 3072));
        f32x16 s = MFMA32(k0, q0, f32x16{}); s = MFMA32(k1, q1, s);
        if (t == t_diag) apply_mask<1>(s, r32, hi);
        if (t == t_far) apply_mask<2>(s, r32, hi);
        const float mn = __builtin_fmaxf(m, rowmax16(s));
        const float ms = (mn == -INFINITY) ? 0.f : mn;
        const float alpha = EXP2(m - ms);
        float rs_ = 0.f;
#pragma unroll
        for (int r = 0; r < 16; ++r) { s[r] = EXP2(s[r] - ms); o[r] *= alpha; }
        const u32x4 p0 = pack8n(s, 0), p1 = pack8n(s, 1);
#pragma unroll
        for (int i = 0; i < 4; ++i) rs_ += __uint_as_float(p0[i] << 16) + __uint_as_float(p0[i] & 0xffff0000u) + __uint_as_float(p1[i] << 16) + __uint_as_float(p1[i] & 0xffff0000u);
        l = l * alpha + rs_; m = mn;
        o = MFMA32(v0, BC8(p0), o); o = MFMA32(v1, BC8(p1), o);
    }
    auto rr = __builtin_amdgcn_permlane32_swap(__float_as_uint(l), __float_as_uint(l), false, false);
    l = __uint_as_float(rr[0]) + __uint_as_float(rr[1]);
    const float inv = 1.0f / l;
    st_sc1(rs, odst, pack8(o, 0, inv)); st_sc1(rs, odst + 1024, pack8(o, 1, inv));
}
__device__ __forceinline__ bool finite_state(const f32x16& o, float l) {
    float a = 0.f;
#pragma unroll
    for (int r = 0; r < 16; ++r) a = __builtin_fmaxf(a, __builtin_fabsf(o[r]));
    bool ok = (l < 1.0e30f) && (a < 3.0e38f);
#pragma unroll
    for (int r = 0; r < 16; ++r) ok = ok && (o[r] == o[r]);
    return ok;
}
struct Fr2 { bf16x8 a, b; };
#define EX4(S, i) do { S[i] = EXP2(S[i]); S[(i) + 1] = EXP2(S[(i) + 1]); S[(i) + 2] = EXP2(S[(i) + 2]); S[(i) + 3] = EXP2(S[(i) + 3]); PIN(S); } while (0)
#define PK4(W, S, i) do { W[0] = cvtpk(S[i], S[(i) + 1]); W[1] = cvtpk(S[(i) + 2], S[(i) + 3]); W[2] = cvtpk(S[(i) + 4], S[(i) + 5]); W[3] = cvtpk(S[(i) + 6], S[(i) + 7]); PIN(W); } while (0)
#define ATT_STEP(SC, SN, CN, Q0, Q1, O, LS, KC, KN, VC, VN, KP, VP, G0) do { \
        SBAR(); \
        SN = MFMA32(KC.a, Q0, CN); PIN(SN); SBAR(); \
        KN.a = LDSV(KP); KN.b = LDSV((KP) + 1024); EX4(SC, 0); SBAR(); \
        O = MFMA32(VC.a, BC8(pw0), O); PIN(O); SBAR(); \
        VN.a = LDSV((VP) + 2048); VN.b = LDSV((VP) + 3072); EX4(SC, 4); SBAR(); \
        SN = MFMA32(KC.b, Q1, SN); PIN(SN); SBAR(); \
        EX4(SC, 8); SBAR(); \
        LS = MFMA16(sel, BC8(pw0), LS); PIN(LS); SBAR(); \
        PK4(pw0, SC, 0); SBAR(); \
        O = MFMA32(VC.b, BC8(pw1), O); PIN(O); SBAR(); \
        EX4(SC, 12); SBAR(); \
        LS = MFMA16(sel, BC8(pw1), LS); PIN(LS); SBAR(); \
        G0; PK4(pw1, SC, 8); SBAR(); \
    } while (0)
#define ATT_DRAIN(O, LS, VC) do { \
        O = MFMA32(VC.a, BC8(pw0), O); O = MFMA32(VC.b, BC8(pw1), O); \
        LS = MFMA16(sel, BC8(pw0), LS); LS = MFMA16(sel, BC8(pw1), LS); \
    } while (0)
#define ZERO_P() do { pw0 = u32x4{}; pw1 = u32x4{}; } while (0)

constexpr int P1A_SLOT = 49152, P1A_DUMMY = 147456;
constexpr int P1B_SLOT = 32768;
constexpr int P2_B0 = 131072, P2_B1 = 98304, P2_WO0 = 65536;
struct FragsA { bf16x8 x[4]; s16x4 wl[4], wh[4]; };
struct FragsB { bf16x8 x[2]; s16x4 wl[4], wh[4]; };
#define WFR(F, n) (bf16x8){F.wl[n][0], F.wl[n][1], F.wl[n][2], F.wl[n][3], F.wh[n][0], F.wh[n][1], F.wh[n][2], F.wh[n][3]}

__global__ __launch_bounds__(512, 2) void fused_kernel(const char* __restrict__ x, const char* __restrict__ wA, const char* __restrict__ woA,
                                                       const char* __restrict__ wB, const char* __restrict__ woB,
                                                       const unsigned short* __restrict__ gate_w, char* __restrict__ ws, unsigned short* __restrict__ out) {
    extern __shared__ __attribute__((aligned(16))) char lds[];
    const int tid = threadIdx.x, lane = tid & 63;
    const int wid = __builtin_amdgcn_readfirstlane(tid >> 6);
    const int xcd = blockIdx.x & 7, jj = blockIdx.x >> 3;
    const int hp = xcd * 2 + (jj & 1), qb = jj >> 1;
    const unsigned lds0 = (unsigned)(uintptr_t)lds;
    const lds_cptr L = (lds_cptr)lds;
    const lds_ptr LW = (lds_ptr)lds;
    const __amdgpu_buffer_rsrc_t rs = __builtin_amdgcn_make_buffer_rsrc((void*)ws, 0, (int)WS_USED, 0x00020000);
    const int i16 = lane & 15, g4 = lane >> 4, q4 = (lane >> 2) & 3, p4 = lane & 3;
    const int r32 = lane & 31, hi = lane >> 5;
    STAMP(0);
    unsigned long long tag;

    const char* xs = x + (size_t)qb * 128 * 2048;
    unsigned vx[2];
#pragma unroll
    for (int p = 0; p < 2; ++p) {
        const int row = 16 * wid + 8 * p + (lane >> 3);
        const int sw = ((row >> 1) & 3) | (((row >> 4) & 1) << 2);
        vx[p] = (unsigned)(row * 2048 + (((lane & 7) ^ sw) * 16));
    }
    const unsigned xdst = lds0 + wid * 2048;
    unsigned vw[2];
#pragma unroll
    for (int e = 0; e < 2; ++e) vw[e] = (unsigned)((lane >> 3) * 6144 + hp * 128 + (((lane & 7) ^ ((((lane >> 4) & 1) << 1) | (e << 2))) * 16));
    const int wf_f = (((q4 >> 1) & 1) << 1) | ((g4 & 1) << 2);

    u32x4 kvs[8]; unsigned kvg0; int kvoK, kvoV;
    const char* wqsrc = (wid < 4 ? wA : wB) + (size_t)((wid & 3) * 16) * 6144;
    const unsigned wqdst = lds0 + 16384 + wid * 2048;
#define P1B_X(T, slot, p) glds16(xs + (15 - (T)) * 128, vx[p], xdst + (slot) + (p) * 1024)
#define P1B_W(T, slot, i) glds16(wqsrc + (size_t)((15 - (T)) * 64 + (i) * 8) * 6144, vw[i], wqdst + (slot) + (i) * 1024)
    {
        const int wm = wid >> 2, wn = wid & 3, br = wn >> 1, hh = wn & 1;
        const int gtok = qb * 128 + hp * 8 + wid;
        u32x4 ga0, ga1, gg0, gg1;
        { const char* xr = x + (size_t)gtok * 2048; const unsigned go = lane * 32;
          asm volatile("global_load_dwordx4 %0, %4, %5\n\tglobal_load_dwordx4 %1, %4, %5 offset:16\n\tglobal_load_dwordx4 %2, %4, %6\n\tglobal_load_dwordx4 %3, %4, %6 offset:16"
                       : "=&v"(ga0), "=&v"(ga1), "=&v"(gg0), "=&v"(gg1) : "v"(go), "s"(xr), "s"(gate_w) : "memory"); }
        const char* wsrc = (wid < 4 ? wA : wB) + (size_t)((wid & 1) * 32) * 6144 + (1 + ((wid >> 1) & 1)) * 2048;
        const unsigned wdst = lds0 + 16384 + wid * 4096;
#define P1A_X(T, slot, p) glds16(xs + (T) * 128, vx[p], xdst + (slot) + (p) * 1024)
#define P1A_W(T, slot, i) glds16(wsrc + (size_t)((T) * 64 + (i) * 8) * 6144, vw[(i) & 1], wdst + (slot) + (i) * 1024)
        const char* pfb; unsigned pfstep;
        { const int gw = blockIdx.x * 8 + wid;
          if (lane == 0) { pfb = x + (size_t)gw * 2048; pfstep = 128; }
          else if (lane < 3) { const int id = gw * 2 + lane - 1, rem = id & 2047;
                 pfb = ((id >> 11) ? wB : wA) + (size_t)((rem >> 5) * 6144 + (1 + ((rem >> 4) & 1)) * 2048 + (rem & 15) * 128); pfstep = 64 * 6144; }
          else { const int rho = gw * 2 + (lane & 1);
                 if (rho < 2048) pfb = ((rho >> 10) ? wB : wA) + (size_t)(rho & 1023) * 6144; else pfb = (((rho - 2048) >> 10) ? woB : woA) + (size_t)(rho & 1023) * 2048;
                 pfstep = 128; } }
#define P1A_PREF(T, F) do { if (lane < 5) touch_lines64(pfb + (size_t)(lane < 3 ? (T) : (F)) * pfstep, lds0 + P1A_DUMMY); } while (0)
        P1A_X(0, 0, 0); P1A_X(0, 0, 1); P1A_W(0, 0, 0); P1A_W(0, 0, 1); P1A_W(0, 0, 2); P1A_W(0, 0, 3);
        P1A_X(1, P1A_SLOT, 0); P1A_W(1, P1A_SLOT, 0); P1A_W(1, P1A_SLOT, 1);
        P1A_X(1, P1A_SLOT, 1); P1A_W(1, P1A_SLOT, 2); P1A_W(1, P1A_SLOT, 3);
        P1A_PREF(2, 12); P1A_PREF(3, 13); P1A_PREF(4, 14); P1A_PREF(5, 15);
        P1A_X(2, 2 * P1A_SLOT, 0); P1A_W(2, 2 * P1A_SLOT, 0); P1A_W(2, 2 * P1A_SLOT, 1);
        asm volatile("s_load_dwordx2 %0, %1, 0x0\n\ts_waitcnt lgkmcnt(0)" : "=s"(tag) : "s"(ws + WS_EPOCH) : "memory");
        tag += 1;
        asm volatile("s_waitcnt vmcnt(19)" : "+v"(ga0), "+v"(ga1), "+v"(gg0), "+v"(gg1) :: "memory");
        {
            float s = 0.f;
#pragma unroll
            for (int i = 0; i < 4; ++i) {
                s += __uint_as_float(ga0[i] << 16) * __uint_as_float(gg0[i] << 16) + __uint_as_float(ga0[i] & 0xffff0000u) * __uint_as_float(gg0[i] & 0xffff0000u);
                s += __uint_as_float(ga1[i] << 16) * __uint_as_float(gg1[i] << 16) + __uint_as_float(ga1[i] & 0xffff0000u) * __uint_as_float(gg1[i] & 0xffff0000u);
            }
#pragma unroll
            for (int o = 32; o > 0; o >>= 1) s += __shfl_xor(s, o);
            if (lane == 0) __hip_atomic_store((gu32*)(ws + WS_G) + gtok, __float_as_uint(1.0f / (1.0f + __expf(-s))), RLX_AGENT);
        }
        int xa[2];
        { const int row = wm * 64 + 16 * (i16 >> 3) + (i16 & 7); const int sw = ((row >> 1) & 3) | (((row >> 4) & 1) << 2);
          xa[0] = row * 128 + ((g4 ^ sw) * 16); xa[1] = row * 128 + (((4 + g4) ^ sw) * 16); }
        int wa[2];
#pragma unroll
        for (int np = 0; np < 2; ++np) wa[np] = 16384 + br * 16384 + (8 * g4 + q4) * 128 + (((hh * 4 + np * 2 + (p4 >> 1)) ^ wf_f) * 16) + (p4 & 1) * 8;
        f32x4 acc[4][4];
#pragma unroll
        for (int m = 0; m < 4; ++m)
#pragma unroll
            for (int n = 0; n < 4; ++n) acc[m][n] = f32x4{};
#define A_LX(F, m, sl, kh) F.x[m] = LDSV(L + (sl) + xa[kh] + ((m) >> 1) * 4096 + ((m) & 1) * 1024)
#define A_LWL(F, n, sl, kh) F.wl[n] = trread(L + (sl) + wa[(n) & 1] + ((n) >> 1) * 8192 + (kh) * 4096)
#define A_LWH(F, n, sl, kh) F.wh[n] = trread(L + (sl) + wa[(n) & 1] + ((n) >> 1) * 8192 + (kh) * 4096 + 512)
#define A_M(F, m, n) do { acc[m][n] = ((n) < 2) ? MFMA16(WFR(F, n), F.x[m], acc[m][n]) : MFMA16(F.x[m], WFR(F, n), acc[m][n]); } while (0)
#define A_HALF(FC, FN, LD, sl, kh, G0, G1, G2) do { \
            A_M(FC, 0, 0); if (LD) A_LX(FN, 0, sl, kh); SBAR(); A_M(FC, 1, 0); if (LD) A_LWL(FN, 0, sl, kh); SBAR(); \
            A_M(FC, 2, 0); if (LD) A_LWH(FN, 0, sl, kh); SBAR(); A_M(FC, 3, 0); G0; SBAR(); \
            A_M(FC, 0, 1); if (LD) A_LX(FN, 1, sl, kh); SBAR(); A_M(FC, 1, 1); if (LD) A_LWL(FN, 1, sl, kh); SBAR(); \
            A_M(FC, 2, 1); if (LD) A_LWH(FN, 1, sl, kh); SBAR(); A_M(FC, 3, 1); G1; SBAR(); \
            A_M(FC, 0, 2); if (LD) A_LX(FN, 2, sl, kh); SBAR(); A_M(FC, 1, 2); if (LD) A_LWL(FN, 2, sl, kh); SBAR(); \
            A_M(FC, 2, 2); if (LD) A_LWH(FN, 2, sl, kh); SBAR(); A_M(FC, 3, 2); G2; SBAR(); \
            A_M(FC, 0, 3); if (LD) A_LX(FN, 3, sl, kh); SBAR(); A_M(FC, 1, 3); if (LD) A_LWL(FN, 3, sl, kh); SBAR(); \
            A_M(FC, 2, 3); if (LD) A_LWH(FN, 3, sl, kh); SBAR(); A_M(FC, 3, 3); SBAR(); \
        } while (0)
        WAIT_BAR(14);
        STAMP(1);
        FragsA FA, FB;
#pragma unroll
        for (int m = 0; m < 4; ++m) { A_LX(FA, m, 0, 0); A_LWL(FA, m, 0, 0); A_LWH(FA, m, 0, 0); }
        int s0 = 0, s1 = P1A_SLOT, s2 = 2 * P1A_SLOT;
#define A_ROT() do { const int tmp = s0; s0 = s1; s1 = s2; s2 = tmp; } while (0)
        for (int t = 0; t < 13; ++t) {
            A_HALF(FA, FB, true, s0, 1, P1A_X(t + 2, s2, 1), P1A_W(t + 2, s2, 2), P1A_W(t + 2, s2, 3));
            P1A_PREF(t + 6 < 16 ? t + 6 : 15, t);
            WAIT_BAR_L(8);
            A_HALF(FB, FA, true, s1, 0, P1A_X(t + 3, s0, 0), P1A_W(t + 3, s0, 0), P1A_W(t + 3, s0, 1));
            A_ROT();
        }
        A_HALF(FA, FB, true, s0, 1, P1A_X(15, s2, 1), P1A_W(15, s2, 2), P1A_W(15, s2, 3));
        WAIT_BAR_L(6);
        A_HALF(FB, FA, true, s1, 0, P1B_X(0, 65536, 0), P1B_W(0, 65536, 0), { P1B_X(0, 65536, 1); P1B_W(0, 65536, 1); });
        A_ROT();
        A_HALF(FA, FB, true, s0, 1, (void)0, (void)0, (void)0);
        WAIT_BAR_L(4);
        A_HALF(FB, FA, true, s1, 0, P1B_X(1, 98304, 0), P1B_W(1, 98304, 0), { P1B_X(1, 98304, 1); P1B_W(1, 98304, 1); });
        A_ROT();
#undef A_ROT
        A_HALF(FA, FB, true, s0, 1, (void)0, (void)0, (void)0);
        A_HALF(FB, FA, false, 0, 0, (void)0, (void)0, (void)0);
#undef A_HALF
#undef A_M
#undef A_LX
#undef A_LWL
#undef A_LWH
#undef P1A_X
#undef P1A_W
        const int h = hp * 2 + hh;
        const int so = (g4 >> 1) * 1024 + (g4 & 1) * 512;
        kvg0 = WS_KV + (unsigned)((br * 32 + h) * 64 + qb * 4 + wm * 2) * 4096;
        kvoK = so + (16 * (i16 >> 3) + (i16 & 7)) * 16; kvoV = 2048 + so + i16 * 16;
#pragma unroll
        for (int T = 0; T < 2; ++T) {
#pragma unroll
            for (int mp = 0; mp < 2; ++mp) kvs[4 * T + mp] = pack44n(acc[2 * T + mp][0], acc[2 * T + mp][1]);
#pragma unroll
            for (int np = 0; np < 2; ++np) kvs[4 * T + 2 + np] = pack44n(acc[2 * T][2 + np], acc[2 * T + 1][2 + np]);
        }
        STAMP(2);
    }
#define KV_ST(i) st_sc1(rs, kvg0 + ((i) >> 2) * 4096 + (((i) & 2) ? kvoV + ((i) & 1) * 256 : kvoK + ((i) & 1) * 128), kvs[i])

    const int hh = wid >> 2, tt = wid & 3, h = hp * 2 + hh;
    const int qtile = qb * 4 + tt;
    const char* kvA = ws + WS_KV + (size_t)(h * 64 + tt) * 4096;
    const char* kvB = kvA + (size_t)32 * 64 * 4096;
    const unsigned voff = lane * 16;
#define COPY4(src, dst) do { glds16((src), voff, (dst)); glds16((src) + 1024, voff, (dst) + 1024); glds16((src) + 2048, voff, (dst) + 2048); glds16((src) + 3072, voff, (dst) + 3072); } while (0)
    bf16x8 qa0, qa1, qb0, qb1;
    {
        WAIT_BAR_L(4);
        P1B_X(2, 0, 0); P1B_W(2, 0, 0); P1B_X(2, 0, 1); P1B_W(2, 0, 1);
        P1B_X(3, 32768, 0); P1B_W(3, 32768, 0);
        int xa[2];
        { const int row = tt * 32 + 16 * (i16 >> 3) + (i16 & 7); const int sw = ((row >> 1) & 3) | (((row >> 4) & 1) << 2);
          xa[0] = row * 128 + ((g4 ^ sw) * 16); xa[1] = row * 128 + (((4 + g4) ^ sw) * 16); }
        int wa[2];
#pragma unroll
        for (int np = 0; np < 2; ++np) wa[np] = 16384 + (8 * g4 + q4) * 128 + (((hh * 4 + np * 2 + (p4 >> 1)) ^ wf_f) * 16) + (p4 & 1) * 8;
        f32x4 acc[2][4];
#pragma unroll
        for (int m = 0; m < 2; ++m)
#pragma unroll
            for (int n = 0; n < 4; ++n) acc[m][n] = f32x4{};
#define B_LX(F, m, sl, kh) F.x[m] = LDSV(L + (sl) + xa[kh] + (m) * 1024)
#define B_LWL(F, n, sl, kh) F.wl[n] = trread(L + (sl) + wa[(n) & 1] + ((n) >> 1) * 8192 + (kh) * 4096)
#define B_LWH(F, n, sl, kh) F.wh[n] = trread(L + (sl) + wa[(n) & 1] + ((n) >> 1) * 8192 + (kh) * 4096 + 512)
#define B_M(F, m, n) do { acc[m][n] = MFMA16(WFR(F, n), F.x[m], acc[m][n]); } while (0)
#define B_HALF(FC, FN, LD, sl, kh, G0, G1) do { \
            B_M(FC, 0, 0); if (LD) { B_LX(FN, 0, sl, kh); B_LWL(FN, 0, sl, kh); } SBAR(); B_M(FC, 1, 0); if (LD) { B_LWH(FN, 0, sl, kh); } SBAR(); \
            B_M(FC, 0, 1); if (LD) { B_LX(FN, 1, sl, kh); B_LWL(FN, 1, sl, kh); } SBAR(); B_M(FC, 1, 1); if (LD) { B_LWH(FN, 1, sl, kh); } G0; SBAR(); \
            B_M(FC, 0, 2); if (LD) { B_LWL(FN, 2, sl, kh); B_LWH(FN, 2, sl, kh); } SBAR(); B_M(FC, 1, 2); G1; SBAR(); \
            B_M(FC, 0, 3); if (LD) { B_LWL(FN, 3, sl, kh); B_LWH(FN, 3, sl, kh); } SBAR(); B_M(FC, 1, 3); SBAR(); \
        } while (0)
        STAMP(3);
        FragsB FA, FB;
#pragma unroll
        for (int m = 0; m < 2; ++m) B_LX(FA, m, 65536, 0);
#pragma unroll
        for (int n = 0; n < 4; ++n) { B_LWL(FA, n, 65536, 0); B_LWH(FA, n, 65536, 0); }
        int s0 = 65536, s1 = 98304, s2 = 0, s3 = 32768;
#define P1B_IT(u, N, PRE, POST) do { \
            PRE; \
            B_HALF(FA, FB, true, s0, 1, if ((u) + 3 < 16) P1B_X((u) + 3, s3, 1), if ((u) + 3 < 16) P1B_W((u) + 3, s3, 1)); \
            if ((u) < 8) { KV_ST((u) & 7); } \
            WAIT_BAR_L(N); \
            POST; \
            B_HALF(FB, FA, true, s1, 0, if ((u) + 4 < 16) P1B_X((u) + 4, s0, 0), if ((u) + 4 < 16) P1B_W((u) + 4, s0, 0)); \
            { const int tmp = s0; s0 = s1; s1 = s2; s2 = s3; s3 = tmp; } \
        } while (0)
        P1B_IT(0, 9, (void)0, (void)0); P1B_IT(1, 10, (void)0, (void)0);
        P1B_IT(2, 11, (void)0, (void)0); P1B_IT(3, 11, (void)0, (void)0); P1B_IT(4, 11, (void)0, (void)0);
        P1B_IT(5, 11, (void)0, (void)0); P1B_IT(6, 11, (void)0, (void)0); P1B_IT(7, 11, (void)0, (void)0);
        P1B_IT(8, 10, (void)0, (void)0); P1B_IT(9, 9, (void)0, (void)0);
        P1B_IT(10, 8, (void)0, if (tid == 0) __hip_atomic_store((gu64*)(ws + WS_FLAG1 + (hp * 16 + qb) * 128), tag, RLX_AGENT));
        P1B_IT(11, 12, COPY4(kvB + qb * 16384, lds0 + P2_B0 + wid * 4096), (void)0);
        P1B_IT(12, 12, (void)0, (void)0);
        P1B_IT(13, 4, (void)0, if (wid == 0) { wait16(ws + WS_FLAG1 + hp * 2048, tag, lane); __builtin_amdgcn_fence(__ATOMIC_ACQUIRE, "agent"); });
        P1B_IT(14, 0, (void)0, (void)0);
#undef P1B_IT
        B_HALF(FA, FB, true, s0, 1, (void)0, (void)0);
        B_HALF(FB, FA, false, 0, 0, (void)0, (void)0);
#undef B_HALF
#undef B_M
#undef B_LX
#undef B_LWL
#undef B_LWH
#undef P1B_X
#undef P1B_W
        STAMP(4);
        LBAR();
        const lds_ptr scr = LW + hh * 49152 + (8 + tt) * 4096;
        const int so = (g4 >> 1) * 1024 + (g4 & 1) * 512 + (16 * (i16 >> 3) + (i16 & 7)) * 16;
#pragma unroll
        for (int b = 0; b < 2; ++b)
#pragma unroll
            for (int mp = 0; mp < 2; ++mp) LDSW(scr + b * 2048 + so + mp * 128) = pack44(acc[mp][2 * b], acc[mp][2 * b + 1], QSCALE);
        asm volatile("s_waitcnt lgkmcnt(0)" ::: "memory");
        qa0 = LDSV(scr + lane * 16); qa1 = LDSV(scr + 1024 + lane * 16); qb0 = LDSV(scr + 2048 + lane * 16); qb1 = LDSV(scr + 3072 + lane * 16);
        asm volatile("s_waitcnt lgkmcnt(0)" : "+v"(qa0), "+v"(qa1), "+v"(qb0), "+v"(qb1) :: "memory");
        COPY4(kvB + ((qb + 1) & 15) * 16384, lds0 + P2_B1 + wid * 4096);
    }

    const int rb = 4 * (xcd >> 1) + (jj & 3), cb = 8 * (xcd & 1) + (jj >> 2);
    const size_t p3off = (size_t)((wid >> 2) * 512 + (wid & 3) * 32) * 2048 + cb * 128;
    const unsigned p3vw = (unsigned)((lane >> 3) * 2048 + (((lane & 7) ^ (((lane >> 4) & 3) << 1)) * 16));
    const unsigned otl = (unsigned)(((jj >> 2) * 4 + (wid & 3)) * 64 + lane);
    const unsigned otoff = WS_O + (unsigned)(rb * 4 + (otl >> 9)) * (64 * 2048) + (otl & 511) * 128;
#define P3_SLOT(c) (((c) % 5) == 0 ? 65536 : ((c) % 5) == 1 ? 0 : ((c) % 5) == 2 ? 32768 : ((c) % 5) == 3 ? 131072 : 98304)
#define P3_STAGE(c) do { const char* src_ = (((c) >> 2) ? woB : woA) + p3off + (size_t)((c) & 3) * (128 * 2048); const unsigned dst_ = lds0 + P3_SLOT(c) + wid * 4096; \
        glds16(src_, p3vw, dst_); glds16(src_ + 8 * 2048, p3vw, dst_ + 1024); glds16(src_ + 16 * 2048, p3vw, dst_ + 2048); glds16(src_ + 24 * 2048, p3vw, dst_ + 3072); } while (0)
    {
        bf16x8 sel;
        { const short one = (((lane >> 4) & 1) == ((lane >> 2) & 1)) ? (short)0x3F80 : (short)0;
#pragma unroll
          for (int i = 0; i < 8; ++i) sel[i] = one; }
        f32x16 SA, SB, negm, oa = f32x16{}, ob = f32x16{};
        f32x4 la = f32x4{}, lb = f32x4{};
        Fr2 kA, kB, vA, vB; u32x4 pw0, pw1;
        const lds_cptr hb = L + hh * 16384 + lane * 16;
        float mB;
        STAMP(5);
        {
            const lds_cptr cur = hb + P2_B0, nxt = hb + P2_B1;
            const char* a0s = kvA + qb * 16384; const char* a1s = kvA + (qb >= 1 ? qb - 1 : qb) * 16384; const char* a2s = kvA + (qb >= 2 ? qb - 2 : qb) * 16384;
            const unsigned ad = lds0 + hh * 49152 + tt * 4096;
            kA.a = LDSV(cur); kA.b = LDSV(cur + 1024);
            SA = MFMA32(kA.a, qb0, f32x16{}); SA = MFMA32(kA.b, qb1, SA);
            mB = rowmax16(SA);
#pragma unroll
            for (int r = 0; r < 16; ++r) { SA[r] -= mB; negm[r] = -mB; }
            kA.a = LDSV(cur + 4096); kA.b = LDSV(cur + 4096 + 1024);
            ZERO_P(); vA = kA;
            ATT_STEP(SA, SB, negm, qb0, qb1, ob, lb, kA, kB, vA, vB, cur + 2 * 4096, cur, COPY4(a0s, ad + 8 * 4096));
            ATT_STEP(SB, SA, negm, qb0, qb1, ob, lb, kB, kA, vB, vA, cur + 3 * 4096, cur + 1 * 4096, COPY4(a1s, ad + 4 * 4096));
            WAIT_BAR(8);
            ATT_STEP(SA, SB, negm, qb0, qb1, ob, lb, kA, kB, vA, vB, nxt, cur + 2 * 4096, COPY4(a2s, ad));
            ATT_STEP(SB, SA, negm, qb0, qb1, ob, lb, kB, kA, vB, vA, nxt + 4096, cur + 3 * 4096, (void)0);
            ATT_STEP(SA, SB, negm, qb0, qb1, ob, lb, kA, kB, vA, vB, nxt + 2 * 4096, nxt, (void)0);
            WAIT_BAR_L(0);
            COPY4(kvB + ((qb + 2) & 15) * 16384, lds0 + P2_B0 + wid * 4096);
            ATT_STEP(SB, SA, negm, qb0, qb1, ob, lb, kB, kA, vB, vA, nxt + 3 * 4096, nxt + 1 * 4096, (void)0);
            ATT_STEP(SA, SB, negm, qb0, qb1, ob, lb, kA, kB, vA, vB, nxt, nxt + 2 * 4096, (void)0);
            ATT_STEP(SB, SA, negm, qb0, qb1, ob, lb, kB, kA, vB, vA, nxt, nxt + 3 * 4096, (void)0);
            ATT_DRAIN(ob, lb, vA);
        }
        LBAR();
        COPY4(kvB + ((qb + 3) & 15) * 16384, lds0 + P2_B1 + wid * 4096);
        STAMP(6);
        {
            const lds_cptr ab = L + hh * 49152 + (8 + tt) * 4096 + lane * 16;
            kA.a = LDSV(ab); kA.b = LDSV(ab + 1024);
            SA = MFMA32(kA.a, qa0, f32x16{}); SA = MFMA32(kA.b, qa1, SA);
            apply_mask<1>(SA, r32, hi);
            const float m = rowmax16(SA);
#pragma unroll
            for (int r = 0; r < 16; ++r) { SA[r] -= m; negm[r] = -m; }
            ZERO_P(); vA = kA;
            const int nfull = qtile < 7 ? qtile : 7;
            const bool far = qtile >= 8;
            int n = nfull + (far ? 1 : 0);
            f32x16 c1 = negm;
            if (far) apply_mask<2>(c1, r32, hi);
            lds_cptr p1 = n == 0 ? ab : (far ? ab - 8 * 4096 : ab - 4096);
            lds_cptr p2 = n <= 1 ? p1 : (far ? ab - 4096 : ab - 2 * 4096);
            kA.a = LDSV(p1); kA.b = LDSV(p1 + 1024);
            ATT_STEP(SA, SB, c1, qa0, qa1, oa, la, kA, kB, vA, vB, p2, ab, (void)0);
            while (n >= 2) {
                const lds_cptr p3 = n > 2 ? p2 - 4096 : p2;
                ATT_STEP(SB, SA, negm, qa0, qa1, oa, la, kB, kA, vB, vA, p3, p1, (void)0);
                const lds_cptr p4 = n > 3 ? p3 - 4096 : p3;
                ATT_STEP(SA, SB, negm, qa0, qa1, oa, la, kA, kB, vA, vB, p4, p2, (void)0);
                p1 = p3; p2 = p4; n -= 2;
            }
            if (n == 1) { ATT_STEP(SB, SA, negm, qa0, qa1, oa, la, kB, kA, vB, vA, p1, p1, (void)0); ATT_DRAIN(oa, la, vA); }
            else { ATT_DRAIN(oa, la, vB); }
            const unsigned oap = WS_O + (unsigned)(qtile * 64 + h) * 2048 + lane * 16;
            if (__all(finite_state(oa, la[0]))) {
                const float inv = __builtin_amdgcn_rcpf(la[0]);
                st_sc1(rs, oap, pack8(oa, 0, inv)); st_sc1(rs, oap + 1024, pack8(oa, 1, inv));
            } else {
                attn_slow(rs, WS_KV + (unsigned)h * (64 * 4096), qtile >= 8 ? qtile - 8 : 0, qtile, qtile, qtile - 8, qa0, qa1, oap, lane);
            }
        }
        LBAR();
        STAMP(7);
        P3_STAGE(0);
        COPY4(kvB + ((qb + 4) & 15) * 16384, lds0 + 0 + wid * 4096);
        WAIT_BAR(10);
        {
            int so0 = P2_B0, so1 = P2_B1, so2 = 0, so3 = 32768;
            lds_cptr cur = hb + so0;
#pragma unroll
            for (int r = 0; r < 16; ++r) negm[r] = -mB;
            kA.a = LDSV(cur); kA.b = LDSV(cur + 1024);
            SA = MFMA32(kA.a, qb0, negm); SA = MFMA32(kA.b, qb1, SA);
            kA.a = LDSV(cur + 4096); kA.b = LDSV(cur + 4096 + 1024);
            ZERO_P(); vA = kA;
            unsigned otouch = 0;
            for (int s = 2; s < 16; ++s) {
                const bool kv = s < 13;
                const char* sb_ = kv ? kvB + ((qb + s + 3) & 15) * 16384 : woA + p3off + (size_t)(s - 12) * (128 * 2048);
                const unsigned sst = kv ? 1024 : 8 * 2048, sv = kv ? voff : p3vw, db_ = lds0 + so3 + wid * 4096;
                const lds_cptr nxt = hb + so1;
                if (s == 9 && wid == 0) wait16(ws + WS_FLAG2 + rb * 2048, tag, lane);
                if (s == 10 && wid < 4) otouch = __builtin_amdgcn_raw_buffer_load_b32(rs, (int)otoff, 0, 16);
                ATT_STEP(SA, SB, negm, qb0, qb1, ob, lb, kA, kB, vA, vB, cur + 2 * 4096, cur, glds16(sb_, sv, db_));
                ATT_STEP(SB, SA, negm, qb0, qb1, ob, lb, kB, kA, vB, vA, cur + 3 * 4096, cur + 1 * 4096, glds16(sb_ + sst, sv, db_ + 1024));
                ATT_STEP(SA, SB, negm, qb0, qb1, ob, lb, kA, kB, vA, vB, nxt, cur + 2 * 4096, glds16(sb_ + 2 * sst, sv, db_ + 2048));
                ATT_STEP(SB, SA, negm, qb0, qb1, ob, lb, kB, kA, vB, vA, nxt + 4096, cur + 3 * 4096, glds16(sb_ + 3 * sst, sv, db_ + 3072));
                cur = nxt; { const int t_ = so0; so0 = so1; so1 = so2; so2 = so3; so3 = t_; }
                WAIT_BAR_L(4);
                if (s == 2 && tid == 0) __hip_atomic_store((gu64*)(ws + WS_FLAG2 + (qb * 16 + hp) * 128), tag, RLX_AGENT);
            }
            asm volatile("" :: "v"(otouch));
            ATT_DRAIN(ob, lb, vA);
        }
        STAMP(8);
        const unsigned obp = WS_O + (unsigned)(qtile * 64 + 32 + h) * 2048 + lane * 16;
        if (__all(finite_state(ob, lb[0]))) {
            const float inv = __builtin_amdgcn_rcpf(lb[0]);
            st_sc1(rs, obp, pack8(ob, 0, inv)); st_sc1(rs, obp + 1024, pack8(ob, 1, inv));
        } else {
            attn_slow(rs, WS_KV + (unsigned)(32 + h) * (64 * 4096), 0, 63, -1, -1, qb0, qb1, obp, lane);
        }
    }

    {
        const int wt = wid & 3, kh = wid >> 2;
        WAIT_BAR_L(2);
        P3_STAGE(4);
        STAMP(9);
        int wa[4];
        { const int f = (2 * (g4 & 1) + (q4 >> 1)) << 1; const int row = 16 * (g4 >> 1) + 4 * (g4 & 1) + q4;
#pragma unroll
          for (int n = 0; n < 4; ++n) wa[n] = kh * 16384 + row * 128 + (((2 * n + (p4 >> 1)) ^ f) * 16) + (p4 & 1) * 8; }
        const unsigned osrc = WS_O + (unsigned)((rb * 4 + wt) * 64 + kh * 16) * 2048 + (g4 >> 1) * 1024 + ((g4 & 1) * 32 + i16) * 16;
        f32x4 accA[2][4], accB[2][4];
#pragma unroll
        for (int t = 0; t < 2; ++t)
#pragma unroll
            for (int n = 0; n < 4; ++n) { accA[t][n] = f32x4{}; accB[t][n] = f32x4{}; }
        u32x4 ofA[8], ofB[8];
#define P3_LDO(OF, c) do { _Pragma("unroll") for (int i = 0; i < 8; ++i) OF[i] = ld_sc1(rs, osrc + (((c) >> 2) * 32 + ((c) & 3) * 4 + (i >> 1)) * 2048 + (i & 1) * 256); } while (0)
#define P3_COMP(ACC, OF, c) do { const lds_cptr wp_ = L + P3_SLOT(c); \
            _Pragma("unroll") for (int hq = 0; hq < 4; ++hq) { \
                bf16x8 wf[4]; \
                _Pragma("unroll") for (int n = 0; n < 4; ++n) wf[n] = trfrag(wp_ + wa[n] + hq * 4096, 1024); \
                _Pragma("unroll") for (int n = 0; n < 4; ++n) { ACC[0][n] = MFMA16(wf[n], BC8(OF[2 * hq]), ACC[0][n]); ACC[1][n] = MFMA16(wf[n], BC8(OF[2 * hq + 1]), ACC[1][n]); } \
            } } while (0)
        P3_LDO(ofA, 0); P3_LDO(ofB, 1);
        const float g = __uint_as_float(__hip_atomic_load((gu32*)(ws + WS_G) + rb * 128 + wt * 32 + i16, RLX_AGENT));
        const float g2 = __uint_as_float(__hip_atomic_load((gu32*)(ws + WS_G) + rb * 128 + wt * 32 + 16 + i16, RLX_AGENT));
        P3_COMP(accA, ofA, 0); LBAR();
        if (tid == 0) __hip_atomic_store((gu64*)(ws + WS_FLAG3 + (qb * 16 + hp) * 128), tag, RLX_AGENT);
        P3_STAGE(5); P3_LDO(ofA, 2);
        P3_COMP(accA, ofB, 1); LBAR(); P3_STAGE(6); P3_LDO(ofB, 3);
        P3_COMP(accA, ofA, 2); LBAR(); P3_STAGE(7);
        if (wid == 0) wait16(ws + WS_FLAG3 + rb * 2048, tag, lane);
        P3_COMP(accA, ofB, 3); LBAR();
        STAMP(10);
        if (blockIdx.x == 0 && tid == 0) __hip_atomic_store((gu64*)(ws + WS_EPOCH), tag, RLX_AGENT);
        unsigned ot1 = 0;
        if (wid < 4) ot1 = __builtin_amdgcn_raw_buffer_load_b32(rs, (int)(otoff + 32 * 2048), 0, 16);
        P3_LDO(ofA, 4); P3_LDO(ofB, 5);
        P3_COMP(accB, ofA, 4); asm volatile("" :: "v"(ot1)); P3_LDO(ofA, 6);
        P3_COMP(accB, ofB, 5); P3_LDO(ofB, 7); LBAR();
        P3_COMP(accB, ofA, 6);
        P3_COMP(accB, ofB, 7);
#undef P3_LDO
#undef P3_COMP
        STAMP(11);
        LBAR();
        LAS f32x4* ex = (LAS f32x4*)(LW + wid * 4096) + lane;
        f32x4 mix[2][4];
#pragma unroll
        for (int t = 0; t < 2; ++t)
#pragma unroll
            for (int n = 0; n < 4; ++n) { const float gt = t ? g2 : g; mix[t][n] = accA[t][n] * gt + accB[t][n] * (1.0f - gt); }
#pragma unroll
        for (int t = 0; t < 2; ++t)
#pragma unroll
            for (int n = 0; n < 2; ++n) ex[(t * 2 + n) * 64] = kh ? mix[t][n] : mix[t][2 + n];
        LBAR();
        const LAS f32x4* ey = (const LAS f32x4*)(LW + (wid ^ 4) * 4096) + lane;
#pragma unroll
        for (int t = 0; t < 2; ++t)
#pragma unroll
            for (int n = 0; n < 2; ++n) {
                const f32x4 y = ey[(t * 2 + n) * 64] + (kh ? mix[t][2 + n] : mix[t][n]);
                unsigned short* op = out + (size_t)(rb * 128 + wt * 32 + t * 16 + i16) * DM + cb * 64 + (2 * kh + n) * 16 + 4 * g4;
                *(u32x2*)op = (u32x2){cvtpk(y[0], y[1]), cvtpk(y[2], y[3])};
            }
        STAMP(12);
    }
}

extern "C" void kernel_launch(void* const* d_in, const int* in_sizes, int n_in, void* d_out, int out_size, void* d_ws, size_t ws_size,
                              hipStream_t stream) {
    (void)hipFuncSetAttribute((const void*)fused_kernel, hipFuncAttributeMaxDynamicSharedMemorySize, LDS_BYTES);
    fused_kernel<<<256, 512, LDS_BYTES, stream>>>((const char*)d_in[0], (const char*)d_in[1], (const char*)d_in[2], (const char*)d_in[4], (const char*)d_in[5],
                                                  (const unsigned short*)d_in[6], (char*)d_ws, (unsigned short*)d_out);
}
```

```cpp
#include <hip/hip_runtime.h>
#include <hip/hip_bf16.h>
#include <stdint.h>
#define NO_CODE_PF 1

#define LAS __attribute__((address_space(3)))
typedef short bf16x8 __attribute__((ext_vector_type(8)));
typedef short s16x4 __attribute__((ext_vector_type(4)));
typedef float f32x16 __attribute__((ext_vector_type(16)));
typedef float f32x4 __attribute__((ext_vector_type(4)));
typedef float f32x2 __attribute__((ext_vector_type(2)));
typedef unsigned u32x4 __attribute__((ext_vector_type(4)));
typedef unsigned u32x2 __attribute__((ext_vector_type(2)));
typedef __bf16 bf16x2_t __attribute__((ext_vector_type(2)));
typedef LAS const char* lds_cptr;
typedef LAS char* lds_ptr;
typedef __attribute__((address_space(1))) unsigned long long gu64;
typedef __attribute__((address_space(1))) unsigned gu32;

#define MFMA32(a, b, c) __builtin_amdgcn_mfma_f32_32x32x16_bf16(a, b, c, 0, 0, 0)
#define MFMA16(a, b, c) __builtin_amdgcn_mfma_f32_16x16x32_bf16(a, b, c, 0, 0, 0)
#define SBAR() __builtin_amdgcn_sched_barrier(0)
#define PIN(x) asm volatile("" : "+v"(x))
#define WAIT_BAR(N) asm volatile("s_waitcnt vmcnt(" #N ")\n\ts_barrier" ::: "memory")
#define WAIT_BAR_L(N) asm volatile("s_waitcnt vmcnt(" #N ") lgkmcnt(0)\n\ts_barrier" ::: "memory")
#define LBAR() asm volatile("s_waitcnt lgkmcnt(0)\n\ts_barrier" ::: "memory")
#define BAR() asm volatile("s_barrier" ::: "memory")
#define RLX_AGENT __ATOMIC_RELAXED, __HIP_MEMORY_SCOPE_AGENT

constexpr int DM = 1024;
constexpr float QSCALE = 0.17677669529663687f * 1.4426950408889634f;

constexpr unsigned WS_KV = 0;
constexpr unsigned WS_O = 16u << 20;
constexpr unsigned WS_G = 24u << 20;
constexpr unsigned WS_EPOCH = WS_G + 65536;
constexpr unsigned WS_FLAG1 = WS_EPOCH + 4096;
constexpr unsigned WS_FLAG2 = WS_FLAG1 + 32768;
constexpr unsigned WS_FLAG3 = WS_FLAG2 + 32768;
constexpr unsigned WS_USED = 32u << 20;
constexpr int LDS_BYTES = 163840;

#define STAMP(i) do {} while (0)

__device__ __forceinline__ unsigned cvtpk(float lo, float hi) {
    f32x2 v = {lo, hi}; bf16x2_t b = __builtin_convertvector(v, bf16x2_t); return __builtin_bit_cast(unsigned, b);
}
__device__ __forceinline__ void glds16(const void* sbase, unsigned voff, unsigned lds_dst) {
    asm volatile("s_mov_b32 m0, %2\n\ts_nop 0\n\tglobal_load_lds_dwordx4 %0, %1" : : "v"(voff), "s"(sbase), "s"(lds_dst) : "memory");
}
__device__ __forceinline__ void touch_lines64(const void* vaddr, unsigned lds_dst) {
    asm volatile("s_mov_b32 m0, %1\n\ts_nop 0\n\tglobal_load_lds_dword %0, off" : : "v"(vaddr), "s"(lds_dst) : "memory");
}
__device__ __forceinline__ void st_sc1(__amdgpu_buffer_rsrc_t rs, unsigned off, u32x4 v) { __builtin_amdgcn_raw_buffer_store_b128(v, rs, (int)off, 0, 16); }
__device__ __forceinline__ u32x4 ld_sc1(__amdgpu_buffer_rsrc_t rs, unsigned off) { return __builtin_amdgcn_raw_buffer_load_b128(rs, (int)off, 0, 16); }
__device__ __forceinline__ void wait16(const char* base, unsigned long long tag, int lane) {
    gu64* f = (gu64*)(base + (lane & 15) * 128);
    for (unsigned spins = 0; spins < (1u << 18); ++spins) {
        const unsigned long long v = __hip_atomic_load(f, RLX_AGENT);
        if (__all(v == tag)) break;
        __builtin_amdgcn_s_sleep(1);
    }
}
__device__ __forceinline__ s16x4 trread(lds_cptr p) {
    typedef short v4i16_t __attribute__((ext_vector_type(4)));
    return __builtin_bit_cast(s16x4, __builtin_amdgcn_ds_read_tr16_b64_v4i16((LAS v4i16_t*)p));
}
__device__ __forceinline__ bf16x8 trfrag(lds_cptr p, int second_off) {
    const s16x4 lo = trread(p), hi = trread(p + second_off);
    return (bf16x8){lo[0], lo[1], lo[2], lo[3], hi[0], hi[1], hi[2], hi[3]};
}
__device__ __forceinline__ u32x4 pack8(const f32x16& a, int s, float sc) {
    u32x4 r;
    r[0] = cvtpk(a[8 * s + 0] * sc, a[8 * s + 1] * sc); r[1] = cvtpk(a[8 * s + 2] * sc, a[8 * s + 3] * sc);
    r[2] = cvtpk(a[8 * s + 4] * sc, a[8 * s + 5] * sc); r[3] = cvtpk(a[8 * s + 6] * sc, a[8 * s + 7] * sc);
    return r;
}
__device__ __forceinline__ u32x4 pack8n(const f32x16& a, int s) {
    u32x4 r;
    r[0] = cvtpk(a[8 * s + 0], a[8 * s + 1]); r[1] = cvtpk(a[8 * s + 2], a[8 * s + 3]);
    r[2] = cvtpk(a[8 * s + 4], a[8 * s + 5]); r[3] = cvtpk(a[8 * s + 6], a[8 * s + 7]);
    return r;
}
__device__ __forceinline__ u32x4 pack44(const f32x4& a, const f32x4& b, float sc) {
    return (u32x4){cvtpk(a[0] * sc, a[1] * sc), cvtpk(a[2] * sc, a[3] * sc), cvtpk(b[0] * sc, b[1] * sc), cvtpk(b[2] * sc, b[3] * sc)};
}
__device__ __forceinline__ u32x4 pack44n(const f32x4& a, const f32x4& b) {
    return (u32x4){cvtpk(a[0], a[1]), cvtpk(a[2], a[3]), cvtpk(b[0], b[1]), cvtpk(b[2], b[3])};
}
#define EXP2(v) __builtin_amdgcn_exp2f(v)
#define LDSV(p) (*(const LAS bf16x8*)(p))
#define LDSW(p) (*(LAS u32x4*)(p))
#define BC8(v) __builtin_bit_cast(bf16x8, v)

__device__ __forceinline__ float halfmax(float m) {
    auto rr = __builtin_amdgcn_permlane32_swap(__float_as_uint(m), __float_as_uint(m), false, false);
    return __builtin_fmaxf(__uint_as_float(rr[0]), __uint_as_float(rr[1]));
}
__device__ __forceinline__ float rowmax16(const f32x16& p) {
    float a = __builtin_fmaxf(p[0], p[1]);
#pragma unroll
    for (int r = 2; r < 16; ++r) a = __builtin_fmaxf(a, p[r]);
    return halfmax(a);
}
template <int MASK> __device__ __forceinline__ void apply_mask(f32x16& s, int r32, int hi) {
#pragma unroll
    for (int r = 0; r < 16; ++r) {
        const int kl = (r & 3) + 8 * (r >> 2) + 4 * hi;
        const bool keep = (MASK == 1) ? (kl <= r32) : (kl > r32);
        if (!keep) s[r] = -INFINITY;
    }
}
__device__ __forceinline__ void attn_slow(__amdgpu_buffer_rsrc_t rs, unsigned kvh, int t_first, int t_last, int t_diag, int t_far, bf16x8 q0, bf16x8 q1, unsigned odst, int lane) {
    const int r32 = lane & 31, hi = lane >> 5;
    f32x16 o = f32x16{}; float m = -INFINITY, l = 0.f;
    for (int t = t_last; t >= t_first; --t) {
        const unsigned p = kvh + (unsigned)t * 4096 + lane * 16;
        const bf16x8 k0 = BC8(ld_sc1(rs, p)), k1 = BC8(ld_sc1(rs, p + 1024)), v0 = BC8(ld_sc1(rs, p + 2048)), v1 = BC8(ld_sc1(rs, p + 3072));
        f32x16 s = MFMA32(k0, q0, f32x16{}); s = MFMA32(k1, q1, s);
        if (t == t_diag) apply_mask<1>(s, r32, hi);
        if (t == t_far) apply_mask<2>(s, r32, hi);
        const float mn = __builtin_fmaxf(m, rowmax16(s));
        const float ms = (mn == -INFINITY) ? 0.f : mn;
        const float alpha = EXP2(m - ms);
        float rs_ = 0.f;
#pragma unroll
        for (int r = 0; r < 16; ++r) { s[r] = EXP2(s[r] - ms); o[r] *= alpha; }
        const u32x4 p0 = pack8n(s, 0), p1 = pack8n(s, 1);
#pragma unroll
        for (int i = 0; i < 4; ++i) rs_ += __uint_as_float(p0[i] << 16) + __uint_as_float(p0[i] & 0xffff0000u) + __uint_as_float(p1[i] << 16) + __uint_as_float(p1[i] & 0xffff0000u);
        l = l * alpha + rs_; m = mn;
        o = MFMA32(v0, BC8(p0), o); o = MFMA32(v1, BC8(p1), o);
    }
    auto rr = __builtin_amdgcn_permlane32_swap(__float_as_uint(l), __float_as_uint(l), false, false);
    l = __uint_as_float(rr[0]) + __uint_as_float(rr[1]);
    const float inv = 1.0f / l;
    st_sc1(rs, odst, pack8(o, 0, inv)); st_sc1(rs, odst + 1024, pack8(o, 1, inv));
}
__device__ __forceinline__ bool finite_state(const f32x16& o, float l) {
    float a = 0.f, b = 0.f;
#pragma unroll
    for (int r = 0; r < 16; r += 2) { a = __builtin_fmaf(o[r], 0.f, a); b = __builtin_fmaf(o[r + 1], 0.f, b); }
    return (l < 1.0e30f) && (a + b == 0.f);
}
struct Fr2 { bf16x8 a, b; };
#define EX4(S, i) do { S[i] = EXP2(S[i]); S[(i) + 1] = EXP2(S[(i) + 1]); S[(i) + 2] = EXP2(S[(i) + 2]); S[(i) + 3] = EXP2(S[(i) + 3]); PIN(S); } while (0)
#define PK4(W, S, i) do { W[0] = cvtpk(S[i], S[(i) + 1]); W[1] = cvtpk(S[(i) + 2], S[(i) + 3]); W[2] = cvtpk(S[(i) + 4], S[(i) + 5]); W[3] = cvtpk(S[(i) + 6], S[(i) + 7]); PIN(W); } while (0)
#define ATT_STEP(SC, SN, CN, Q0, Q1, O, LS, KC, KN, VC, VN, KP, VP, G0) do { \
        SBAR(); \
        SN = MFMA32(KC.a, Q0, CN); PIN(SN); SBAR(); \
        KN.a = LDSV(KP); KN.b = LDSV((KP) + 1024); EX4(SC, 0); SBAR(); \
        O = MFMA32(VC.a, BC8(pw0), O); PIN(O); SBAR(); \
        VN.a = LDSV((VP) + 2048); VN.b = LDSV((VP) + 3072); EX4(SC, 4); SBAR(); \
        SN = MFMA32(KC.b, Q1, SN); PIN(SN); SBAR(); \
        EX4(SC, 8); SBAR(); \
        LS = MFMA16(sel, BC8(pw0), LS); PIN(LS); SBAR(); \
        PK4(pw0, SC, 0); SBAR(); \
        O = MFMA32(VC.b, BC8(pw1), O); PIN(O); SBAR(); \
        EX4(SC, 12); SBAR(); \
        LS = MFMA16(sel, BC8(pw1), LS); PIN(LS); SBAR(); \
        G0; PK4(pw1, SC, 8); SBAR(); \
    } while (0)
#define ATT_DRAIN(O, LS, VC) do { \
        O = MFMA32(VC.a, BC8(pw0), O); O = MFMA32(VC.b, BC8(pw1), O); \
        LS = MFMA16(sel, BC8(pw0), LS); LS = MFMA16(sel, BC8(pw1), LS); \
    } while (0)
#define ZERO_P() do { pw0 = u32x4{}; pw1 = u32x4{}; } while (0)

constexpr int CODE_LINES = 288;
constexpr int P1A_SLOT = 49152, P1A_DUMMY = 147456;
constexpr int P1B_SLOT = 32768;
constexpr int P2_B0 = 131072, P2_B1 = 98304, P2_WO0 = 65536;
struct FragsA { bf16x8 x[4]; s16x4 wl[4], wh[4]; };
struct FragsB { bf16x8 x[2]; s16x4 wl[4], wh[4]; };
#define WFR(F, n) (bf16x8){F.wl[n][0], F.wl[n][1], F.wl[n][2], F.wl[n][3], F.wh[n][0], F.wh[n][1], F.wh[n][2], F.wh[n][3]}

__global__ __launch_bounds__(512, 2) void fused_kernel(const char* __restrict__ x, const char* __restrict__ wA, const char* __restrict__ woA,
                                                       const char* __restrict__ wB, const char* __restrict__ woB,
                                                       const unsigned short* __restrict__ gate_w, char* __restrict__ ws, unsigned short* __restrict__ out) {
    extern __shared__ __attribute__((aligned(16))) char lds[];
    const int tid = threadIdx.x, lane = tid & 63;
    const int wid = __builtin_amdgcn_readfirstlane(tid >> 6);
    const int xcd = blockIdx.x & 7, jj = blockIdx.x >> 3;
    const int hp = xcd * 2 + (jj & 1), qb = jj >> 1;
    const unsigned lds0 = (unsigned)(uintptr_t)lds;
    const lds_cptr L = (lds_cptr)lds;
    const lds_ptr LW = (lds_ptr)lds;
    const __amdgpu_buffer_rsrc_t rs = __builtin_amdgcn_make_buffer_rsrc((void*)ws, 0, (int)WS_USED, 0x00020000);
    const int i16 = lane & 15, g4 = lane >> 4, q4 = (lane >> 2) & 3, p4 = lane & 3;
    const int r32 = lane & 31, hi = lane >> 5;
    STAMP(0);
    unsigned long long tag;

    const char* xs = x + (size_t)qb * 128 * 2048;
    unsigned vx[2];
#pragma unroll
    for (int p = 0; p < 2; ++p) {
        const int row = 16 * wid + 8 * p + (lane >> 3);
        const int sw = ((row >> 1) & 3) | (((row >> 4) & 1) << 2);
        vx[p] = (unsigned)(row * 2048 + (((lane & 7) ^ sw) * 16));
    }
    const unsigned xdst = lds0 + wid * 2048;
    unsigned vw[2];
#pragma unroll
    for (int e = 0; e < 2; ++e) vw[e] = (unsigned)((lane >> 3) * 6144 + hp * 128 + (((lane & 7) ^ ((((lane >> 4) & 1) << 1) | (e << 2))) * 16));
    const int wf_f = (((q4 >> 1) & 1) << 1) | ((g4 & 1) << 2);

    u32x4 kvs[8]; unsigned kvg0; int kvoK, kvoV;
    const char* wqsrc = (wid < 4 ? wA : wB) + (size_t)((wid & 3) * 16) * 6144;
    const unsigned wqdst = lds0 + 16384 + wid * 2048;
#define P1B_X(T, slot, p) glds16(xs + (15 - (T)) * 128, vx[p], xdst + (slot) + (p) * 1024)
#define P1B_W(T, slot, i) glds16(wqsrc + (size_t)((15 - (T)) * 64 + (i) * 8) * 6144, vw[i], wqdst + (slot) + (i) * 1024)
    {
        const int wm = wid >> 2, wn = wid & 3, br = wn >> 1, hh = wn & 1;
        const int gtok = qb * 128 + hp * 8 + wid;
        u32x4 ga0, ga1, gg0, gg1;
        { const char* xr = x + (size_t)gtok * 2048; const unsigned go = lane * 32;
          asm volatile("global_load_dwordx4 %0, %4, %5\n\tglobal_load_dwordx4 %1, %4, %5 offset:16\n\tglobal_load_dwordx4 %2, %4, %6\n\tglobal_load_dwordx4 %3, %4, %6 offset:16"
                       : "=&v"(ga0), "=&v"(ga1), "=&v"(gg0), "=&v"(gg1) : "v"(go), "s"(xr), "s"(gate_w) : "memory"); }
        const char* wsrc = (wid < 4 ? wA : wB) + (size_t)((wid & 1) * 32) * 6144 + (1 + ((wid >> 1) & 1)) * 2048;
        const unsigned wdst = lds0 + 16384 + wid * 4096;
#define P1A_X(T, slot, p) glds16(xs + (T) * 128, vx[p], xdst + (slot) + (p) * 1024)
#define P1A_W(T, slot, i) glds16(wsrc + (size_t)((T) * 64 + (i) * 8) * 6144, vw[(i) & 1], wdst + (slot) + (i) * 1024)
        const char* pfb; unsigned pfstep;
        { const int gw = blockIdx.x * 8 + wid;
          if (lane == 0) { pfb = x + (size_t)gw * 2048; pfstep = 128; }
          else if (lane < 3) { const int id = gw * 2 + lane - 1, rem = id & 2047;
                 pfb = ((id >> 11) ? wB : wA) + (size_t)((rem >> 5) * 6144 + (1 + ((rem >> 4) & 1)) * 2048 + (rem & 15) * 128); pfstep = 64 * 6144; }
          else { const int rho = gw * 2 + (lane & 1);
                 if (rho < 2048) pfb = ((rho >> 10) ? wB : wA) + (size_t)(rho & 1023) * 6144; else pfb = (((rho - 2048) >> 10) ? woB : woA) + (size_t)(rho & 1023) * 2048;
                 pfstep = 128; } }
#define P1A_PREF(T, F) do { if (lane < 5) touch_lines64(pfb + (size_t)(lane < 3 ? (T) : (F)) * pfstep, lds0 + P1A_DUMMY); } while (0)
        P1A_X(0, 0, 0); P1A_X(0, 0, 1); P1A_W(0, 0, 0); P1A_W(0, 0, 1); P1A_W(0, 0, 2); P1A_W(0, 0, 3);
        P1A_X(1, P1A_SLOT, 0); P1A_W(1, P1A_SLOT, 0); P1A_W(1, P1A_SLOT, 1);
        P1A_X(1, P1A_SLOT, 1); P1A_W(1, P1A_SLOT, 2); P1A_W(1, P1A_SLOT, 3);
        {
            unsigned long long pc; asm volatile("s_getpc_b64 %0" : "=s"(pc));
            const int id = jj * 8 + wid + (lane & 1) * 256;
            if (lane < 2) touch_lines64((const char*)pc, lds0 + P1A_DUMMY);
        }
        P1A_PREF(2, 12); P1A_PREF(3, 13); P1A_PREF(4, 14); P1A_PREF(5, 15);
        P1A_X(2, 2 * P1A_SLOT, 0); P1A_W(2, 2 * P1A_SLOT, 0); P1A_W(2, 2 * P1A_SLOT, 1);
        asm volatile("s_load_dwordx2 %0, %1, 0x0\n\ts_waitcnt lgkmcnt(0)" : "=s"(tag) : "s"(ws + WS_EPOCH) : "memory");
        tag += 1;
        asm volatile("s_waitcnt vmcnt(20)" : "+v"(ga0), "+v"(ga1), "+v"(gg0), "+v"(gg1) :: "memory");
        {
            float s = 0.f;
#pragma unroll
            for (int i = 0; i < 4; ++i) {
                s += __uint_as_float(ga0[i] << 16) * __uint_as_float(gg0[i] << 16) + __uint_as_float(ga0[i] & 0xffff0000u) * __uint_as_float(gg0[i] & 0xffff0000u);
                s += __uint_as_float(ga1[i] << 16) * __uint_as_float(gg1[i] << 16) + __uint_as_float(ga1[i] & 0xffff0000u) * __uint_as_float(gg1[i] & 0xffff0000u);
            }
#pragma unroll
            for (int o = 32; o > 0; o >>= 1) s += __shfl_xor(s, o);
            if (lane == 0) __hip_atomic_store((gu32*)(ws + WS_G) + gtok, __float_as_uint(1.0f / (1.0f + __expf(-s))), RLX_AGENT);
        }
        int xa[2];
        { const int row = wm * 64 + 16 * (i16 >> 3) + (i16 & 7); const int sw = ((row >> 1) & 3) | (((row >> 4) & 1) << 2);
          xa[0] = row * 128 + ((g4 ^ sw) * 16); xa[1] = row * 128 + (((4 + g4) ^ sw) * 16); }
        int wa[2];
#pragma unroll
        for (int np = 0; np < 2; ++np) wa[np] = 16384 + br * 16384 + (8 * g4 + q4) * 128 + (((hh * 4 + np * 2 + (p4 >> 1)) ^ wf_f) * 16) + (p4 & 1) * 8;
        f32x4 acc[4][4];
#pragma unroll
        for (int m = 0; m < 4; ++m)
#pragma unroll
            for (int n = 0; n < 4; ++n) acc[m][n] = f32x4{};
#define A_LX(F, m, sl, kh) F.x[m] = LDSV(L + (sl) + xa[kh] + ((m) >> 1) * 4096 + ((m) & 1) * 1024)
#define A_LWL(F, n, sl, kh) F.wl[n] = trread(L + (sl) + wa[(n) & 1] + ((n) >> 1) * 8192 + (kh) * 4096)
#define A_LWH(F, n, sl, kh) F.wh[n] = trread(L + (sl) + wa[(n) & 1] + ((n) >> 1) * 8192 + (kh) * 4096 + 512)
#define A_M(F, m, n) do { acc[m][n] = ((n) < 2) ? MFMA16(WFR(F, n), F.x[m], acc[m][n]) : MFMA16(F.x[m], WFR(F, n), acc[m][n]); } while (0)
#define A_HALF(FC, FN, LD, sl, kh, G0, G1, G2) do { \
            A_M(FC, 0, 0); if (LD) A_LX(FN, 0, sl, kh); SBAR(); A_M(FC, 1, 0); if (LD) A_LWL(FN, 0, sl, kh); SBAR(); \
            A_M(FC, 2, 0); if (LD) A_LWH(FN, 0, sl, kh); SBAR(); A_M(FC, 3, 0); G0; SBAR(); \
            A_M(FC, 0, 1); if (LD) A_LX(FN, 1, sl, kh); SBAR(); A_M(FC, 1, 1); if (LD) A_LWL(FN, 1, sl, kh); SBAR(); \
            A_M(FC, 2, 1); if (LD) A_LWH(FN, 1, sl, kh); SBAR(); A_M(FC, 3, 1); G1; SBAR(); \
            A_M(FC, 0, 2); if (LD) A_LX(FN, 2, sl, kh); SBAR(); A_M(FC, 1, 2); if (LD) A_LWL(FN, 2, sl, kh); SBAR(); \
            A_M(FC, 2, 2); if (LD) A_LWH(FN, 2, sl, kh); SBAR(); A_M(FC, 3, 2); G2; SBAR(); \
            A_M(FC, 0, 3); if (LD) A_LX(FN, 3, sl, kh); SBAR(); A_M(FC, 1, 3); if (LD) A_LWL(FN, 3, sl, kh); SBAR(); \
            A_M(FC, 2, 3); if (LD) A_LWH(FN, 3, sl, kh); SBAR(); A_M(FC, 3, 3); SBAR(); \
        } while (0)
        WAIT_BAR(15);
        STAMP(1);
        FragsA FA, FB;
#pragma unroll
        for (int m = 0; m < 4; ++m) { A_LX(FA, m, 0, 0); A_LWL(FA, m, 0, 0); A_LWH(FA, m, 0, 0); }
        int s0 = 0, s1 = P1A_SLOT, s2 = 2 * P1A_SLOT;
#define A_ROT() do { const int tmp = s0; s0 = s1; s1 = s2; s2 = tmp; } while (0)
        for (int t = 0; t < 13; ++t) {
            A_HALF(FA, FB, true, s0, 1, P1A_X(t + 2, s2, 1), P1A_W(t + 2, s2, 2), P1A_W(t + 2, s2, 3));
            P1A_PREF(t + 6 < 16 ? t + 6 : 15, t);
            WAIT_BAR_L(8);
            A_HALF(FB, FA, true, s1, 0, P1A_X(t + 3, s0, 0), P1A_W(t + 3, s0, 0), P1A_W(t + 3, s0, 1));
            A_ROT();
        }
        A_HALF(FA, FB, true, s0, 1, P1A_X(15, s2, 1), P1A_W(15, s2, 2), P1A_W(15, s2, 3));
        WAIT_BAR_L(6);
        A_HALF(FB, FA, true, s1, 0, P1B_X(0, 65536, 0), P1B_W(0, 65536, 0), { P1B_X(0, 65536, 1); P1B_W(0, 65536, 1); });
        A_ROT();
        A_HALF(FA, FB, true, s0, 1, (void)0, (void)0, (void)0);
        WAIT_BAR_L(4);
        A_HALF(FB, FA, true, s1, 0, P1B_X(1, 98304, 0), P1B_W(1, 98304, 0), { P1B_X(1, 98304, 1); P1B_W(1, 98304, 1); });
        A_ROT();
#undef A_ROT
        A_HALF(FA, FB, true, s0, 1, (void)0, (void)0, (void)0);
        A_HALF(FB, FA, false, 0, 0, (void)0, (void)0, (void)0);
#undef A_HALF
#undef A_M
#undef A_LX
#undef A_LWL
#undef A_LWH
#undef P1A_X
#undef P1A_W
        const int h = hp * 2 + hh;
        const int so = (g4 >> 1) * 1024 + (g4 & 1) * 512;
        kvg0 = WS_KV + (unsigned)((br * 32 + h) * 64 + qb * 4 + wm * 2) * 4096;
        kvoK = so + (16 * (i16 >> 3) + (i16 & 7)) * 16; kvoV = 2048 + so + i16 * 16;
#pragma unroll
        for (int T = 0; T < 2; ++T) {
#pragma unroll
            for (int mp = 0; mp < 2; ++mp) kvs[4 * T + mp] = pack44n(acc[2 * T + mp][0], acc[2 * T + mp][1]);
#pragma unroll
            for (int np = 0; np < 2; ++np) kvs[4 * T + 2 + np] = pack44n(acc[2 * T][2 + np], acc[2 * T + 1][2 + np]);
        }
        STAMP(2);
    }
#define KV_ST(i) st_sc1(rs, kvg0 + ((i) >> 2) * 4096 + (((i) & 2) ? kvoV + ((i) & 1) * 256 : kvoK + ((i) & 1) * 128), kvs[i])

    const int hh = wid >> 2, tt = wid & 3, h = hp * 2 + hh;
    const int qtile = qb * 4 + tt;
    const char* kvA = ws + WS_KV + (size_t)(h * 64 + tt) * 4096;
    const char* kvB = kvA + (size_t)32 * 64 * 4096;
    const unsigned voff = lane * 16;
#define COPY4(src, dst) do { glds16((src), voff, (dst)); glds16((src) + 1024, voff, (dst) + 1024); glds16((src) + 2048, voff, (dst) + 2048); glds16((src) + 3072, voff, (dst) + 3072); } while (0)
    bf16x8 qa0, qa1, qb0, qb1;
    {
        WAIT_BAR_L(4);
        P1B_X(2, 0, 0); P1B_W(2, 0, 0); P1B_X(2, 0, 1); P1B_W(2, 0, 1);
        P1B_X(3, 32768, 0); P1B_W(3, 32768, 0);
        int xa[2];
        { const int row = tt * 32 + 16 * (i16 >> 3) + (i16 & 7); const int sw = ((row >> 1) & 3) | (((row >> 4) & 1) << 2);
          xa[0] = row * 128 + ((g4 ^ sw) * 16); xa[1] = row * 128 + (((4 + g4) ^ sw) * 16); }
        int wa[2];
#pragma unroll
        for (int np = 0; np < 2; ++np) wa[np] = 16384 + (8 * g4 + q4) * 128 + (((hh * 4 + np * 2 + (p4 >> 1)) ^ wf_f) * 16) + (p4 & 1) * 8;
        f32x4 acc[2][4];
#pragma unroll
        for (int m = 0; m < 2; ++m)
#pragma unroll
            for (int n = 0; n < 4; ++n) acc[m][n] = f32x4{};
#define B_LX(F, m, sl, kh) F.x[m] = LDSV(L + (sl) + xa[kh] + (m) * 1024)
#define B_LWL(F, n, sl, kh) F.wl[n] = trread(L + (sl) + wa[(n) & 1] + ((n) >> 1) * 8192 + (kh) * 4096)
#define B_LWH(F, n, sl, kh) F.wh[n] = trread(L + (sl) + wa[(n) & 1] + ((n) >> 1) * 8192 + (kh) * 4096 + 512)
#define B_M(F, m, n) do { acc[m][n] = MFMA16(WFR(F, n), F.x[m], acc[m][n]); } while (0)
#define B_HALF(FC, FN, LD, sl, kh, G0, G1) do { \
            B_M(FC, 0, 0); if (LD) { B_LX(FN, 0, sl, kh); B_LWL(FN, 0, sl, kh); } SBAR(); B_M(FC, 1, 0); if (LD) { B_LWH(FN, 0, sl, kh); } SBAR(); \
            B_M(FC, 0, 1); if (LD) { B_LX(FN, 1, sl, kh); B_LWL(FN, 1, sl, kh); } SBAR(); B_M(FC, 1, 1); if (LD) { B_LWH(FN, 1, sl, kh); } G0; SBAR(); \
            B_M(FC, 0, 2); if (LD) { B_LWL(FN, 2, sl, kh); B_LWH(FN, 2, sl, kh); } SBAR(); B_M(FC, 1, 2); G1; SBAR(); \
            B_M(FC, 0, 3); if (LD) { B_LWL(FN, 3, sl, kh); B_LWH(FN, 3, sl, kh); } SBAR(); B_M(FC, 1, 3); SBAR(); \
        } while (0)
        STAMP(3);
        FragsB FA, FB;
#pragma unroll
        for (int m = 0; m < 2; ++m) B_LX(FA, m, 65536, 0);
#pragma unroll
        for (int n = 0; n < 4; ++n) { B_LWL(FA, n, 65536, 0); B_LWH(FA, n, 65536, 0); }
        int s0 = 65536, s1 = 98304, s2 = 0, s3 = 32768;
#define P1B_IT(u, N, PRE, POST) do { \
            PRE; \
            B_HALF(FA, FB, true, s0, 1, if ((u) + 3 < 16) P1B_X((u) + 3, s3, 1), if ((u) + 3 < 16) P1B_W((u) + 3, s3, 1)); \
            if ((u) < 8) { KV_ST((u) & 7); } \
            WAIT_BAR_L(N); \
            POST; \
            B_HALF(FB, FA, true, s1, 0, if ((u) + 4 < 16) P1B_X((u) + 4, s0, 0), if ((u) + 4 < 16) P1B_W((u) + 4, s0, 0)); \
            { const int tmp = s0; s0 = s1; s1 = s2; s2 = s3; s3 = tmp; } \
        } while (0)
        P1B_IT(0, 9, (void)0, (void)0); P1B_IT(1, 10, (void)0, (void)0);
        P1B_IT(2, 11, (void)0, (void)0); P1B_IT(3, 11, (void)0, (void)0); P1B_IT(4, 11, (void)0, (void)0);
        P1B_IT(5, 11, (void)0, (void)0); P1B_IT(6, 11, (void)0, (void)0); P1B_IT(7, 11, (void)0, (void)0);
        P1B_IT(8, 10, (void)0, (void)0); P1B_IT(9, 9, (void)0, (void)0);
        P1B_IT(10, 8, (void)0, if (tid == 0) __hip_atomic_store((gu64*)(ws + WS_FLAG1 + (hp * 16 + qb) * 128), tag, RLX_AGENT));
        P1B_IT(11, 12, COPY4(kvB + qb * 16384, lds0 + P2_B0 + wid * 4096), (void)0);
        P1B_IT(12, 12, (void)0, (void)0);
        P1B_IT(13, 4, (void)0, if (wid == 0) { wait16(ws + WS_FLAG1 + hp * 2048, tag, lane); __builtin_amdgcn_fence(__ATOMIC_ACQUIRE, "agent"); });
        P1B_IT(14, 0, (void)0, (void)0);
#undef P1B_IT
        B_HALF(FA, FB, true, s0, 1, (void)0, (void)0);
        B_HALF(FB, FA, false, 0, 0, (void)0, (void)0);
#undef B_HALF
#undef B_M
#undef B_LX
#undef B_LWL
#undef B_LWH
#undef P1B_X
#undef P1B_W
        STAMP(4);
        LBAR();
        const lds_ptr scr = LW + hh * 49152 + (8 + tt) * 4096;
        const int so = (g4 >> 1) * 1024 + (g4 & 1) * 512 + (16 * (i16 >> 3) + (i16 & 7)) * 16;
#pragma unroll
        for (int b = 0; b < 2; ++b)
#pragma unroll
            for (int mp = 0; mp < 2; ++mp) LDSW(scr + b * 2048 + so + mp * 128) = pack44(acc[mp][2 * b], acc[mp][2 * b + 1], QSCALE);
        asm volatile("s_waitcnt lgkmcnt(0)" ::: "memory");
        qa0 = LDSV(scr + lane * 16); qa1 = LDSV(scr + 1024 + lane * 16); qb0 = LDSV(scr + 2048 + lane * 16); qb1 = LDSV(scr + 3072 + lane * 16);
        asm volatile("s_waitcnt lgkmcnt(0)" : "+v"(qa0), "+v"(qa1), "+v"(qb0), "+v"(qb1) :: "memory");
        COPY4(kvB + ((qb + 1) & 15) * 16384, lds0 + P2_B1 + wid * 4096);
    }

    const int rb = 4 * (xcd >> 1) + (jj & 3), cb = 8 * (xcd & 1) + (jj >> 2);
    const size_t p3off = (size_t)((wid >> 2) * 512 + (wid & 3) * 32) * 2048 + cb * 128;
    const unsigned p3vw = (unsigned)((lane >> 3) * 2048 + (((lane & 7) ^ (((lane >> 4) & 3) << 1)) * 16));
    const unsigned otl = (unsigned)(((jj >> 2) * 4 + (wid & 3)) * 64 + lane);
    const unsigned otoff = WS_O + (unsigned)(rb * 4 + (otl >> 9)) * (64 * 2048) + (otl & 511) * 128;
#define P3_SLOT(c) (((c) % 5) == 0 ? 65536 : ((c) % 5) == 1 ? 0 : ((c) % 5) == 2 ? 32768 : ((c) % 5) == 3 ? 131072 : 98304)
#define P3_STAGE(c) do { const char* src_ = (((c) >> 2) ? woB : woA) + p3off + (size_t)((c) & 3) * (128 * 2048); const unsigned dst_ = lds0 + P3_SLOT(c) + wid * 4096; \
        glds16(src_, p3vw, dst_); glds16(src_ + 8 * 2048, p3vw, dst_ + 1024); glds16(src_ + 16 * 2048, p3vw, dst_ + 2048); glds16(src_ + 24 * 2048, p3vw, dst_ + 3072); } while (0)
    {
        bf16x8 sel;
        { const short one = (((lane >> 4) & 1) == ((lane >> 2) & 1)) ? (short)0x3F80 : (short)0;
#pragma unroll
          for (int i = 0; i < 8; ++i) sel[i] = one; }
        f32x16 SA, SB, negm, oa = f32x16{}, ob = f32x16{};
        f32x4 la = f32x4{}, lb = f32x4{};
        Fr2 kA, kB, vA, vB; u32x4 pw0, pw1;
        const lds_cptr hb = L + hh * 16384 + lane * 16;
        float mB;
        STAMP(5);
        {
            const lds_cptr cur = hb + P2_B0, nxt = hb + P2_B1;
            const char* a0s = kvA + qb * 16384; const char* a1s = kvA + (qb >= 1 ? qb - 1 : qb) * 16384; const char* a2s = kvA + (qb >= 2 ? qb - 2 : qb) * 16384;
            const unsigned ad = lds0 + hh * 49152 + tt * 4096;
            kA.a = LDSV(cur); kA.b = LDSV(cur + 1024);
            SA = MFMA32(kA.a, qb0, f32x16{}); SA = MFMA32(kA.b, qb1, SA);
            mB = rowmax16(SA);
#pragma unroll
            for (int r = 0; r < 16; ++r) { SA[r] -= mB; negm[r] = -mB; }
            kA.a = LDSV(cur + 4096); kA.b = LDSV(cur + 4096 + 1024);
            ZERO_P(); vA = kA;
            ATT_STEP(SA, SB, negm, qb0, qb1, ob, lb, kA, kB, vA, vB, cur + 2 * 4096, cur, COPY4(a0s, ad + 8 * 4096));
            ATT_STEP(SB, SA, negm, qb0, qb1, ob, lb, kB, kA, vB, vA, cur + 3 * 4096, cur + 1 * 4096, COPY4(a1s, ad + 4 * 4096));
            WAIT_BAR(8);
            ATT_STEP(SA, SB, negm, qb0, qb1, ob, lb, kA, kB, vA, vB, nxt, cur + 2 * 4096, COPY4(a2s, ad));
            ATT_STEP(SB, SA, negm, qb0, qb1, ob, lb, kB, kA, vB, vA, nxt + 4096, cur + 3 * 4096, (void)0);
            ATT_STEP(SA, SB, negm, qb0, qb1, ob, lb, kA, kB, vA, vB, nxt + 2 * 4096, nxt, (void)0);
            WAIT_BAR_L(0);
            COPY4(kvB + ((qb + 2) & 15) * 16384, lds0 + P2_B0 + wid * 4096);
            ATT_STEP(SB, SA, negm, qb0, qb1, ob, lb, kB, kA, vB, vA, nxt + 3 * 4096, nxt + 1 * 4096, (void)0);
            ATT_STEP(SA, SB, negm, qb0, qb1, ob, lb, kA, kB, vA, vB, nxt, nxt + 2 * 4096, (void)0);
            ATT_STEP(SB, SA, negm, qb0, qb1, ob, lb, kB, kA, vB, vA, nxt, nxt + 3 * 4096, (void)0);
            ATT_DRAIN(ob, lb, vA);
        }
        LBAR();
        COPY4(kvB + ((qb + 3) & 15) * 16384, lds0 + P2_B1 + wid * 4096);
        STAMP(6);
        {
            const lds_cptr ab = L + hh * 49152 + (8 + tt) * 4096 + lane * 16;
            kA.a = LDSV(ab); kA.b = LDSV(ab + 1024);
            SA = MFMA32(kA.a, qa0, f32x16{}); SA = MFMA32(kA.b, qa1, SA);
            apply_mask<1>(SA, r32, hi);
            const float m = rowmax16(SA);
#pragma unroll
            for (int r = 0; r < 16; ++r) { SA[r] -= m; negm[r] = -m; }
            ZERO_P(); vA = kA;
            const int nfull = qtile < 7 ? qtile : 7;
            const bool far = qtile >= 8;
            int n = nfull + (far ? 1 : 0);
            f32x16 c1 = negm;
            if (far) apply_mask<2>(c1, r32, hi);
            lds_cptr p1 = n == 0 ? ab : (far ? ab - 8 * 4096 : ab - 4096);
            lds_cptr p2 = n <= 1 ? p1 : (far ? ab - 4096 : ab - 2 * 4096);
            kA.a = LDSV(p1); kA.b = LDSV(p1 + 1024);
            ATT_STEP(SA, SB, c1, qa0, qa1, oa, la, kA, kB, vA, vB, p2, ab, (void)0);
            while (n >= 2) {
                const lds_cptr p3 = n > 2 ? p2 - 4096 : p2;
                ATT_STEP(SB, SA, negm, qa0, qa1, oa, la, kB, kA, vB, vA, p3, p1, (void)0);
                const lds_cptr p4 = n > 3 ? p3 - 4096 : p3;
                ATT_STEP(SA, SB, negm, qa0, qa1, oa, la, kA, kB, vA, vB, p4, p2, (void)0);
                p1 = p3; p2 = p4; n -= 2;
            }
            if (n == 1) { ATT_STEP(SB, SA, negm, qa0, qa1, oa, la, kB, kA, vB, vA, p1, p1, (void)0); ATT_DRAIN(oa, la, vA); }
            else { ATT_DRAIN(oa, la, vB); }
            const unsigned oap = WS_O + (unsigned)(qtile * 64 + h) * 2048 + lane * 16;
            if (__all(finite_state(oa, la[0]))) {
                const float inv = __builtin_amdgcn_rcpf(la[0]);
                st_sc1(rs, oap, pack8(oa, 0, inv)); st_sc1(rs, oap + 1024, pack8(oa, 1, inv));
            } else {
                attn_slow(rs, WS_KV + (unsigned)h * (64 * 4096), qtile >= 8 ? qtile - 8 : 0, qtile, qtile, qtile - 8, qa0, qa1, oap, lane);
            }
        }
        LBAR();
        STAMP(7);
        P3_STAGE(0);
        COPY4(kvB + ((qb + 4) & 15) * 16384, lds0 + 0 + wid * 4096);
        WAIT_BAR(10);
        {
            int so0 = P2_B0, so1 = P2_B1, so2 = 0, so3 = 32768;
            lds_cptr cur = hb + so0;
#pragma unroll
            for (int r = 0; r < 16; ++r) negm[r] = -mB;
            kA.a = LDSV(cur); kA.b = LDSV(cur + 1024);
            SA = MFMA32(kA.a, qb0, negm); SA = MFMA32(kA.b, qb1, SA);
            kA.a = LDSV(cur + 4096); kA.b = LDSV(cur + 4096 + 1024);
            ZERO_P(); vA = kA;
            unsigned otouch = 0;
            const unsigned long long wo1 = (unsigned long long)(woA + p3off) - ((unsigned long long)12 << 18);
            for (int s = 2; s < 16; ++s) {
                const bool kv = s < 13;
                unsigned long long pk_ = (unsigned long long)kvB + (unsigned)(((qb + s + 3) & 15) << 14), pw_ = wo1 + ((unsigned long long)(unsigned)s << 18);
                asm volatile("" : "+s"(pk_), "+s"(pw_));
                const char* sb_ = (const char*)(kv ? pk_ : pw_);
                const unsigned sst = kv ? 1024 : 8 * 2048, sv = kv ? voff : p3vw, db_ = lds0 + so3 + wid * 4096;
                const lds_cptr nxt = hb + so1;
                if (__builtin_expect(s == 9 && wid == 0, 0)) wait16(ws + WS_FLAG2 + rb * 2048, tag, lane);
                if (__builtin_expect(s == 10 && wid < 4, 0)) otouch = __builtin_amdgcn_raw_buffer_load_b32(rs, (int)otoff, 0, 16);
                ATT_STEP(SA, SB, negm, qb0, qb1, ob, lb, kA, kB, vA, vB, cur + 2 * 4096, cur, glds16(sb_, sv, db_));
                ATT_STEP(SB, SA, negm, qb0, qb1, ob, lb, kB, kA, vB, vA, cur + 3 * 4096, cur + 1 * 4096, glds16(sb_ + sst, sv, db_ + 1024));
                ATT_STEP(SA, SB, negm, qb0, qb1, ob, lb, kA, kB, vA, vB, nxt, cur + 2 * 4096, glds16(sb_ + 2 * sst, sv, db_ + 2048));
                ATT_STEP(SB, SA, negm, qb0, qb1, ob, lb, kB, kA, vB, vA, nxt + 4096, cur + 3 * 4096, glds16(sb_ + 3 * sst, sv, db_ + 3072));
                cur = nxt; { const int t_ = so0; so0 = so1; so1 = so2; so2 = so3; so3 = t_; }
                WAIT_BAR_L(4);
                if (__builtin_expect(s == 2 && wid == 0, 0)) { if (lane == 0) __hip_atomic_store((gu64*)(ws + WS_FLAG2 + (qb * 16 + hp) * 128), tag, RLX_AGENT); }
            }
            asm volatile("" :: "v"(otouch));
            ATT_DRAIN(ob, lb, vA);
        }
        STAMP(8);
        const unsigned obp = WS_O + (unsigned)(qtile * 64 + 32 + h) * 2048 + lane * 16;
        if (__all(finite_state(ob, lb[0]))) {
            const float inv = __builtin_amdgcn_rcpf(lb[0]);
            st_sc1(rs, obp, pack8(ob, 0, inv)); st_sc1(rs, obp + 1024, pack8(ob, 1, inv));
        } else {
            attn_slow(rs, WS_KV + (unsigned)(32 + h) * (64 * 4096), 0, 63, -1, -1, qb0, qb1, obp, lane);
        }
    }

    {
        const int wt = wid & 3, kh = wid >> 2;
        WAIT_BAR_L(2);
        P3_STAGE(4);
        STAMP(9);
        int wa[4];
        { const int f = (2 * (g4 & 1) + (q4 >> 1)) << 1; const int row = 16 * (g4 >> 1) + 4 * (g4 & 1) + q4;
#pragma unroll
          for (int n = 0; n < 4; ++n) wa[n] = kh * 16384 + row * 128 + (((2 * n + (p4 >> 1)) ^ f) * 16) + (p4 & 1) * 8; }
        const unsigned osrc = WS_O + (unsigned)((rb * 4 + wt) * 64 + kh * 16) * 2048 + (g4 >> 1) * 1024 + ((g4 & 1) * 32 + i16) * 16;
        f32x4 accA[2][4], accB[2][4];
#pragma unroll
        for (int t = 0; t < 2; ++t)
#pragma unroll
            for (int n = 0; n < 4; ++n) { accA[t][n] = f32x4{}; accB[t][n] = f32x4{}; }
        u32x4 ofA[8], ofB[8];
#define P3_LDO(OF, c) do { _Pragma("unroll") for (int i = 0; i < 8; ++i) OF[i] = ld_sc1(rs, osrc + (((c) >> 2) * 32 + ((c) & 3) * 4 + (i >> 1)) * 2048 + (i & 1) * 256); } while (0)
#define P3_COMP(ACC, OF, c) do { const lds_cptr wp_ = L + P3_SLOT(c); \
            _Pragma("unroll") for (int hq = 0; hq < 4; ++hq) { \
                bf16x8 wf[4]; \
                _Pragma("unroll") for (int n = 0; n < 4; ++n) wf[n] = trfrag(wp_ + wa[n] + hq * 4096, 1024); \
                _Pragma("unroll") for (int n = 0; n < 4; ++n) { ACC[0][n] = MFMA16(wf[n], BC8(OF[2 * hq]), ACC[0][n]); ACC[1][n] = MFMA16(wf[n], BC8(OF[2 * hq + 1]), ACC[1][n]); } \
            } } while (0)
        P3_LDO(ofA, 0); P3_LDO(ofB, 1);
        const float g = __uint_as_float(__hip_atomic_load((gu32*)(ws + WS_G) + rb * 128 + wt * 32 + i16, RLX_AGENT));
        const float g2 = __uint_as_float(__hip_atomic_load((gu32*)(ws + WS_G) + rb * 128 + wt * 32 + 16 + i16, RLX_AGENT));
        P3_COMP(accA, ofA, 0); LBAR();
        if (tid == 0) __hip_atomic_store((gu64*)(ws + WS_FLAG3 + (qb * 16 + hp) * 128), tag, RLX_AGENT);
        P3_STAGE(5); P3_LDO(ofA, 2);
        P3_COMP(accA, ofB, 1); LBAR(); P3_STAGE(6); P3_LDO(ofB, 3);
        P3_COMP(accA, ofA, 2); LBAR(); P3_STAGE(7);
        if (wid == 0) wait16(ws + WS_FLAG3 + rb * 2048, tag, lane);
        P3_COMP(accA, ofB, 3); LBAR();
        STAMP(10);
        if (blockIdx.x == 0 && tid == 0) __hip_atomic_store((gu64*)(ws + WS_EPOCH), tag, RLX_AGENT);
        unsigned ot1 = 0;
        if (wid < 4) ot1 = __builtin_amdgcn_raw_buffer_load_b32(rs, (int)(otoff + 32 * 2048), 0, 16);
        P3_LDO(ofA, 4); P3_LDO(ofB, 5);
        P3_COMP(accB, ofA, 4); asm volatile("" :: "v"(ot1)); P3_LDO(ofA, 6);
        P3_COMP(accB, ofB, 5); P3_LDO(ofB, 7); LBAR();
        P3_COMP(accB, ofA, 6);
        P3_COMP(accB, ofB, 7);
#undef P3_LDO
#undef P3_COMP
        STAMP(11);
        LBAR();
        LAS f32x4* ex = (LAS f32x4*)(LW + wid * 4096) + lane;
        f32x4 mix[2][4];
#pragma unroll
        for (int t = 0; t < 2; ++t)
#pragma unroll
            for (int n = 0; n < 4; ++n) { const float gt = t ? g2 : g; mix[t][n] = accA[t][n] * gt + accB[t][n] * (1.0f - gt); }
#pragma unroll
        for (int t = 0; t < 2; ++t)
#pragma unroll
            for (int n = 0; n < 2; ++n) ex[(t * 2 + n) * 64] = kh ? mix[t][n] : mix[t][2 + n];
        LBAR();
        const LAS f32x4* ey = (const LAS f32x4*)(LW + (wid ^ 4) * 4096) + lane;
#pragma unroll
        for (int t = 0; t < 2; ++t)
#pragma unroll
            for (int n = 0; n < 2; ++n) {
                const f32x4 y = ey[(t * 2 + n) * 64] + (kh ? mix[t][2 + n] : mix[t][n]);
                unsigned short* op = out + (size_t)(rb * 128 + wt * 32 + t * 16 + i16) * DM + cb * 64 + (2 * kh + n) * 16 + 4 * g4;
                *(u32x2*)op = (u32x2){cvtpk(y[0], y[1]), cvtpk(y[2], y[3])};
            }
        STAMP(12);
    }
}

extern "C" void kernel_launch(void* const* d_in, const int* in_sizes, int n_in, void* d_out, int out_size, void* d_ws, size_t ws_size,
                              hipStream_t stream) {
    (void)hipFuncSetAttribute((const void*)fused_kernel, hipFuncAttributeMaxDynamicSharedMemorySize, LDS_BYTES);
    fused_kernel<<<256, 512, LDS_BYTES, stream>>>((const char*)d_in[0], (const char*)d_in[1], (const char*)d_in[2], (const char*)d_in[4], (const char*)d_in[5],
                                                  (const unsigned short*)d_in[6], (char*)d_ws, (unsigned short*)d_out);
}
```

```cpp
#include <hip/hip_runtime.h>
#include <hip/hip_bf16.h>
#include <stdint.h>

#define LAS __attribute__((address_space(3)))
typedef short bf16x8 __attribute__((ext_vector_type(8)));
typedef short s16x4 __attribute__((ext_vector_type(4)));
typedef float f32x16 __attribute__((ext_vector_type(16)));
typedef float f32x4 __attribute__((ext_vector_type(4)));
typedef float f32x2 __attribute__((ext_vector_type(2)));
typedef unsigned u32x4 __attribute__((ext_vector_type(4)));
typedef unsigned u32x2 __attribute__((ext_vector_type(2)));
typedef __bf16 bf16x2_t __attribute__((ext_vector_type(2)));
typedef LAS const char* lds_cptr;
typedef LAS char* lds_ptr;
typedef __attribute__((address_space(1))) unsigned long long gu64;
typedef __attribute__((address_space(1))) unsigned gu32;

#define MFMA32(a, b, c) __builtin_amdgcn_mfma_f32_32x32x16_bf16(a, b, c, 0, 0, 0)
#define MFMA16(a, b, c) __builtin_amdgcn_mfma_f32_16x16x32_bf16(a, b, c, 0, 0, 0)
#define SBAR() __builtin_amdgcn_sched_barrier(0)
#define PIN(x) asm volatile("" : "+v"(x))
#define WAIT_BAR(N) asm volatile("s_waitcnt vmcnt(" #N ")\n\ts_barrier" ::: "memory")
#define WAIT_BAR_L(N) asm volatile("s_waitcnt vmcnt(" #N ") lgkmcnt(0)\n\ts_barrier" ::: "memory")
#define LBAR() asm volatile("s_waitcnt lgkmcnt(0)\n\ts_barrier" ::: "memory")
#define BAR() asm volatile("s_barrier" ::: "memory")
#define RLX_AGENT __ATOMIC_RELAXED, __HIP_MEMORY_SCOPE_AGENT

constexpr int DM = 1024;
constexpr float QSCALE = 0.17677669529663687f * 1.4426950408889634f;

constexpr unsigned WS_KV = 0;
constexpr unsigned WS_O = 16u << 20;
constexpr unsigned WS_G = 24u << 20;
constexpr unsigned WS_EPOCH = WS_G + 65536;
constexpr unsigned WS_FLAG1 = WS_EPOCH + 4096;
constexpr unsigned WS_FLAG2 = WS_FLAG1 + 32768;
constexpr unsigned WS_FLAG3 = WS_FLAG2 + 32768;
constexpr unsigned WS_USED = 32u << 20;
constexpr int LDS_BYTES = 163840;

#define STAMP(i) do {} while (0)

__device__ __forceinline__ unsigned cvtpk(float lo, float hi) {
    f32x2 v = {lo, hi}; bf16x2_t b = __builtin_convertvector(v, bf16x2_t); return __builtin_bit_cast(unsigned, b);
}
__device__ __forceinline__ void glds16(const void* sbase, unsigned voff, unsigned lds_dst) {
    asm volatile("s_mov_b32 m0, %2\n\ts_nop 0\n\tglobal_load_lds_dwordx4 %0, %1" : : "v"(voff), "s"(sbase), "s"(lds_dst) : "memory");
}
__device__ __forceinline__ void touch_lines64(const void* vaddr, unsigned lds_dst) {
    asm volatile("s_mov_b32 m0, %1\n\ts_nop 0\n\tglobal_load_lds_dword %0, off" : : "v"(vaddr), "s"(lds_dst) : "memory");
}
__device__ __forceinline__ void st_sc1(__amdgpu_buffer_rsrc_t rs, unsigned off, u32x4 v) { __builtin_amdgcn_raw_buffer_store_b128(v, rs, (int)off, 0, 16); }
#define LD_AUX 16
__device__ __forceinline__ u32x4 ld_sc1(__amdgpu_buffer_rsrc_t rs, unsigned off) { return __builtin_amdgcn_raw_buffer_load_b128(rs, (int)off, 0, LD_AUX); }
__device__ __forceinline__ void wait16(const char* base, unsigned long long tag, int lane) {
    gu64* f = (gu64*)(base + (lane & 15) * 128);
    for (unsigned spins = 0; spins < (1u << 18); ++spins) {
        const unsigned long long v = __hip_atomic_load(f, RLX_AGENT);
        if (__all(v == tag)) break;
        __builtin_amdgcn_s_sleep(1);
    }
}
__device__ __forceinline__ s16x4 trread(lds_cptr p) {
    typedef short v4i16_t __attribute__((ext_vector_type(4)));
    return __builtin_bit_cast(s16x4, __builtin_amdgcn_ds_read_tr16_b64_v4i16((LAS v4i16_t*)p));
}
__device__ __forceinline__ bf16x8 trfrag(lds_cptr p, int second_off) {
    const s16x4 lo = trread(p), hi = trread(p + second_off);
    return (bf16x8){lo[0], lo[1], lo[2], lo[3], hi[0], hi[1], hi[2], hi[3]};
}
__device__ __forceinline__ u32x4 pack8(const f32x16& a, int s, float sc) {
    u32x4 r;
    r[0] = cvtpk(a[8 * s + 0] * sc, a[8 * s + 1] * sc); r[1] = cvtpk(a[8 * s + 2] * sc, a[8 * s + 3] * sc);
    r[2] = cvtpk(a[8 * s + 4] * sc, a[8 * s + 5] * sc); r[3] = cvtpk(a[8 * s + 6] * sc, a[8 * s + 7] * sc);
    return r;
}
__device__ __forceinline__ u32x4 pack8n(const f32x16& a, int s) {
    u32x4 r;
    r[0] = cvtpk(a[8 * s + 0], a[8 * s + 1]); r[1] = cvtpk(a[8 * s + 2], a[8 * s + 3]);
    r[2] = cvtpk(a[8 * s + 4], a[8 * s + 5]); r[3] = cvtpk(a[8 * s + 6], a[8 * s + 7]);
    return r;
}
__device__ __forceinline__ u32x4 pack44(const f32x4& a, const f32x4& b, float sc) {
    return (u32x4){cvtpk(a[0] * sc, a[1] * sc), cvtpk(a[2] * sc, a[3] * sc), cvtpk(b[0] * sc, b[1] * sc), cvtpk(b[2] * sc, b[3] * sc)};
}
__device__ __forceinline__ u32x4 pack44n(const f32x4& a, const f32x4& b) {
    return (u32x4){cvtpk(a[0], a[1]), cvtpk(a[2], a[3]), cvtpk(b[0], b[1]), cvtpk(b[2], b[3])};
}
#define EXP2(v) __builtin_amdgcn_exp2f(v)
#define LDSV(p) (*(const LAS bf16x8*)(p))
#define LDSW(p) (*(LAS u32x4*)(p))
#define BC8(v) __builtin_bit_cast(bf16x8, v)

__device__ __forceinline__ float halfmax(float m) {
    auto rr = __builtin_amdgcn_permlane32_swap(__float_as_uint(m), __float_as_uint(m), false, false);
    return __builtin_fmaxf(__uint_as_float(rr[0]), __uint_as_float(rr[1]));
}
__device__ __forceinline__ float rowmax16(const f32x16& p) {
    float a = __builtin_fmaxf(p[0], p[1]);
#pragma unroll
    for (int r = 2; r < 16; ++r) a = __builtin_fmaxf(a, p[r]);
    return halfmax(a);
}
template <int MASK> __device__ __forceinline__ void apply_mask(f32x16& s, int r32, int hi) {
#pragma unroll
    for (int r = 0; r < 16; ++r) {
        const int kl = (r & 3) + 8 * (r >> 2) + 4 * hi;
        const bool keep = (MASK == 1) ? (kl <= r32) : (kl > r32);
        if (!keep) s[r] = -INFINITY;
    }
}
__device__ __forceinline__ void attn_slow(__amdgpu_buffer_rsrc_t rs, unsigned kvh, int t_first, int t_last, int t_diag, int t_far, bf16x8 q0, bf16x8 q1, unsigned odst, int lane) {
    const int r32 = lane & 31, hi = lane >> 5;
    f32x16 o = f32x16{}; float m = -INFINITY, l = 0.f;
    for (int t = t_last; t >= t_first; --t) {
        const unsigned p = kvh + (unsigned)t * 4096 + lane * 16;
        const bf16x8 k0 = BC8(ld_sc1(rs, p)), k1 = BC8(ld_sc1(rs, p + 1024)), v0 = BC8(ld_sc1(rs, p + 2048)), v1 = BC8(ld_sc1(rs, p + 3072));
        f32x16 s = MFMA32(k0, q0, f32x16{}); s = MFMA32(k1, q1, s);
        if (t == t_diag) apply_mask<1>(s, r32, hi);
        if (t == t_far) apply_mask<2>(s, r32, hi);
        const float mn = __builtin_fmaxf(m, rowmax16(s));
        const float ms = (mn == -INFINITY) ? 0.f : mn;
        const float alpha = EXP2(m - ms);
        float rs_ = 0.f;
#pragma unroll
        for (int r = 0; r < 16; ++r) { s[r] = EXP2(s[r] - ms); o[r] *= alpha; }
        const u32x4 p0 = pack8n(s, 0), p1 = pack8n(s, 1);
#pragma unroll
        for (int i = 0; i < 4; ++i) rs_ += __uint_as_float(p0[i] << 16) + __uint_as_float(p0[i] & 0xffff0000u) + __uint_as_float(p1[i] << 16) + __uint_as_float(p1[i] & 0xffff0000u);
        l = l * alpha + rs_; m = mn;
        o = MFMA32(v0, BC8(p0), o); o = MFMA32(v1, BC8(p1), o);
    }
    auto rr = __builtin_amdgcn_permlane32_swap(__float_as_uint(l), __float_as_uint(l), false, false);
    l = __uint_as_float(rr[0]) + __uint_as_float(rr[1]);
    const float inv = 1.0f / l;
    st_sc1(rs, odst, pack8(o, 0, inv)); st_sc1(rs, odst + 1024, pack8(o, 1, inv));
}
__device__ __forceinline__ bool finite_state(const f32x16& o, float l) {
    float a = 0.f, b = 0.f;
#pragma unroll
    for (int r = 0; r < 16; r += 2) { a = __builtin_fmaf(o[r], 0.f, a); b = __builtin_fmaf(o[r + 1], 0.f, b); }
    return (l < 1.0e30f) && (a + b == 0.f);
}
struct Fr2 { bf16x8 a, b; };
#define EX4(S, i) do { S[i] = EXP2(S[i]); S[(i) + 1] = EXP2(S[(i) + 1]); S[(i) + 2] = EXP2(S[(i) + 2]); S[(i) + 3] = EXP2(S[(i) + 3]); PIN(S); } while (0)
#define PK4(W, S, i) do { W[0] = cvtpk(S[i], S[(i) + 1]); W[1] = cvtpk(S[(i) + 2], S[(i) + 3]); W[2] = cvtpk(S[(i) + 4], S[(i) + 5]); W[3] = cvtpk(S[(i) + 6], S[(i) + 7]); PIN(W); } while (0)
#define ATT_STEP(SC, SN, CN, Q0, Q1, O, LS, KC, KN, VC, VN, KP, VP, G0) do { \
        SBAR(); \
        SN = MFMA32(KC.a, Q0, CN); PIN(SN); SBAR(); \
        KN.a = LDSV(KP); KN.b = LDSV((KP) + 1024); EX4(SC, 0); SBAR(); \
        O = MFMA32(VC.a, BC8(pw0), O); PIN(O); SBAR(); \
        VN.a = LDSV((VP) + 2048); VN.b = LDSV((VP) + 3072); EX4(SC, 4); SBAR(); \
        SN = MFMA32(KC.b, Q1, SN); PIN(SN); SBAR(); \
        EX4(SC, 8); SBAR(); \
        LS = MFMA16(sel, BC8(pw0), LS); PIN(LS); SBAR(); \
        PK4(pw0, SC, 0); SBAR(); \
        O = MFMA32(VC.b, BC8(pw1), O); PIN(O); SBAR(); \
        EX4(SC, 12); SBAR(); \
        LS = MFMA16(sel, BC8(pw1), LS); PIN(LS); SBAR(); \
        G0; PK4(pw1, SC, 8); SBAR(); \
    } while (0)
#define ATT_DRAIN(O, LS, VC) do { \
        O = MFMA32(VC.a, BC8(pw0), O); O = MFMA32(VC.b, BC8(pw1), O); \
        LS = MFMA16(sel, BC8(pw0), LS); LS = MFMA16(sel, BC8(pw1), LS); \
    } while (0)
#define ZERO_P() do { pw0 = u32x4{}; pw1 = u32x4{}; } while (0)

constexpr int CODE_LINES = 288;
#define P3BR(c) ((c) >> 2)
constexpr int P1A_SLOT = 49152, P1A_DUMMY = 147456;
constexpr int P1B_SLOT = 32768;
constexpr int P2_B0 = 131072, P2_B1 = 98304, P2_WO0 = 65536;
struct FragsA { bf16x8 x[4]; s16x4 wl[4], wh[4]; };
struct FragsB { bf16x8 x[2]; s16x4 wl[4], wh[4]; };
#define WFR(F, n) (bf16x8){F.wl[n][0], F.wl[n][1], F.wl[n][2], F.wl[n][3], F.wh[n][0], F.wh[n][1], F.wh[n][2], F.wh[n][3]}

__global__ __launch_bounds__(512, 2) void fused_kernel(const char* __restrict__ x, const char* __restrict__ wA, const char* __restrict__ woA,
                                                       const char* __restrict__ wB, const char* __restrict__ woB,
                                                       const unsigned short* __restrict__ gate_w, char* __restrict__ ws, unsigned short* __restrict__ out) {
    extern __shared__ __attribute__((aligned(16))) char lds[];
    const int tid = threadIdx.x, lane = tid & 63;
    const int wid = __builtin_amdgcn_readfirstlane(tid >> 6);
    const int xcd = blockIdx.x & 7, jj = blockIdx.x >> 3;
    const int hp = xcd * 2 + (jj & 1), qb = jj >> 1;
    const unsigned lds0 = (unsigned)(uintptr_t)lds;
    const lds_cptr L = (lds_cptr)lds;
    const lds_ptr LW = (lds_ptr)lds;
    const __amdgpu_buffer_rsrc_t rs = __builtin_amdgcn_make_buffer_rsrc((void*)ws, 0, (int)WS_USED, 0x00020000);
    const int i16 = lane & 15, g4 = lane >> 4, q4 = (lane >> 2) & 3, p4 = lane & 3;
    const int r32 = lane & 31, hi = lane >> 5;
    STAMP(0);
    unsigned long long tag;

    const char* xs = x + (size_t)qb * 128 * 2048;
    unsigned vx[2];
#pragma unroll
    for (int p = 0; p < 2; ++p) {
        const int row = 16 * wid + 8 * p + (lane >> 3);
        const int sw = ((row >> 1) & 3) | (((row >> 4) & 1) << 2);
        vx[p] = (unsigned)(row * 2048 + (((lane & 7) ^ sw) * 16));
    }
    const unsigned xdst = lds0 + wid * 2048;
    unsigned vw[2];
#pragma unroll
    for (int e = 0; e < 2; ++e) vw[e] = (unsigned)((lane >> 3) * 6144 + hp * 128 + (((lane & 7) ^ ((((lane >> 4) & 1) << 1) | (e << 2))) * 16));
    const int wf_f = (((q4 >> 1) & 1) << 1) | ((g4 & 1) << 2);

    u32x4 kvs[8]; unsigned kvg0; int kvoK, kvoV;
    const char* wqsrc = (wid < 4 ? wA : wB) + (size_t)((wid & 3) * 16) * 6144;
    const unsigned wqdst = lds0 + 16384 + wid * 2048;
#define P1B_X(T, slot, p) glds16(xs + (15 - (T)) * 128, vx[p], xdst + (slot) + (p) * 1024)
#define P1B_W(T, slot, i) glds16(wqsrc + (size_t)((15 - (T)) * 64 + (i) * 8) * 6144, vw[i], wqdst + (slot) + (i) * 1024)
    {
        const int wm = wid >> 2, wn = wid & 3, br = wn >> 1, hh = wn & 1;
        const int gtok = qb * 128 + hp * 8 + wid;
        u32x4 ga0, ga1, gg0, gg1;
        { const char* xr = x + (size_t)gtok * 2048; const unsigned go = lane * 32;
          asm volatile("global_load_dwordx4 %0, %4, %5\n\tglobal_load_dwordx4 %1, %4, %5 offset:16\n\tglobal_load_dwordx4 %2, %4, %6\n\tglobal_load_dwordx4 %3, %4, %6 offset:16"
                       : "=&v"(ga0), "=&v"(ga1), "=&v"(gg0), "=&v"(gg1) : "v"(go), "s"(xr), "s"(gate_w) : "memory"); }
        const char* wsrc = (wid < 4 ? wA : wB) + (size_t)((wid & 1) * 32) * 6144 + (1 + ((wid >> 1) & 1)) * 2048;
        const unsigned wdst = lds0 + 16384 + wid * 4096;
#define P1A_X(T, slot, p) glds16(xs + (T) * 128, vx[p], xdst + (slot) + (p) * 1024)
#define P1A_W(T, slot, i) glds16(wsrc + (size_t)((T) * 64 + (i) * 8) * 6144, vw[(i) & 1], wdst + (slot) + (i) * 1024)
        const char* pfb; unsigned pfstep;
        { const int gw = blockIdx.x * 8 + wid;
          if (lane == 0) { pfb = x + (size_t)gw * 2048; pfstep = 128; }
          else if (lane < 3) { const int id = gw * 2 + lane - 1, rem = id & 2047;
                 pfb = ((id >> 11) ? wB : wA) + (size_t)((rem >> 5) * 6144 + (1 + ((rem >> 4) & 1)) * 2048 + (rem & 15) * 128); pfstep = 64 * 6144; }
          else { const int rho = gw * 2 + (lane & 1);
                 if (rho < 2048) pfb = ((rho >> 10) ? wB : wA) + (size_t)(rho & 1023) * 6144; else pfb = (((rho - 2048) >> 10) ? woB : woA) + (size_t)(rho & 1023) * 2048;
                 pfstep = 128; } }
#define P1A_PREF(T, F) do { if (lane < 5) touch_lines64(pfb + (size_t)(lane < 3 ? (T) : (F)) * pfstep, lds0 + P1A_DUMMY); } while (0)
        P1A_X(0, 0, 0); P1A_X(0, 0, 1); P1A_W(0, 0, 0); P1A_W(0, 0, 1); P1A_W(0, 0, 2); P1A_W(0, 0, 3);
        P1A_X(1, P1A_SLOT, 0); P1A_W(1, P1A_SLOT, 0); P1A_W(1, P1A_SLOT, 1);
        P1A_X(1, P1A_SLOT, 1); P1A_W(1, P1A_SLOT, 2); P1A_W(1, P1A_SLOT, 3);
        {
            unsigned long long pc; asm volatile("s_getpc_b64 %0" : "=s"(pc));
            const int id = jj * 8 + wid + (lane & 1) * 256;
            if (lane < 2) touch_lines64((const char*)pc + (size_t)(id < CODE_LINES ? id : 0) * 128, lds0 + P1A_DUMMY);
        }
        P1A_PREF(2, 12); P1A_PREF(3, 13); P1A_PREF(4, 14); P1A_PREF(5, 15);
        P1A_X(2, 2 * P1A_SLOT, 0); P1A_W(2, 2 * P1A_SLOT, 0); P1A_W(2, 2 * P1A_SLOT, 1);
        asm volatile("s_load_dwordx2 %0, %1, 0x0\n\ts_waitcnt lgkmcnt(0)" : "=s"(tag) : "s"(ws + WS_EPOCH) : "memory");
        tag += 1;
        asm volatile("s_waitcnt vmcnt(20)" : "+v"(ga0), "+v"(ga1), "+v"(gg0), "+v"(gg1) :: "memory");
        {
            float s = 0.f;
#pragma unroll
            for (int i = 0; i < 4; ++i) {
                s += __uint_as_float(ga0[i] << 16) * __uint_as_float(gg0[i] << 16) + __uint_as_float(ga0[i] & 0xffff0000u) * __uint_as_float(gg0[i] & 0xffff0000u);
                s += __uint_as_float(ga1[i] << 16) * __uint_as_float(gg1[i] << 16) + __uint_as_float(ga1[i] & 0xffff0000u) * __uint_as_float(gg1[i] & 0xffff0000u);
            }
#pragma unroll
            for (int o = 32; o > 0; o >>= 1) s += __shfl_xor(s, o);
            if (lane == 0) __hip_atomic_store((gu32*)(ws + WS_G) + gtok, __float_as_uint(1.0f / (1.0f + __expf(-s))), RLX_AGENT);
        }
        int xa[2];
        { const int row = wm * 64 + 16 * (i16 >> 3) + (i16 & 7); const int sw = ((row >> 1) & 3) | (((row >> 4) & 1) << 2);
          xa[0] = row * 128 + ((g4 ^ sw) * 16); xa[1] = row * 128 + (((4 + g4) ^ sw) * 16); }
        int wa[2];
#pragma unroll
        for (int np = 0; np < 2; ++np) wa[np] = 16384 + br * 16384 + (8 * g4 + q4) * 128 + (((hh * 4 + np * 2 + (p4 >> 1)) ^ wf_f) * 16) + (p4 & 1) * 8;
        f32x4 acc[4][4];
#pragma unroll
        for (int m = 0; m < 4; ++m)
#pragma unroll
            for (int n = 0; n < 4; ++n) acc[m][n] = f32x4{};
#define A_LX(F, m, sl, kh) F.x[m] = LDSV(L + (sl) + xa[kh] + ((m) >> 1) * 4096 + ((m) & 1) * 1024)
#define A_LWL(F, n, sl, kh) F.wl[n] = trread(L + (sl) + wa[(n) & 1] + ((n) >> 1) * 8192 + (kh) * 4096)
#define A_LWH(F, n, sl, kh) F.wh[n] = trread(L + (sl) + wa[(n) & 1] + ((n) >> 1) * 8192 + (kh) * 4096 + 512)
#define A_M(F, m, n) do { acc[m][n] = ((n) < 2) ? MFMA16(WFR(F, n), F.x[m], acc[m][n]) : MFMA16(F.x[m], WFR(F, n), acc[m][n]); } while (0)
#define A_HALF(FC, FN, LD, sl, kh, G0, G1, G2) do { \
            A_M(FC, 0, 0); if (LD) A_LX(FN, 0, sl, kh); SBAR(); A_M(FC, 1, 0); if (LD) A_LWL(FN, 0, sl, kh); SBAR(); \
            A_M(FC, 2, 0); if (LD) A_LWH(FN, 0, sl, kh); SBAR(); A_M(FC, 3, 0); G0; SBAR(); \
            A_M(FC, 0, 1); if (LD) A_LX(FN, 1, sl, kh); SBAR(); A_M(FC, 1, 1); if (LD) A_LWL(FN, 1, sl, kh); SBAR(); \
            A_M(FC, 2, 1); if (LD) A_LWH(FN, 1, sl, kh); SBAR(); A_M(FC, 3, 1); G1; SBAR(); \
            A_M(FC, 0, 2); if (LD) A_LX(FN, 2, sl, kh); SBAR(); A_M(FC, 1, 2); if (LD) A_LWL(FN, 2, sl, kh); SBAR(); \
            A_M(FC, 2, 2); if (LD) A_LWH(FN, 2, sl, kh); SBAR(); A_M(FC, 3, 2); G2; SBAR(); \
            A_M(FC, 0, 3); if (LD) A_LX(FN, 3, sl, kh); SBAR(); A_M(FC, 1, 3); if (LD) A_LWL(FN, 3, sl, kh); SBAR(); \
            A_M(FC, 2, 3); if (LD) A_LWH(FN, 3, sl, kh); SBAR(); A_M(FC, 3, 3); SBAR(); \
        } while (0)
        WAIT_BAR(15);
        STAMP(1);
        FragsA FA, FB;
#pragma unroll
        for (int m = 0; m < 4; ++m) { A_LX(FA, m, 0, 0); A_LWL(FA, m, 0, 0); A_LWH(FA, m, 0, 0); }
        int s0 = 0, s1 = P1A_SLOT, s2 = 2 * P1A_SLOT;
#define A_ROT() do { const int tmp = s0; s0 = s1; s1 = s2; s2 = tmp; } while (0)
        for (int t = 0; t < 13; ++t) {
            A_HALF(FA, FB, true, s0, 1, P1A_X(t + 2, s2, 1), P1A_W(t + 2, s2, 2), P1A_W(t + 2, s2, 3));
            P1A_PREF(t + 6 < 16 ? t + 6 : 15, t);
            WAIT_BAR_L(8);
            A_HALF(FB, FA, true, s1, 0, P1A_X(t + 3, s0, 0), P1A_W(t + 3, s0, 0), P1A_W(t + 3, s0, 1));
            A_ROT();
        }
        A_HALF(FA, FB, true, s0, 1, P1A_X(15, s2, 1), P1A_W(15, s2, 2), P1A_W(15, s2, 3));
        WAIT_BAR_L(6);
        A_HALF(FB, FA, true, s1, 0, P1B_X(0, 65536, 0), P1B_W(0, 65536, 0), { P1B_X(0, 65536, 1); P1B_W(0, 65536, 1); });
        A_ROT();
        A_HALF(FA, FB, true, s0, 1, (void)0, (void)0, (void)0);
        WAIT_BAR_L(4);
        A_HALF(FB, FA, true, s1, 0, P1B_X(1, 98304, 0), P1B_W(1, 98304, 0), { P1B_X(1, 98304, 1); P1B_W(1, 98304, 1); });
        A_ROT();
#undef A_ROT
        A_HALF(FA, FB, true, s0, 1, (void)0, (void)0, (void)0);
        A_HALF(FB, FA, false, 0, 0, (void)0, (void)0, (void)0);
#undef A_HALF
#undef A_M
#undef A_LX
#undef A_LWL
#undef A_LWH
#undef P1A_X
#undef P1A_W
        const int h = hp * 2 + hh;
        const int so = (g4 >> 1) * 1024 + (g4 & 1) * 512;
        kvg0 = WS_KV + (unsigned)((br * 32 + h) * 64 + qb * 4 + wm * 2) * 4096;
        kvoK = so + (16 * (i16 >> 3) + (i16 & 7)) * 16; kvoV = 2048 + so + i16 * 16;
#pragma unroll
        for (int T = 0; T < 2; ++T) {
#pragma unroll
            for (int mp = 0; mp < 2; ++mp) kvs[4 * T + mp] = pack44n(acc[2 * T + mp][0], acc[2 * T + mp][1]);
#pragma unroll
            for (int np = 0; np < 2; ++np) kvs[4 * T + 2 + np] = pack44n(acc[2 * T][2 + np], acc[2 * T + 1][2 + np]);
        }
        STAMP(2);
    }
#define KV_ST(i) st_sc1(rs, kvg0 + ((i) >> 2) * 4096 + (((i) & 2) ? kvoV + ((i) & 1) * 256 : kvoK + ((i) & 1) * 128), kvs[i])

    const int hh = wid >> 2, tt = wid & 3, h = hp * 2 + hh;
    const int qtile = qb * 4 + tt;
    const char* kvA = ws + WS_KV + (size_t)(h * 64 + tt) * 4096;
    const char* kvB = kvA + (size_t)32 * 64 * 4096;
    const unsigned voff = lane * 16;
#define COPY4(src, dst) do { glds16((src), voff, (dst)); glds16((src) + 1024, voff, (dst) + 1024); glds16((src) + 2048, voff, (dst) + 2048); glds16((src) + 3072, voff, (dst) + 3072); } while (0)
    bf16x8 qa0, qa1, qb0, qb1;
    {
        WAIT_BAR_L(4);
        P1B_X(2, 0, 0); P1B_W(2, 0, 0); P1B_X(2, 0, 1); P1B_W(2, 0, 1);
        P1B_X(3, 32768, 0); P1B_W(3, 32768, 0);
        int xa[2];
        { const int row = tt * 32 + 16 * (i16 >> 3) + (i16 & 7); const int sw = ((row >> 1) & 3) | (((row >> 4) & 1) << 2);
          xa[0] = row * 128 + ((g4 ^ sw) * 16); xa[1] = row * 128 + (((4 + g4) ^ sw) * 16); }
        int wa[2];
#pragma unroll
        for (int np = 0; np < 2; ++np) wa[np] = 16384 + (8 * g4 + q4) * 128 + (((hh * 4 + np * 2 + (p4 >> 1)) ^ wf_f) * 16) + (p4 & 1) * 8;
        f32x4 acc[2][4];
#pragma unroll
        for (int m = 0; m < 2; ++m)
#pragma unroll
            for (int n = 0; n < 4; ++n) acc[m][n] = f32x4{};
#define B_LX(F, m, sl, kh) F.x[m] = LDSV(L + (sl) + xa[kh] + (m) * 1024)
#define B_LWL(F, n, sl, kh) F.wl[n] = trread(L + (sl) + wa[(n) & 1] + ((n) >> 1) * 8192 + (kh) * 4096)
#define B_LWH(F, n, sl, kh) F.wh[n] = trread(L + (sl) + wa[(n) & 1] + ((n) >> 1) * 8192 + (kh) * 4096 + 512)
#define B_M(F, m, n) do { acc[m][n] = MFMA16(WFR(F, n), F.x[m], acc[m][n]); } while (0)
#define B_HALF(FC, FN, LD, sl, kh, G0, G1) do { \
            B_M(FC, 0, 0); if (LD) { B_LX(FN, 0, sl, kh); B_LWL(FN, 0, sl, kh); } SBAR(); B_M(FC, 1, 0); if (LD) { B_LWH(FN, 0, sl, kh); } SBAR(); \
            B_M(FC, 0, 1); if (LD) { B_LX(FN, 1, sl, kh); B_LWL(FN, 1, sl, kh); } SBAR(); B_M(FC, 1, 1); if (LD) { B_LWH(FN, 1, sl, kh); } G0; SBAR(); \
            B_M(FC, 0, 2); if (LD) { B_LWL(FN, 2, sl, kh); B_LWH(FN, 2, sl, kh); } SBAR(); B_M(FC, 1, 2); G1; SBAR(); \
            B_M(FC, 0, 3); if (LD) { B_LWL(FN, 3, sl, kh); B_LWH(FN, 3, sl, kh); } SBAR(); B_M(FC, 1, 3); SBAR(); \
        } while (0)
        STAMP(3);
        FragsB FA, FB;
#pragma unroll
        for (int m = 0; m < 2; ++m) B_LX(FA, m, 65536, 0);
#pragma unroll
        for (int n = 0; n < 4; ++n) { B_LWL(FA, n, 65536, 0); B_LWH(FA, n, 65536, 0); }
        int s0 = 65536, s1 = 98304, s2 = 0, s3 = 32768;
#define P1B_IT(u, N, PRE, POST) do { \
            PRE; \
            B_HALF(FA, FB, true, s0, 1, if ((u) + 3 < 16) P1B_X((u) + 3, s3, 1), if ((u) + 3 < 16) P1B_W((u) + 3, s3, 1)); \
            if ((u) < 8) { KV_ST((u) & 7); } \
            WAIT_BAR_L(N); \
            POST; \
            B_HALF(FB, FA, true, s1, 0, if ((u) + 4 < 16) P1B_X((u) + 4, s0, 0), if ((u) + 4 < 16) P1B_W((u) + 4, s0, 0)); \
            { const int tmp = s0; s0 = s1; s1 = s2; s2 = s3; s3 = tmp; } \
        } while (0)
        P1B_IT(0, 9, (void)0, (void)0); P1B_IT(1, 10, (void)0, (void)0);
        P1B_IT(2, 11, (void)0, (void)0); P1B_IT(3, 11, (void)0, (void)0); P1B_IT(4, 11, (void)0, (void)0);
        P1B_IT(5, 11, (void)0, (void)0); P1B_IT(6, 11, (void)0, (void)0); P1B_IT(7, 11, (void)0, (void)0);
        P1B_IT(8, 10, (void)0, (void)0); P1B_IT(9, 9, (void)0, (void)0);
        P1B_IT(10, 8, (void)0, if (tid == 0) __hip_atomic_store((gu64*)(ws + WS_FLAG1 + (hp * 16 + qb) * 128), tag, RLX_AGENT));
        P1B_IT(11, 12, COPY4(kvB + qb * 16384, lds0 + P2_B0 + wid * 4096), (void)0);
        P1B_IT(12, 12, (void)0, (void)0);
        P1B_IT(13, 4, (void)0, if (wid == 0) { wait16(ws + WS_FLAG1 + hp * 2048, tag, lane); __builtin_amdgcn_fence(__ATOMIC_ACQUIRE, "agent"); });
        P1B_IT(14, 0, (void)0, (void)0);
#undef P1B_IT
        B_HALF(FA, FB, true, s0, 1, (void)0, (void)0);
        B_HALF(FB, FA, false, 0, 0, (void)0, (void)0);
#undef B_HALF
#undef B_M
#undef B_LX
#undef B_LWL
#undef B_LWH
#undef P1B_X
#undef P1B_W
        STAMP(4);
        LBAR();
        const lds_ptr scr = LW + hh * 49152 + (8 + tt) * 4096;
        const int so = (g4 >> 1) * 1024 + (g4 & 1) * 512 + (16 * (i16 >> 3) + (i16 & 7)) * 16;
#pragma unroll
        for (int b = 0; b < 2; ++b)
#pragma unroll
            for (int mp = 0; mp < 2; ++mp) LDSW(scr + b * 2048 + so + mp * 128) = pack44(acc[mp][2 * b], acc[mp][2 * b + 1], QSCALE);
        asm volatile("s_waitcnt lgkmcnt(0)" ::: "memory");
        qa0 = LDSV(scr + lane * 16); qa1 = LDSV(scr + 1024 + lane * 16); qb0 = LDSV(scr + 2048 + lane * 16); qb1 = LDSV(scr + 3072 + lane * 16);
        asm volatile("s_waitcnt lgkmcnt(0)" : "+v"(qa0), "+v"(qa1), "+v"(qb0), "+v"(qb1) :: "memory");
        COPY4(kvB + ((qb + 1) & 15) * 16384, lds0 + P2_B1 + wid * 4096);
    }

    const int rb = 4 * (xcd >> 1) + (jj & 3), cb = 8 * (xcd & 1) + (jj >> 2);
    const size_t p3off = (size_t)((wid >> 2) * 512 + (wid & 3) * 32) * 2048 + cb * 128;
    const unsigned p3vw = (unsigned)((lane >> 3) * 2048 + (((lane & 7) ^ (((lane >> 4) & 3) << 1)) * 16));
    const unsigned osrc = WS_O + (unsigned)((rb * 4 + (wid & 3)) * 64 + (wid >> 2) * 16) * 2048 + (g4 >> 1) * 1024 + ((g4 & 1) * 32 + i16) * 16;
    u32x4 o0[8], o1[8], o2[8]; float g, g2;
#define P3_LDO(OF, c) do { _Pragma("unroll") for (int i = 0; i < 8; ++i) OF[i] = ld_sc1(rs, osrc + (P3BR(c) * 32 + ((c) & 3) * 4 + (i >> 1)) * 2048 + (i & 1) * 256); } while (0)
#define P3_SLOT(c) (((c) % 5) == 0 ? 65536 : ((c) % 5) == 1 ? 0 : ((c) % 5) == 2 ? 32768 : ((c) % 5) == 3 ? 131072 : 98304)
#define P3_STAGE(c) do { const char* src_ = (((c) >> 2) ? woB : woA) + p3off + (size_t)((c) & 3) * (128 * 2048); const unsigned dst_ = lds0 + P3_SLOT(c) + wid * 4096; \
        glds16(src_, p3vw, dst_); glds16(src_ + 8 * 2048, p3vw, dst_ + 1024); glds16(src_ + 16 * 2048, p3vw, dst_ + 2048); glds16(src_ + 24 * 2048, p3vw, dst_ + 3072); } while (0)
    {
        bf16x8 sel;
        { const short one = (((lane >> 4) & 1) == ((lane >> 2) & 1)) ? (short)0x3F80 : (short)0;
#pragma unroll
          for (int i = 0; i < 8; ++i) sel[i] = one; }
        f32x16 SA, SB, negm, oa = f32x16{}, ob = f32x16{};
        f32x4 la = f32x4{}, lb = f32x4{};
        Fr2 kA, kB, vA, vB; u32x4 pw0, pw1;
        const lds_cptr hb = L + hh * 16384 + lane * 16;
        float mB;
        STAMP(5);
        {
            const lds_cptr cur = hb + P2_B0, nxt = hb + P2_B1;
            const char* a0s = kvA + qb * 16384; const char* a1s = kvA + (qb >= 1 ? qb - 1 : qb) * 16384; const char* a2s = kvA + (qb >= 2 ? qb - 2 : qb) * 16384;
            const unsigned ad = lds0 + hh * 49152 + tt * 4096;
            kA.a = LDSV(cur); kA.b = LDSV(cur + 1024);
            SA = MFMA32(kA.a, qb0, f32x16{}); SA = MFMA32(kA.b, qb1, SA);
            mB = rowmax16(SA);
#pragma unroll
            for (int r = 0; r < 16; ++r) { SA[r] -= mB; negm[r] = -mB; }
            kA.a = LDSV(cur + 4096); kA.b = LDSV(cur + 4096 + 1024);
            ZERO_P(); vA = kA;
            ATT_STEP(SA, SB, negm, qb0, qb1, ob, lb, kA, kB, vA, vB, cur + 2 * 4096, cur, COPY4(a0s, ad + 8 * 4096));
            ATT_STEP(SB, SA, negm, qb0, qb1, ob, lb, kB, kA, vB, vA, cur + 3 * 4096, cur + 1 * 4096, COPY4(a1s, ad + 4 * 4096));
            WAIT_BAR(8);
            ATT_STEP(SA, SB, negm, qb0, qb1, ob, lb, kA, kB, vA, vB, nxt, cur + 2 * 4096, COPY4(a2s, ad));
            ATT_STEP(SB, SA, negm, qb0, qb1, ob, lb, kB, kA, vB, vA, nxt + 4096, cur + 3 * 4096, (void)0);
            ATT_STEP(SA, SB, negm, qb0, qb1, ob, lb, kA, kB, vA, vB, nxt + 2 * 4096, nxt, (void)0);
            WAIT_BAR_L(0);
            COPY4(kvB + ((qb + 2) & 15) * 16384, lds0 + P2_B0 + wid * 4096);
            ATT_STEP(SB, SA, negm, qb0, qb1, ob, lb, kB, kA, vB, vA, nxt + 3 * 4096, nxt + 1 * 4096, (void)0);
            ATT_STEP(SA, SB, negm, qb0, qb1, ob, lb, kA, kB, vA, vB, nxt, nxt + 2 * 4096, (void)0);
            ATT_STEP(SB, SA, negm, qb0, qb1, ob, lb, kB, kA, vB, vA, nxt, nxt + 3 * 4096, (void)0);
            ATT_DRAIN(ob, lb, vA);
        }
        LBAR();
        COPY4(kvB + ((qb + 3) & 15) * 16384, lds0 + P2_B1 + wid * 4096);
        STAMP(6);
        {
            const lds_cptr ab = L + hh * 49152 + (8 + tt) * 4096 + lane * 16;
            kA.a = LDSV(ab); kA.b = LDSV(ab + 1024);
            SA = MFMA32(kA.a, qa0, f32x16{}); SA = MFMA32(kA.b, qa1, SA);
            apply_mask<1>(SA, r32, hi);
            const float m = rowmax16(SA);
#pragma unroll
            for (int r = 0; r < 16; ++r) { SA[r] -= m; negm[r] = -m; }
            ZERO_P(); vA = kA;
            const int nfull = qtile < 7 ? qtile : 7;
            const bool far = qtile >= 8;
            int n = nfull + (far ? 1 : 0);
            f32x16 c1 = negm;
            if (far) apply_mask<2>(c1, r32, hi);
            lds_cptr p1 = n == 0 ? ab : (far ? ab - 8 * 4096 : ab - 4096);
            lds_cptr p2 = n <= 1 ? p1 : (far ? ab - 4096 : ab - 2 * 4096);
            kA.a = LDSV(p1); kA.b = LDSV(p1 + 1024);
            ATT_STEP(SA, SB, c1, qa0, qa1, oa, la, kA, kB, vA, vB, p2, ab, (void)0);
            while (n >= 2) {
                const lds_cptr p3 = n > 2 ? p2 - 4096 : p2;
                ATT_STEP(SB, SA, negm, qa0, qa1, oa, la, kB, kA, vB, vA, p3, p1, (void)0);
                const lds_cptr p4 = n > 3 ? p3 - 4096 : p3;
                ATT_STEP(SA, SB, negm, qa0, qa1, oa, la, kA, kB, vA, vB, p4, p2, (void)0);
                p1 = p3; p2 = p4; n -= 2;
            }
            if (n == 1) { ATT_STEP(SB, SA, negm, qa0, qa1, oa, la, kB, kA, vB, vA, p1, p1, (void)0); ATT_DRAIN(oa, la, vA); }
            else { ATT_DRAIN(oa, la, vB); }
            const unsigned oap = WS_O + (unsigned)(qtile * 64 + h) * 2048 + lane * 16;
            if (__all(finite_state(oa, la[0]))) {
                const float inv = __builtin_amdgcn_rcpf(la[0]);
                st_sc1(rs, oap, pack8(oa, 0, inv)); st_sc1(rs, oap + 1024, pack8(oa, 1, inv));
            } else {
                attn_slow(rs, WS_KV + (unsigned)h * (64 * 4096), qtile >= 8 ? qtile - 8 : 0, qtile, qtile, qtile - 8, qa0, qa1, oap, lane);
            }
        }
        LBAR();
        STAMP(7);
        P3_STAGE(0);
        COPY4(kvB + ((qb + 4) & 15) * 16384, lds0 + 0 + wid * 4096);
        WAIT_BAR(10);
        {
            int so0 = P2_B0, so1 = P2_B1, so2 = 0, so3 = 32768;
            lds_cptr cur = hb + so0;
#pragma unroll
            for (int r = 0; r < 16; ++r) negm[r] = -mB;
            kA.a = LDSV(cur); kA.b = LDSV(cur + 1024);
            SA = MFMA32(kA.a, qb0, negm); SA = MFMA32(kA.b, qb1, SA);
            kA.a = LDSV(cur + 4096); kA.b = LDSV(cur + 4096 + 1024);
            ZERO_P(); vA = kA;
            const unsigned long long wo1 = (unsigned long long)(woA + p3off) - ((unsigned long long)12 << 18);
#define P2_BODY(s, PRE) do { \
                const bool kv = (s) < 13; \
                unsigned long long pk_ = (unsigned long long)kvB + (unsigned)(((qb + (s) + 3) & 15) << 14), pw_ = wo1 + ((unsigned long long)(unsigned)(s) << 18); \
                asm volatile("" : "+s"(pk_), "+s"(pw_)); \
                const char* sb_ = (const char*)(kv ? pk_ : pw_); \
                const unsigned sst = kv ? 1024 : 8 * 2048, sv = kv ? voff : p3vw, db_ = lds0 + so3 + wid * 4096; \
                const lds_cptr nxt = hb + so1; \
                PRE; \
                ATT_STEP(SA, SB, negm, qb0, qb1, ob, lb, kA, kB, vA, vB, cur + 2 * 4096, cur, glds16(sb_, sv, db_)); \
                ATT_STEP(SB, SA, negm, qb0, qb1, ob, lb, kB, kA, vB, vA, cur + 3 * 4096, cur + 1 * 4096, glds16(sb_ + sst, sv, db_ + 1024)); \
                ATT_STEP(SA, SB, negm, qb0, qb1, ob, lb, kA, kB, vA, vB, nxt, cur + 2 * 4096, glds16(sb_ + 2 * sst, sv, db_ + 2048)); \
                ATT_STEP(SB, SA, negm, qb0, qb1, ob, lb, kB, kA, vB, vA, nxt + 4096, cur + 3 * 4096, glds16(sb_ + 3 * sst, sv, db_ + 3072)); \
                cur = nxt; { const int t_ = so0; so0 = so1; so1 = so2; so2 = so3; so3 = t_; } \
                WAIT_BAR_L(4); \
            } while (0)
            for (int s = 2; s < 10; ++s) {
                P2_BODY(s, if (__builtin_expect(s == 9 && wid == 0, 0)) wait16(ws + WS_FLAG2 + rb * 2048, tag, lane));
                if (__builtin_expect(s == 2 && wid == 0, 0)) { if (lane == 0) __hip_atomic_store((gu64*)(ws + WS_FLAG2 + (qb * 16 + hp) * 128), tag, RLX_AGENT); }
            }
            if (blockIdx.x == 0 && tid == 0) __hip_atomic_store((gu64*)(ws + WS_EPOCH), tag, RLX_AGENT);
            P2_BODY(10, P3_LDO(o0, 0));
            P2_BODY(11, P3_LDO(o1, 1));
            P2_BODY(12, P3_LDO(o2, 2));
            for (int s = 13; s < 16; ++s) P2_BODY(s, (void)0);
#undef P2_BODY
            ATT_DRAIN(ob, lb, vA);
        }
        STAMP(8);
        g = __uint_as_float(__hip_atomic_load((gu32*)(ws + WS_G) + rb * 128 + (wid & 3) * 32 + i16, RLX_AGENT));
        g2 = __uint_as_float(__hip_atomic_load((gu32*)(ws + WS_G) + rb * 128 + (wid & 3) * 32 + 16 + i16, RLX_AGENT));
        const unsigned obp = WS_O + (unsigned)(qtile * 64 + 32 + h) * 2048 + lane * 16;
        if (__all(finite_state(ob, lb[0]))) {
            const float inv = __builtin_amdgcn_rcpf(lb[0]);
            st_sc1(rs, obp, pack8(ob, 0, inv)); st_sc1(rs, obp + 1024, pack8(ob, 1, inv));
        } else {
            attn_slow(rs, WS_KV + (unsigned)(32 + h) * (64 * 4096), 0, 63, -1, -1, qb0, qb1, obp, lane);
        }
    }

    {
        const int wt = wid & 3, kh = wid >> 2;
        WAIT_BAR_L(4);
        P3_STAGE(4);
        STAMP(9);
        int wa[4];
        { const int f = (2 * (g4 & 1) + (q4 >> 1)) << 1; const int row = 16 * (g4 >> 1) + 4 * (g4 & 1) + q4;
#pragma unroll
          for (int n = 0; n < 4; ++n) wa[n] = kh * 16384 + row * 128 + (((2 * n + (p4 >> 1)) ^ f) * 16) + (p4 & 1) * 8; }
        f32x4 accA[2][4], accB[2][4];
#pragma unroll
        for (int t = 0; t < 2; ++t)
#pragma unroll
            for (int n = 0; n < 4; ++n) { accA[t][n] = f32x4{}; accB[t][n] = f32x4{}; }
        bf16x8 wfA[4], wfB[4];
#define P3_HEAD(ACC, WC, WN, PN, OA, OB) do { \
            _Pragma("unroll") for (int n = 0; n < 4; ++n) { \
                WN[n] = trfrag(L + (PN) + wa[n], 1024); \
                ACC[0][n] = MFMA16(WC[n], BC8(OA), ACC[0][n]); ACC[1][n] = MFMA16(WC[n], BC8(OB), ACC[1][n]); PIN(ACC[0][n]); PIN(ACC[1][n]); SBAR(); } \
        } while (0)
#define P3_COMP(ACC, OF, c, cn) do { \
            P3_HEAD(ACC, wfA, wfB, P3_SLOT(c) + 1 * 4096, OF[0], OF[1]); P3_HEAD(ACC, wfB, wfA, P3_SLOT(c) + 2 * 4096, OF[2], OF[3]); \
            P3_HEAD(ACC, wfA, wfB, P3_SLOT(c) + 3 * 4096, OF[4], OF[5]); P3_HEAD(ACC, wfB, wfA, P3_SLOT(cn), OF[6], OF[7]); \
        } while (0)
#pragma unroll
        for (int n = 0; n < 4; ++n) wfA[n] = trfrag(L + P3_SLOT(0) + wa[n], 1024);
        P3_COMP(accA, o0, 0, 1);
        WAIT_BAR_L(4);
        if (tid == 0) __hip_atomic_store((gu64*)(ws + WS_FLAG3 + (qb * 16 + hp) * 128), tag, RLX_AGENT);
        P3_STAGE(5); P3_LDO(o0, 3);
        P3_COMP(accA, o1, 1, 2);
        LBAR(); P3_STAGE(6);
        P3_COMP(accA, o2, 2, 3);
        WAIT_BAR_L(16);
        P3_STAGE(7);
        P3_HEAD(accA, wfA, wfB, P3_SLOT(3) + 1 * 4096, o0[0], o0[1]); P3_HEAD(accA, wfB, wfA, P3_SLOT(3) + 2 * 4096, o0[2], o0[3]);
        P3_HEAD(accA, wfA, wfB, P3_SLOT(3) + 3 * 4096, o0[4], o0[5]); P3_HEAD(accA, wfB, wfA, P3_SLOT(3), o0[6], o0[7]);
        WAIT_BAR_L(0);
        STAMP(10);
        gu64* f3 = (gu64*)(ws + WS_FLAG3 + rb * 2048 + (kh * 8 + (lane & 7)) * 128);
        unsigned picked = 0;
        auto pick = [&]() -> unsigned {
            unsigned cand = 0;
            for (unsigned spins = 0; spins < (1u << 18); ++spins) {
                const unsigned long long v = __hip_atomic_load(f3, RLX_AGENT);
                const unsigned m = (unsigned)__ballot(v == tag) & 0xffu, r2 = m & (m >> 1);
                cand = ((r2 & 1u) | ((r2 >> 1) & 2u) | ((r2 >> 2) & 4u) | ((r2 >> 3) & 8u)) & ~picked;
                if (cand) break;
                __builtin_amdgcn_s_sleep(1);
            }
            if (!cand) cand = ~picked & 15u;
            const unsigned a_ = (unsigned)__builtin_amdgcn_readfirstlane(__builtin_ctz(cand));
            picked |= 1u << a_;
            return a_;
        };
#define P3_LDB(OF, a_) do { _Pragma("unroll") for (int i = 0; i < 8; ++i) \
            OF[i] = __builtin_amdgcn_raw_buffer_load_b128(rs, (int)(osrc + (32 + (i >> 1)) * 2048 + (i & 1) * 256), (int)((a_) * 8192), 16); } while (0)
#define P3_SLOTB(a_) ((a_) == 0 ? P3_SLOT(4) : (a_) == 1 ? P3_SLOT(5) : (a_) == 2 ? P3_SLOT(6) : P3_SLOT(7))
#define P3_COMPB(OF, a_) do { const int sl_ = P3_SLOTB(a_); \
            _Pragma("unroll") for (int n = 0; n < 4; ++n) wfA[n] = trfrag(L + sl_ + wa[n], 1024); \
            P3_HEAD(accB, wfA, wfB, sl_ + 1 * 4096, OF[0], OF[1]); P3_HEAD(accB, wfB, wfA, sl_ + 2 * 4096, OF[2], OF[3]); \
            P3_HEAD(accB, wfA, wfB, sl_ + 3 * 4096, OF[4], OF[5]); P3_HEAD(accB, wfB, wfA, sl_, OF[6], OF[7]); \
        } while (0)
        const unsigned a0 = pick(); P3_LDB(o1, a0);
        const unsigned a1 = pick(); P3_LDB(o2, a1);
        P3_COMPB(o1, a0);
        const unsigned a2 = pick(); P3_LDB(o1, a2);
        P3_COMPB(o2, a1);
        const unsigned a3 = pick(); P3_LDB(o2, a3);
        P3_COMPB(o1, a2);
        P3_COMPB(o2, a3);
#undef P3_LDB
#undef P3_COMPB
#undef P3_LDO
#undef P3_COMP
#undef P3_HEAD
        STAMP(11);
        LBAR();
        LAS f32x4* ex = (LAS f32x4*)(LW + wid * 4096) + lane;
        f32x4 mix[2][4];
#pragma unroll
        for (int t = 0; t < 2; ++t)
#pragma unroll
            for (int n = 0; n < 4; ++n) { const float gt = t ? g2 : g; mix[t][n] = accA[t][n] * gt + accB[t][n] * (1.0f - gt); }
#pragma unroll
        for (int t = 0; t < 2; ++t)
#pragma unroll
            for (int n = 0; n < 2; ++n) ex[(t * 2 + n) * 64] = kh ? mix[t][n] : mix[t][2 + n];
        LBAR();
        const LAS f32x4* ey = (const LAS f32x4*)(LW + (wid ^ 4) * 4096) + lane;
#pragma unroll
        for (int t = 0; t < 2; ++t)
#pragma unroll
            for (int n = 0; n < 2; ++n) {
                const f32x4 y = ey[(t * 2 + n) * 64] + (kh ? mix[t][2 + n] : mix[t][n]);
                unsigned short* op = out + (size_t)(rb * 128 + wt * 32 + t * 16 + i16) * DM + cb * 64 + (2 * kh + n) * 16 + 4 * g4;
                *(u32x2*)op = (u32x2){cvtpk(y[0], y[1]), cvtpk(y[2], y[3])};
            }
        STAMP(12);
    }
}

extern "C" void kernel_launch(void* const* d_in, const int* in_sizes, int n_in, void* d_out, int out_size, void* d_ws, size_t ws_size,
                              hipStream_t stream) {
    (void)hipFuncSetAttribute((const void*)fused_kernel, hipFuncAttributeMaxDynamicSharedMemorySize, LDS_BYTES);
    fused_kernel<<<256, 512, LDS_BYTES, stream>>>((const char*)d_in[0], (const char*)d_in[1], (const char*)d_in[2], (const char*)d_in[4], (const char*)d_in[5],
                                                  (const unsigned short*)d_in[6], (char*)d_ws, (unsigned short*)d_out);
}
```

```cpp
#include <hip/hip_runtime.h>
#include <hip/hip_bf16.h>
#include <stdint.h>

#define LAS __attribute__((address_space(3)))
typedef short bf16x8 __attribute__((ext_vector_type(8)));
typedef short s16x4 __attribute__((ext_vector_type(4)));
typedef float f32x16 __attribute__((ext_vector_type(16)));
typedef float f32x4 __attribute__((ext_vector_type(4)));
typedef float f32x2 __attribute__((ext_vector_type(2)));
typedef unsigned u32x4 __attribute__((ext_vector_type(4)));
typedef unsigned u32x2 __attribute__((ext_vector_type(2)));
typedef __bf16 bf16x2_t __attribute__((ext_vector_type(2)));
typedef LAS const char* lds_cptr;
typedef LAS char* lds_ptr;
typedef __attribute__((address_space(1))) unsigned long long gu64;
typedef __attribute__((address_space(1))) unsigned gu32;

#define MFMA32(a, b, c) __builtin_amdgcn_mfma_f32_32x32x16_bf16(a, b, c, 0, 0, 0)
#define MFMA16(a, b, c) __builtin_amdgcn_mfma_f32_16x16x32_bf16(a, b, c, 0, 0, 0)
#define SBAR() __builtin_amdgcn_sched_barrier(0)
#define PIN(x) asm volatile("" : "+v"(x))
#define WAIT_BAR(N) asm volatile("s_waitcnt vmcnt(" #N ")\n\ts_barrier" ::: "memory")
#define WAIT_BAR_L(N) asm volatile("s_waitcnt vmcnt(" #N ") lgkmcnt(0)\n\ts_barrier" ::: "memory")
#define LBAR() asm volatile("s_waitcnt lgkmcnt(0)\n\ts_barrier" ::: "memory")
#define BAR() asm volatile("s_barrier" ::: "memory")
#define RLX_AGENT __ATOMIC_RELAXED, __HIP_MEMORY_SCOPE_AGENT

constexpr int DM = 1024;
constexpr float QSCALE = 0.17677669529663687f * 1.4426950408889634f;

constexpr unsigned WS_KV = 0;
constexpr unsigned WS_O = 16u << 20;
constexpr unsigned WS_G = 24u << 20;
constexpr unsigned WS_EPOCH = WS_G + 65536;
constexpr unsigned WS_FLAG1 = WS_EPOCH + 4096;
constexpr unsigned WS_FLAG2 = WS_FLAG1 + 32768;
constexpr unsigned WS_FLAG3 = WS_FLAG2 + 32768;
constexpr unsigned WS_USED = 32u << 20;
constexpr int LDS_BYTES = 163840;

#define STAMP(i) do {} while (0)

__device__ __forceinline__ unsigned cvtpk(float lo, float hi) {
    f32x2 v = {lo, hi}; bf16x2_t b = __builtin_convertvector(v, bf16x2_t); return __builtin_bit_cast(unsigned, b);
}
__device__ __forceinline__ void glds16(const void* sbase, unsigned voff, unsigned lds_dst) {
    asm volatile("s_mov_b32 m0, %2\n\ts_nop 0\n\tglobal_load_lds_dwordx4 %0, %1" : : "v"(voff), "s"(sbase), "s"(lds_dst) : "memory");
}
__device__ __forceinline__ void touch_lines64(const void* vaddr, unsigned lds_dst) {
    asm volatile("s_mov_b32 m0, %1\n\ts_nop 0\n\tglobal_load_lds_dword %0, off" : : "v"(vaddr), "s"(lds_dst) : "memory");
}
__device__ __forceinline__ void st_sc1(__amdgpu_buffer_rsrc_t rs, unsigned off, u32x4 v) { __builtin_amdgcn_raw_buffer_store_b128(v, rs, (int)off, 0, 16); }
#define LD_AUX 16
__device__ __forceinline__ u32x4 ld_sc1(__amdgpu_buffer_rsrc_t rs, unsigned off) { return __builtin_amdgcn_raw_buffer_load_b128(rs, (int)off, 0, LD_AUX); }
__device__ __forceinline__ void wait16(const char* base, unsigned long long tag, int lane) {
    gu64* f = (gu64*)(base + (lane & 15) * 128);
    for (unsigned spins = 0; spins < (1u << 18); ++spins) {
        const unsigned long long v = __hip_atomic_load(f, RLX_AGENT);
        if (__all(v == tag)) break;
        __builtin_amdgcn_s_sleep(1);
    }
}
__device__ __forceinline__ s16x4 trread(lds_cptr p) {
    typedef short v4i16_t __attribute__((ext_vector_type(4)));
    return __builtin_bit_cast(s16x4, __builtin_amdgcn_ds_read_tr16_b64_v4i16((LAS v4i16_t*)p));
}
__device__ __forceinline__ bf16x8 trfrag(lds_cptr p, int second_off) {
    const s16x4 lo = trread(p), hi = trread(p + second_off);
    return (bf16x8){lo[0], lo[1], lo[2], lo[3], hi[0], hi[1], hi[2], hi[3]};
}
__device__ __forceinline__ u32x4 pack8(const f32x16& a, int s, float sc) {
    u32x4 r;
    r[0] = cvtpk(a[8 * s + 0] * sc, a[8 * s + 1] * sc); r[1] = cvtpk(a[8 * s + 2] * sc, a[8 * s + 3] * sc);
    r[2] = cvtpk(a[8 * s + 4] * sc, a[8 * s + 5] * sc); r[3] = cvtpk(a[8 * s + 6] * sc, a[8 * s + 7] * sc);
    return r;
}
__device__ __forceinline__ u32x4 pack8n(const f32x16& a, int s) {
    u32x4 r;
    r[0] = cvtpk(a[8 * s + 0], a[8 * s + 1]); r[1] = cvtpk(a[8 * s + 2], a[8 * s + 3]);
    r[2] = cvtpk(a[8 * s + 4], a[8 * s + 5]); r[3] = cvtpk(a[8 * s + 6], a[8 * s + 7]);
    return r;
}
__device__ __forceinline__ u32x4 pack44(const f32x4& a, const f32x4& b, float sc) {
    return (u32x4){cvtpk(a[0] * sc, a[1] * sc), cvtpk(a[2] * sc, a[3] * sc), cvtpk(b[0] * sc, b[1] * sc), cvtpk(b[2] * sc, b[3] * sc)};
}
__device__ __forceinline__ u32x4 pack44n(const f32x4& a, const f32x4& b) {
    return (u32x4){cvtpk(a[0], a[1]), cvtpk(a[2], a[3]), cvtpk(b[0], b[1]), cvtpk(b[2], b[3])};
}
#define EXP2(v) __builtin_amdgcn_exp2f(v)
#define LDSV(p) (*(const LAS bf16x8*)(p))
#define LDSW(p) (*(LAS u32x4*)(p))
#define BC8(v) __builtin_bit_cast(bf16x8, v)

__device__ __forceinline__ float halfmax(float m) {
    auto rr = __builtin_amdgcn_permlane32_swap(__float_as_uint(m), __float_as_uint(m), false, false);
    return __builtin_fmaxf(__uint_as_float(rr[0]), __uint_as_float(rr[1]));
}
__device__ __forceinline__ float rowmax16(const f32x16& p) {
    float a = __builtin_fmaxf(p[0], p[1]);
#pragma unroll
    for (int r = 2; r < 16; ++r) a = __builtin_fmaxf(a, p[r]);
    return halfmax(a);
}
template <int MASK> __device__ __forceinline__ void apply_mask(f32x16& s, int r32, int hi) {
#pragma unroll
    for (int r = 0; r < 16; ++r) {
        const int kl = (r & 3) + 8 * (r >> 2) + 4 * hi;
        const bool keep = (MASK == 1) ? (kl <= r32) : (kl > r32);
        if (!keep) s[r] = -INFINITY;
    }
}
__device__ __forceinline__ void attn_slow(__amdgpu_buffer_rsrc_t rs, unsigned kvh, int t_first, int t_last, int t_diag, int t_far, bf16x8 q0, bf16x8 q1, unsigned odst, int lane) {
    const int r32 = lane & 31, hi = lane >> 5;
    f32x16 o = f32x16{}; float m = -INFINITY, l = 0.f;
    for (int t = t_last; t >= t_first; --t) {
        const unsigned p = kvh + (unsigned)t * 4096 + lane * 16;
        const bf16x8 k0 = BC8(ld_sc1(rs, p)), k1 = BC8(ld_sc1(rs, p + 1024)), v0 = BC8(ld_sc1(rs, p + 2048)), v1 = BC8(ld_sc1(rs, p + 3072));
        f32x16 s = MFMA32(k0, q0, f32x16{}); s = MFMA32(k1, q1, s);
        if (t == t_diag) apply_mask<1>(s, r32, hi);
        if (t == t_far) apply_mask<2>(s, r32, hi);
        const float mn = __builtin_fmaxf(m, rowmax16(s));
        const float ms = (mn == -INFINITY) ? 0.f : mn;
        const float alpha = EXP2(m - ms);
        float rs_ = 0.f;
#pragma unroll
        for (int r = 0; r < 16; ++r) { s[r] = EXP2(s[r] - ms); o[r] *= alpha; }
        const u32x4 p0 = pack8n(s, 0), p1 = pack8n(s, 1);
#pragma unroll
        for (int i = 0; i < 4; ++i) rs_ += __uint_as_float(p0[i] << 16) + __uint_as_float(p0[i] & 0xffff0000u) + __uint_as_float(p1[i] << 16) + __uint_as_float(p1[i] & 0xffff0000u);
        l = l * alpha + rs_; m = mn;
        o = MFMA32(v0, BC8(p0), o); o = MFMA32(v1, BC8(p1), o);
    }
    auto rr = __builtin_amdgcn_permlane32_swap(__float_as_uint(l), __float_as_uint(l), false, false);
    l = __uint_as_float(rr[0]) + __uint_as_float(rr[1]);
    const float inv = 1.0f / l;
    st_sc1(rs, odst, pack8(o, 0, inv)); st_sc1(rs, odst + 1024, pack8(o, 1, inv));
}
__device__ __forceinline__ bool finite_state(const f32x16& o, float l) {
    float a = 0.f, b = 0.f;
#pragma unroll
    for (int r = 0; r < 16; r += 2) { a = __builtin_fmaf(o[r], 0.f, a); b = __builtin_fmaf(o[r + 1], 0.f, b); }
    return (l < 1.0e30f) && (a + b == 0.f);
}
struct Fr2 { bf16x8 a, b; };
#define EX4(S, i) do { S[i] = EXP2(S[i]); S[(i) + 1] = EXP2(S[(i) + 1]); S[(i) + 2] = EXP2(S[(i) + 2]); S[(i) + 3] = EXP2(S[(i) + 3]); PIN(S); } while (0)
#define PK4(W, S, i) do { W[0] = cvtpk(S[i], S[(i) + 1]); W[1] = cvtpk(S[(i) + 2], S[(i) + 3]); W[2] = cvtpk(S[(i) + 4], S[(i) + 5]); W[3] = cvtpk(S[(i) + 6], S[(i) + 7]); PIN(W); } while (0)
#define ATT_STEP(SC, SN, CN, Q0, Q1, O, LS, KC, KN, VC, VN, KP, VP, G0) do { \
        SBAR(); \
        SN = MFMA32(KC.a, Q0, CN); PIN(SN); SBAR(); \
        KN.a = LDSV(KP); KN.b = LDSV((KP) + 1024); EX4(SC, 0); SBAR(); \
        O = MFMA32(VC.a, BC8(pw0), O); PIN(O); SBAR(); \
        VN.a = LDSV((VP) + 2048); VN.b = LDSV((VP) + 3072); EX4(SC, 4); SBAR(); \
        SN = MFMA32(KC.b, Q1, SN); PIN(SN); SBAR(); \
        EX4(SC, 8); SBAR(); \
        LS = MFMA16(sel, BC8(pw0), LS); PIN(LS); SBAR(); \
        PK4(pw0, SC, 0); SBAR(); \
        O = MFMA32(VC.b, BC8(pw1), O); PIN(O); SBAR(); \
        EX4(SC, 12); SBAR(); \
        LS = MFMA16(sel, BC8(pw1), LS); PIN(LS); SBAR(); \
        G0; PK4(pw1, SC, 8); SBAR(); \
    } while (0)
#define ATT_DRAIN(O, LS, VC) do { \
        O = MFMA32(VC.a, BC8(pw0), O); O = MFMA32(VC.b, BC8(pw1), O); \
        LS = MFMA16(sel, BC8(pw0), LS); LS = MFMA16(sel, BC8(pw1), LS); \
    } while (0)
#define ZERO_P() do { pw0 = u32x4{}; pw1 = u32x4{}; } while (0)

#define P3BR(c) ((c) >> 2)
constexpr int P1_WR = 49152, P1_WSLOT = 24576, P1_DUMMY = 147456;
constexpr int P2_QX = 0;
constexpr int P2_BOWN = 98304;
struct P1Frags { bf16x8 x[4]; s16x4 wl[6], wh[6]; };
#define WFR(F, n) (bf16x8){F.wl[n][0], F.wl[n][1], F.wl[n][2], F.wl[n][3], F.wh[n][0], F.wh[n][1], F.wh[n][2], F.wh[n][3]}

__global__ __launch_bounds__(512, 2) void fused_kernel(const char* __restrict__ x, const char* __restrict__ wA, const char* __restrict__ woA,
                                                       const char* __restrict__ wB, const char* __restrict__ woB,
                                                       const unsigned short* __restrict__ gate_w, char* __restrict__ ws, unsigned short* __restrict__ out) {
    extern __shared__ __attribute__((aligned(16))) char lds[];
    const int tid = threadIdx.x, lane = tid & 63;
    const int wid = __builtin_amdgcn_readfirstlane(tid >> 6);
    const int xcd = blockIdx.x & 7, jj = blockIdx.x >> 3;
    const int hp = xcd * 2 + (jj & 1), qb = jj >> 1;
    const unsigned lds0 = (unsigned)(uintptr_t)lds;
    const lds_cptr L = (lds_cptr)lds;
    const lds_ptr LW = (lds_ptr)lds;
    const __amdgpu_buffer_rsrc_t rs = __builtin_amdgcn_make_buffer_rsrc((void*)ws, 0, (int)WS_USED, 0x00020000);
    const int i16 = lane & 15, g4 = lane >> 4, q4 = (lane >> 2) & 3, p4 = lane & 3;
    const int r32 = lane & 31, hi = lane >> 5;
    STAMP(0);
    unsigned long long tag;

    const char* xs = x + (size_t)qb * 128 * 2048;
    unsigned vx[2];
#pragma unroll
    for (int p = 0; p < 2; ++p) {
        const int row = 16 * wid + 8 * p + (lane >> 3);
        const int sw = ((row >> 1) & 3) | (((row >> 4) & 1) << 2);
        vx[p] = (unsigned)(row * 2048 + (((lane & 7) ^ sw) * 16));
    }
    const unsigned xdst = lds0 + wid * 2048;
    const int wf_f = (((q4 >> 1) & 1) << 1) | ((g4 & 1) << 2);
    {
        const int wm = wid >> 2, wn = wid & 3, br = wn >> 1, hh = wn & 1;
        const int gtok = qb * 128 + hp * 8 + wid;
        u32x4 ga0, ga1, gg0, gg1;
        { const char* xr = x + (size_t)gtok * 2048; const unsigned go = lane * 32;
          asm volatile("global_load_dwordx4 %0, %4, %5\n\tglobal_load_dwordx4 %1, %4, %5 offset:16\n\tglobal_load_dwordx4 %2, %4, %6\n\tglobal_load_dwordx4 %3, %4, %6 offset:16"
                       : "=&v"(ga0), "=&v"(ga1), "=&v"(gg0), "=&v"(gg1) : "v"(go), "s"(xr), "s"(gate_w) : "memory"); }
        const char* wsrc = (wid < 4 ? wA : wB);
        unsigned vw[3];
#pragma unroll
        for (int i = 0; i < 3; ++i) {
            const int id = wid * 3 + i, sg = id >> 2, rg = id & 3, part = sg % 3;
            const int row = rg * 8 + (lane >> 3);
            const int f = (((row >> 1) & 1) << 1) | (((row >> 3) & 1) << 2);
            vw[i] = (unsigned)(row * 6144 + part * 2048 + hp * 128 + (((lane & 7) ^ f) * 16));
        }
        const unsigned wdst = lds0 + P1_WR + wid * 3072;
#define P1_X(T, slot, p) glds16(xs + (T) * 128, vx[p], xdst + (slot) + (p) * 1024)
#define P1_W(j, i) glds16(wsrc + (size_t)(j) * (32 * 6144), vw[i], wdst + ((j) & 3) * P1_WSLOT + (i) * 1024)
        P1_X(0, 0, 0); P1_X(0, 0, 1); P1_W(0, 0); P1_W(0, 1); P1_W(0, 2);
        P1_W(1, 0); P1_W(1, 1); P1_W(1, 2); P1_X(1, 16384, 0);
        P1_W(2, 0); P1_W(2, 1); P1_W(2, 2); P1_X(1, 16384, 1);
        const char* pfb; unsigned pfstep;
        { const int gw = blockIdx.x * 8 + wid;
          if (lane == 0) { pfb = x + (size_t)gw * 2048; pfstep = 128; }
          else if (lane < 4) { const int id = gw * 3 + lane - 1, bq = id / 3072, rem = id % 3072, row = rem / 48, rest = rem % 48;
                 pfb = (bq ? wB : wA) + (size_t)(row * 6144 + rest * 128); pfstep = 64 * 6144; }
          else { pfb = ((gw >> 10) ? woB : woA) + (size_t)(gw & 1023) * 2048; pfstep = 128; } }
#define P1_PREF(T, F) do { if (lane < 5) touch_lines64(pfb + (size_t)(lane < 4 ? (T) : (F)) * pfstep, lds0 + P1_DUMMY); } while (0)
        P1_PREF(2, 12); P1_PREF(3, 13); P1_PREF(4, 14); P1_PREF(5, 15);
        asm volatile("s_load_dwordx2 %0, %1, 0x0\n\ts_waitcnt lgkmcnt(0)" : "=s"(tag) : "s"(ws + WS_EPOCH) : "memory");
        tag += 1;
        asm volatile("s_waitcnt vmcnt(17)" : "+v"(ga0), "+v"(ga1), "+v"(gg0), "+v"(gg1) :: "memory");
        {
            float s = 0.f;
#pragma unroll
            for (int i = 0; i < 4; ++i) {
                s += __uint_as_float(ga0[i] << 16) * __uint_as_float(gg0[i] << 16) + __uint_as_float(ga0[i] & 0xffff0000u) * __uint_as_float(gg0[i] & 0xffff0000u);
                s += __uint_as_float(ga1[i] << 16) * __uint_as_float(gg1[i] << 16) + __uint_as_float(ga1[i] & 0xffff0000u) * __uint_as_float(gg1[i] & 0xffff0000u);
            }
#pragma unroll
            for (int o = 32; o > 0; o >>= 1) s += __shfl_xor(s, o);
            if (lane == 0) __hip_atomic_store((gu32*)(ws + WS_G) + gtok, __float_as_uint(1.0f / (1.0f + __expf(-s))), RLX_AGENT);
        }
        int xa[2];
        { const int row = wm * 64 + 16 * (i16 >> 3) + (i16 & 7); const int sw = ((row >> 1) & 3) | (((row >> 4) & 1) << 2);
          xa[0] = row * 128 + ((g4 ^ sw) * 16); xa[1] = row * 128 + (((4 + g4) ^ sw) * 16); }
        int wa[2];
#pragma unroll
        for (int np = 0; np < 2; ++np) wa[np] = P1_WR + br * 3 * 4096 + (8 * g4 + q4) * 128 + (((hh * 4 + np * 2 + (p4 >> 1)) ^ wf_f) * 16) + (p4 & 1) * 8;
        f32x4 acc[4][6];
#pragma unroll
        for (int m = 0; m < 4; ++m)
#pragma unroll
            for (int n = 0; n < 6; ++n) acc[m][n] = f32x4{};
#define P1_LX(F, m, xo, kh) F.x[m] = LDSV(L + (xo) + xa[kh] + ((m) >> 1) * 4096 + ((m) & 1) * 1024)
#define P1_LWL(F, n, wo) F.wl[n] = trread(L + (wo) + wa[(n) & 1] + ((n) >> 1) * 4096)
#define P1_LWH(F, n, wo) F.wh[n] = trread(L + (wo) + wa[(n) & 1] + ((n) >> 1) * 4096 + 512)
#define P1_M(F, m, n) do { acc[m][n] = ((n) < 4) ? MFMA16(WFR(F, n), F.x[m], acc[m][n]) : MFMA16(F.x[m], WFR(F, n), acc[m][n]); } while (0)
#define P1_STEP(FC, FN, LD, xo, kh, wo, G0, G1, G2, G3) do { \
            P1_M(FC, 0, 0); if (LD) P1_LX(FN, 0, xo, kh); SBAR();  P1_M(FC, 1, 0); if (LD) P1_LWL(FN, 0, wo); SBAR(); \
            P1_M(FC, 2, 0); if (LD) P1_LWH(FN, 0, wo); SBAR();     P1_M(FC, 3, 0); G0; SBAR(); \
            P1_M(FC, 0, 1); if (LD) P1_LWL(FN, 1, wo); SBAR();     P1_M(FC, 1, 1); if (LD) P1_LWH(FN, 1, wo); SBAR(); \
            P1_M(FC, 2, 1); if (LD) P1_LX(FN, 1, xo, kh); SBAR();  P1_M(FC, 3, 1); if (LD) P1_LWL(FN, 2, wo); SBAR(); \
            P1_M(FC, 0, 2); if (LD) P1_LWH(FN, 2, wo); SBAR();     P1_M(FC, 1, 2); G1; SBAR(); \
            P1_M(FC, 2, 2); if (LD) P1_LWL(FN, 3, wo); SBAR();     P1_M(FC, 3, 2); if (LD) P1_LWH(FN, 3, wo); SBAR(); \
            P1_M(FC, 0, 3); if (LD) P1_LX(FN, 2, xo, kh); SBAR();  P1_M(FC, 1, 3); if (LD) P1_LWL(FN, 4, wo); SBAR(); \
            P1_M(FC, 2, 3); if (LD) P1_LWH(FN, 4, wo); SBAR();     P1_M(FC, 3, 3); G2; SBAR(); \
            P1_M(FC, 0, 4); if (LD) P1_LWL(FN, 5, wo); SBAR();     P1_M(FC, 1, 4); if (LD) P1_LWH(FN, 5, wo); SBAR(); \
            P1_M(FC, 2, 4); if (LD) P1_LX(FN, 3, xo, kh); SBAR();  P1_M(FC, 3, 4); SBAR(); \
            P1_M(FC, 0, 5); G3; SBAR();                            P1_M(FC, 1, 5); SBAR(); \
            P1_M(FC, 2, 5); SBAR();                                P1_M(FC, 3, 5); SBAR(); \
        } while (0)
        WAIT_BAR(9);
        STAMP(1);
        P1Frags FA, FB;
#pragma unroll
        for (int m = 0; m < 4; ++m) P1_LX(FA, m, 0, 0);
#pragma unroll
        for (int n = 0; n < 6; ++n) { P1_LWL(FA, n, 0); P1_LWH(FA, n, 0); }
        int xcur = 0, xnxt = 16384, xfree = 32768;
#define P1_TILE(t, GW0, GX0, GW1, GX1, PF, N0, N1, LASTT) do { \
            const int w1 = ((t) & 1) * 2 * P1_WSLOT + P1_WSLOT, wn0 = (((t) + 1) & 1) * 2 * P1_WSLOT; \
            P1_STEP(FA, FB, true, xcur, 1, w1, if (GW0) P1_W(2 * (t) + 3, 0), if (GW0) P1_W(2 * (t) + 3, 1), if (GW0) P1_W(2 * (t) + 3, 2), if (GX0) P1_X((t) + 2, xfree, 0)); \
            WAIT_BAR(N0); \
            P1_STEP(FB, FA, !(LASTT), xnxt, 0, wn0, if (GW1) P1_W(2 * (t) + 4, 0), if (GW1) P1_W(2 * (t) + 4, 1), if (GW1) P1_W(2 * (t) + 4, 2), if (GX1) P1_X((t) + 2, xfree, 1)); \
            if (PF) P1_PREF((t) + 6 < 16 ? (t) + 6 : 15, (t) < 12 ? (t) : 11); \
            if (!(LASTT)) { WAIT_BAR(N1); } \
            { const int tmp = xcur; xcur = xnxt; xnxt = xfree; xfree = tmp; } \
        } while (0)
        for (int t = 0; t < 14; ++t) P1_TILE(t, true, true, true, true, true, 5, 5, false);
        P1_TILE(14, true, false, false, false, false, 4, 0, false);
        P1_TILE(15, false, false, false, false, false, 0, 0, true);
#undef P1_TILE
#undef P1_STEP
#undef P1_M
#undef P1_LX
#undef P1_LWL
#undef P1_LWH
#undef P1_X
#undef P1_W
        STAMP(2);
        LBAR();
        const int h = hp * 2 + hh;
        const int so = (g4 >> 1) * 1024 + (g4 & 1) * 512;
        const int kvoK = so + (16 * (i16 >> 3) + (i16 & 7)) * 16, kvoV = 2048 + so + i16 * 16;
#pragma unroll
        for (int T = 0; T < 2; ++T) {
            const int tl = wm * 2 + T;
            const unsigned kvg = WS_KV + (unsigned)((br * 32 + h) * 64 + qb * 4 + tl) * 4096;
            const lds_ptr kvl = LW + (br ? P2_BOWN + hh * 16384 + tl * 4096 : hh * 49152 + (8 + tl) * 4096);
            const lds_ptr qxl = LW + P2_QX + ((br * 2 + hh) * 4 + tl) * 2048;
#pragma unroll
            for (int mp = 0; mp < 2; ++mp) {
                const u32x4 kf = pack44n(acc[2 * T + mp][2], acc[2 * T + mp][3]);
                st_sc1(rs, kvg + kvoK + mp * 128, kf); LDSW(kvl + kvoK + mp * 128) = kf;
            }
#pragma unroll
            for (int np = 0; np < 2; ++np) {
                const u32x4 vf = pack44n(acc[2 * T][4 + np], acc[2 * T + 1][4 + np]);
                st_sc1(rs, kvg + kvoV + np * 256, vf); LDSW(kvl + kvoV + np * 256) = vf;
            }
#pragma unroll
            for (int mp = 0; mp < 2; ++mp) LDSW(qxl + kvoK + mp * 128) = pack44(acc[2 * T + mp][0], acc[2 * T + mp][1], QSCALE);
        }
        LBAR();
        STAMP(3);
    }

    const int hh = wid >> 2, tt = wid & 3, h = hp * 2 + hh;
    const int qtile = qb * 4 + tt;
    const char* kvA = ws + WS_KV + (size_t)(h * 64 + tt) * 4096;
    const char* kvB = kvA + (size_t)32 * 64 * 4096;
    const unsigned voff = lane * 16;
#define COPY4(src, dst) do { glds16((src), voff, (dst)); glds16((src) + 1024, voff, (dst) + 1024); glds16((src) + 2048, voff, (dst) + 2048); glds16((src) + 3072, voff, (dst) + 3072); } while (0)
    const bf16x8 qa0 = LDSV(L + P2_QX + (hh * 4 + tt) * 2048 + lane * 16), qa1 = LDSV(L + P2_QX + (hh * 4 + tt) * 2048 + 1024 + lane * 16);
    const bf16x8 qb0 = LDSV(L + P2_QX + ((2 + hh) * 4 + tt) * 2048 + lane * 16), qb1 = LDSV(L + P2_QX + ((2 + hh) * 4 + tt) * 2048 + 1024 + lane * 16);

    const int rb = 4 * (xcd >> 1) + (jj & 3), cb = 8 * (xcd & 1) + (jj >> 2);
    const size_t p3off = (size_t)((wid >> 2) * 512 + (wid & 3) * 32) * 2048 + cb * 128;
    const unsigned p3vw = (unsigned)((lane >> 3) * 2048 + (((lane & 7) ^ (((lane >> 4) & 3) << 1)) * 16));
    const unsigned osrc = WS_O + (unsigned)((rb * 4 + (wid & 3)) * 64 + (wid >> 2) * 16) * 2048 + (g4 >> 1) * 1024 + ((g4 & 1) * 32 + i16) * 16;
    u32x4 o0[8], o1[8], o2[8]; float g, g2;
#define P3_LDO(OF, c) do { _Pragma("unroll") for (int i = 0; i < 8; ++i) OF[i] = ld_sc1(rs, osrc + (P3BR(c) * 32 + ((c) & 3) * 4 + (i >> 1)) * 2048 + (i & 1) * 256); } while (0)
#define P3_SLOT(c) (((c) % 5) == 0 ? 65536 : ((c) % 5) == 1 ? 32768 : ((c) % 5) == 2 ? 131072 : ((c) % 5) == 3 ? 98304 : 0)
#define P3_STAGE(c) do { const char* src_ = (((c) >> 2) ? woB : woA) + p3off + (size_t)((c) & 3) * (128 * 2048); const unsigned dst_ = lds0 + P3_SLOT(c) + wid * 4096; \
        glds16(src_, p3vw, dst_); glds16(src_ + 8 * 2048, p3vw, dst_ + 1024); glds16(src_ + 16 * 2048, p3vw, dst_ + 2048); glds16(src_ + 24 * 2048, p3vw, dst_ + 3072); } while (0)
    {
        bf16x8 sel;
        { const short one = (((lane >> 4) & 1) == ((lane >> 2) & 1)) ? (short)0x3F80 : (short)0;
#pragma unroll
          for (int i = 0; i < 8; ++i) sel[i] = one; }
        f32x16 SA, SB, negm, oa = f32x16{}, ob = f32x16{};
        f32x4 la = f32x4{}, lb = f32x4{};
        Fr2 kA, kB, vA, vB; u32x4 pw0, pw1;
        const lds_cptr hb = L + hh * 16384 + lane * 16;
        float mB;
#define A_RUN(pf, pn_, n_) do { \
            int n = (n_); lds_cptr p1 = n == 0 ? (pf) : (pn_); lds_cptr p2 = n <= 1 ? p1 : p1 - 4096; \
            kA.a = LDSV(p1); kA.b = LDSV(p1 + 1024); ZERO_P(); vA = kA; \
            ATT_STEP(SA, SB, negm, qa0, qa1, oa, la, kA, kB, vA, vB, p2, (pf), (void)0); \
            while (n >= 2) { \
                const lds_cptr p3 = n > 2 ? p2 - 4096 : p2; \
                ATT_STEP(SB, SA, negm, qa0, qa1, oa, la, kB, kA, vB, vA, p3, p1, (void)0); \
                const lds_cptr p4 = n > 3 ? p3 - 4096 : p3; \
                ATT_STEP(SA, SB, negm, qa0, qa1, oa, la, kA, kB, vA, vB, p4, p2, (void)0); \
                p1 = p3; p2 = p4; n -= 2; \
            } \
            if (n == 1) { ATT_STEP(SB, SA, negm, qa0, qa1, oa, la, kB, kA, vB, vA, p1, p1, (void)0); ATT_DRAIN(oa, la, vA); } \
            else { ATT_DRAIN(oa, la, vB); } \
        } while (0)
        const lds_cptr ab = L + hh * 49152 + (8 + tt) * 4096 + lane * 16;
        float mA;
        {
            kA.a = LDSV(ab); kA.b = LDSV(ab + 1024);
            SA = MFMA32(kA.a, qa0, f32x16{}); SA = MFMA32(kA.b, qa1, SA);
            apply_mask<1>(SA, r32, hi);
            mA = rowmax16(SA);
#pragma unroll
            for (int r = 0; r < 16; ++r) { SA[r] -= mA; negm[r] = -mA; }
            A_RUN(ab, ab - 4096, tt);
        }
        WAIT_BAR(0);
        if (tid == 0) __hip_atomic_store((gu64*)(ws + WS_FLAG1 + (hp * 16 + qb) * 128), tag, RLX_AGENT);
        STAMP(4);
        const unsigned kvAo = WS_KV + (unsigned)(h * 64 + tt) * 4096 + lane * 16, kvBo = kvAo + 32u * 64 * 4096;
        const int b2 = qb >= 2 ? qb - 2 : qb, b1 = qb >= 1 ? qb - 1 : qb;
        u32x4 st0[4], st1[4];
        {
            const lds_cptr cur = hb + P2_BOWN;
            kA.a = LDSV(cur); kA.b = LDSV(cur + 1024);
            SA = MFMA32(kA.a, qb0, f32x16{}); SA = MFMA32(kA.b, qb1, SA);
            mB = rowmax16(SA);
#pragma unroll
            for (int r = 0; r < 16; ++r) { SA[r] -= mB; negm[r] = -mB; }
            kA.a = LDSV(cur + 4096); kA.b = LDSV(cur + 4096 + 1024);
            ZERO_P(); vA = kA;
            ATT_STEP(SA, SB, negm, qb0, qb1, ob, lb, kA, kB, vA, vB, cur + 2 * 4096, cur, (void)0);
            ATT_STEP(SB, SA, negm, qb0, qb1, ob, lb, kB, kA, vB, vA, cur + 3 * 4096, cur + 1 * 4096, (void)0);
            {
                gu64* f0 = (gu64*)(ws + WS_FLAG1 + (hp * 16 + ((lane & 1) ? b1 : b2)) * 128);
                for (unsigned spins = 0; spins < (1u << 18); ++spins) {
                    const unsigned long long v = __hip_atomic_load(f0, RLX_AGENT);
                    if (__all(v == tag)) break;
                    __builtin_amdgcn_s_sleep(1);
                }
#pragma unroll
                for (int i = 0; i < 4; ++i) st0[i] = ld_sc1(rs, kvAo + b1 * 16384 + i * 1024);
#pragma unroll
                for (int i = 0; i < 4; ++i) st1[i] = ld_sc1(rs, kvAo + b2 * 16384 + i * 1024);
            }
            ATT_STEP(SA, SB, negm, qb0, qb1, ob, lb, kA, kB, vA, vB, cur, cur + 2 * 4096, (void)0);
            ATT_STEP(SB, SA, negm, qb0, qb1, ob, lb, kB, kA, vB, vA, cur, cur + 3 * 4096, (void)0);
            ATT_DRAIN(ob, lb, vA);
        }
        STAMP(5);
        {
            const lds_ptr ad = LW + hh * 49152 + tt * 4096 + lane * 16;
#pragma unroll
            for (int i = 0; i < 4; ++i) LDSW(ad + 4 * 4096 + i * 1024) = st0[i];
#pragma unroll
            for (int i = 0; i < 4; ++i) LDSW(ad + i * 1024) = st1[i];
        }
        wait16(ws + WS_FLAG1 + hp * 2048, tag, lane);
        if (wid == 0) __builtin_amdgcn_fence(__ATOMIC_ACQUIRE, "agent");
#pragma unroll
        for (int i = 0; i < 4; ++i) st0[i] = ld_sc1(rs, kvBo + ((qb + 1) & 15) * 16384 + i * 1024);
#pragma unroll
        for (int i = 0; i < 4; ++i) st1[i] = ld_sc1(rs, kvBo + ((qb + 2) & 15) * 16384 + i * 1024);
        LBAR();
        STAMP(6);
        if (qb >= 1) {
            const bool far = qb >= 2;
            const lds_cptr p7 = L + hh * 49152 + 7 * 4096 + lane * 16;
            const lds_cptr pf = far ? ab - 8 * 4096 : p7;
#pragma unroll
            for (int r = 0; r < 16; ++r) negm[r] = -mA;
            f32x16 c1 = negm;
            if (far) apply_mask<2>(c1, r32, hi);
            kA.a = LDSV(pf); kA.b = LDSV(pf + 1024);
            SA = MFMA32(kA.a, qa0, c1); SA = MFMA32(kA.b, qa1, SA);
            A_RUN(pf, far ? p7 : p7 - 4096, far ? 7 - tt : 3);
        }
#undef A_RUN
#pragma unroll
        for (int i = 0; i < 4; ++i) { LDSW(LW + 131072 + wid * 4096 + i * 1024 + lane * 16) = st0[i]; LDSW(LW + 98304 + wid * 4096 + i * 1024 + lane * 16) = st1[i]; }
        {
            const unsigned oap = WS_O + (unsigned)(qtile * 64 + h) * 2048 + lane * 16;
            if (__all(finite_state(oa, la[0]))) {
                const float inv = __builtin_amdgcn_rcpf(la[0]);
                st_sc1(rs, oap, pack8(oa, 0, inv)); st_sc1(rs, oap + 1024, pack8(oa, 1, inv));
            } else {
                attn_slow(rs, WS_KV + (unsigned)h * (64 * 4096), qtile >= 8 ? qtile - 8 : 0, qtile, qtile, qtile - 8, qa0, qa1, oap, lane);
            }
        }
        WAIT_BAR_L(2);
        STAMP(7);
        P3_STAGE(0);
        COPY4(kvB + ((qb + 3) & 15) * 16384, lds0 + 0 + wid * 4096);
        {
            int so0 = 131072, so1 = 98304, so2 = 0, so3 = 32768;
            lds_cptr cur = hb + so0;
#pragma unroll
            for (int r = 0; r < 16; ++r) negm[r] = -mB;
            kA.a = LDSV(cur); kA.b = LDSV(cur + 1024);
            SA = MFMA32(kA.a, qb0, negm); SA = MFMA32(kA.b, qb1, SA);
            kA.a = LDSV(cur + 4096); kA.b = LDSV(cur + 4096 + 1024);
            ZERO_P(); vA = kA;
            const unsigned long long wo1 = (unsigned long long)(woA + p3off) - ((unsigned long long)12 << 18);
#define P2_BODY(s, PRE) do { \
                const bool kv = (s) < 13; \
                unsigned long long pk_ = (unsigned long long)kvB + (unsigned)(((qb + (s) + 3) & 15) << 14), pw_ = wo1 + ((unsigned long long)(unsigned)(s) << 18); \
                asm volatile("" : "+s"(pk_), "+s"(pw_)); \
                const char* sb_ = (const char*)(kv ? pk_ : pw_); \
                const unsigned sst = kv ? 1024 : 8 * 2048, sv = kv ? voff : p3vw, db_ = lds0 + so3 + wid * 4096; \
                const lds_cptr nxt = hb + so1; \
                PRE; \
                ATT_STEP(SA, SB, negm, qb0, qb1, ob, lb, kA, kB, vA, vB, cur + 2 * 4096, cur, glds16(sb_, sv, db_)); \
                ATT_STEP(SB, SA, negm, qb0, qb1, ob, lb, kB, kA, vB, vA, cur + 3 * 4096, cur + 1 * 4096, glds16(sb_ + sst, sv, db_ + 1024)); \
                ATT_STEP(SA, SB, negm, qb0, qb1, ob, lb, kA, kB, vA, vB, nxt, cur + 2 * 4096, glds16(sb_ + 2 * sst, sv, db_ + 2048)); \
                ATT_STEP(SB, SA, negm, qb0, qb1, ob, lb, kB, kA, vB, vA, nxt + 4096, cur + 3 * 4096, glds16(sb_ + 3 * sst, sv, db_ + 3072)); \
                cur = nxt; { const int t_ = so0; so0 = so1; so1 = so2; so2 = so3; so3 = t_; } \
                WAIT_BAR_L(4); \
            } while (0)
            for (int s = 1; s < 10; ++s) {
                P2_BODY(s, if (__builtin_expect(s == 9 && wid == 0, 0)) wait16(ws + WS_FLAG2 + rb * 2048, tag, lane));
                if (__builtin_expect(s == 1 && wid == 0, 0)) { if (lane == 0) __hip_atomic_store((gu64*)(ws + WS_FLAG2 + (qb * 16 + hp) * 128), tag, RLX_AGENT); }
            }
            if (blockIdx.x == 0 && tid == 0) __hip_atomic_store((gu64*)(ws + WS_EPOCH), tag, RLX_AGENT);
            P2_BODY(10, P3_LDO(o0, 0));
            P2_BODY(11, P3_LDO(o1, 1));
            P2_BODY(12, P3_LDO(o2, 2));
            for (int s = 13; s < 16; ++s) P2_BODY(s, (void)0);
#undef P2_BODY
            ATT_DRAIN(ob, lb, vA);
        }
        STAMP(8);
        g = __uint_as_float(__hip_atomic_load((gu32*)(ws + WS_G) + rb * 128 + (wid & 3) * 32 + i16, RLX_AGENT));
        g2 = __uint_as_float(__hip_atomic_load((gu32*)(ws + WS_G) + rb * 128 + (wid & 3) * 32 + 16 + i16, RLX_AGENT));
        const unsigned obp = WS_O + (unsigned)(qtile * 64 + 32 + h) * 2048 + lane * 16;
        if (__all(finite_state(ob, lb[0]))) {
            const float inv = __builtin_amdgcn_rcpf(lb[0]);
            st_sc1(rs, obp, pack8(ob, 0, inv)); st_sc1(rs, obp + 1024, pack8(ob, 1, inv));
        } else {
            attn_slow(rs, WS_KV + (unsigned)(32 + h) * (64 * 4096), 0, 63, -1, -1, qb0, qb1, obp, lane);
        }
    }

    {
        const int wt = wid & 3, kh = wid >> 2;
        WAIT_BAR_L(4);
        P3_STAGE(4);
        STAMP(9);
        int wa[4];
        { const int f = (2 * (g4 & 1) + (q4 >> 1)) << 1; const int row = 16 * (g4 >> 1) + 4 * (g4 & 1) + q4;
#pragma unroll
          for (int n = 0; n < 4; ++n) wa[n] = kh * 16384 + row * 128 + (((2 * n + (p4 >> 1)) ^ f) * 16) + (p4 & 1) * 8; }
        f32x4 accA[2][4], accB[2][4];
#pragma unroll
        for (int t = 0; t < 2; ++t)
#pragma unroll
            for (int n = 0; n < 4; ++n) { accA[t][n] = f32x4{}; accB[t][n] = f32x4{}; }
        bf16x8 wfA[4], wfB[4];
#define P3_HEAD(ACC, WC, WN, PN, OA, OB) do { \
            _Pragma("unroll") for (int n = 0; n < 4; ++n) { \
                WN[n] = trfrag(L + (PN) + wa[n], 1024); \
                ACC[0][n] = MFMA16(WC[n], BC8(OA), ACC[0][n]); ACC[1][n] = MFMA16(WC[n], BC8(OB), ACC[1][n]); PIN(ACC[0][n]); PIN(ACC[1][n]); SBAR(); } \
        } while (0)
#define P3_COMP(ACC, OF, c, cn) do { \
            P3_HEAD(ACC, wfA, wfB, P3_SLOT(c) + 1 * 4096, OF[0], OF[1]); P3_HEAD(ACC, wfB, wfA, P3_SLOT(c) + 2 * 4096, OF[2], OF[3]); \
            P3_HEAD(ACC, wfA, wfB, P3_SLOT(c) + 3 * 4096, OF[4], OF[5]); P3_HEAD(ACC, wfB, wfA, P3_SLOT(cn), OF[6], OF[7]); \
        } while (0)
#pragma unroll
        for (int n = 0; n < 4; ++n) wfA[n] = trfrag(L + P3_SLOT(0) + wa[n], 1024);
        P3_COMP(accA, o0, 0, 1);
        WAIT_BAR_L(4);
        if (tid == 0) __hip_atomic_store((gu64*)(ws + WS_FLAG3 + (qb * 16 + hp) * 128), tag, RLX_AGENT);
        P3_STAGE(5); P3_LDO(o0, 3);
        P3_COMP(accA, o1, 1, 2);
        LBAR(); P3_STAGE(6);
        P3_COMP(accA, o2, 2, 3);
        WAIT_BAR_L(16);
        P3_STAGE(7);
        P3_HEAD(accA, wfA, wfB, P3_SLOT(3) + 1 * 4096, o0[0], o0[1]); P3_HEAD(accA, wfB, wfA, P3_SLOT(3) + 2 * 4096, o0[2], o0[3]);
        P3_HEAD(accA, wfA, wfB, P3_SLOT(3) + 3 * 4096, o0[4], o0[5]); P3_HEAD(accA, wfB, wfA, P3_SLOT(3), o0[6], o0[7]);
        WAIT_BAR_L(0);
        STAMP(10);
        auto wait_group = [&](unsigned a_) {
            gu64* f3 = (gu64*)(ws + WS_FLAG3 + rb * 2048 + (kh * 8 + a_ * 2 + (lane & 1)) * 128);
            for (unsigned spins = 0; spins < (1u << 18); ++spins) {
                const unsigned long long v = __hip_atomic_load(f3, RLX_AGENT);
                if (__all(v == tag)) break;
                __builtin_amdgcn_s_sleep(1);
            }
        };
#define P3_LDB(OF, a_) do { _Pragma("unroll") for (int i = 0; i < 8; ++i) \
            OF[i] = __builtin_amdgcn_raw_buffer_load_b128(rs, (int)(osrc + (32 + (i >> 1)) * 2048 + (i & 1) * 256), (int)((a_) * 8192), 16); } while (0)
#define P3_SLOTB(a_) ((a_) == 0 ? P3_SLOT(4) : (a_) == 1 ? P3_SLOT(5) : (a_) == 2 ? P3_SLOT(6) : P3_SLOT(7))
#define P3_COMPB(OF, a_) do { const int sl_ = P3_SLOTB(a_); \
            _Pragma("unroll") for (int n = 0; n < 4; ++n) wfA[n] = trfrag(L + sl_ + wa[n], 1024); \
            P3_HEAD(accB, wfA, wfB, sl_ + 1 * 4096, OF[0], OF[1]); P3_HEAD(accB, wfB, wfA, sl_ + 2 * 4096, OF[2], OF[3]); \
            P3_HEAD(accB, wfA, wfB, sl_ + 3 * 4096, OF[4], OF[5]); P3_HEAD(accB, wfB, wfA, sl_, OF[6], OF[7]); \
        } while (0)
        const unsigned a0 = (xcd + 1) & 3, a1 = (xcd + 2) & 3, a2 = (xcd + 3) & 3, a3 = xcd & 3;
        wait_group(a0); P3_LDB(o1, a0);
        wait_group(a1); P3_LDB(o2, a1);
        P3_COMPB(o1, a0);
        wait_group(a2); P3_LDB(o1, a2);
        P3_COMPB(o2, a1);
        wait_group(a3); P3_LDB(o2, a3);
        P3_COMPB(o1, a2);
        P3_COMPB(o2, a3);
#undef P3_LDB
#undef P3_COMPB
#undef P3_LDO
#undef P3_COMP
#undef P3_HEAD
        STAMP(11);
        LBAR();
        LAS f32x4* ex = (LAS f32x4*)(LW + wid * 4096) + lane;
        f32x4 mix[2][4];
#pragma unroll
        for (int t = 0; t < 2; ++t)
#pragma unroll
            for (int n = 0; n < 4; ++n) { const float gt = t ? g2 : g; mix[t][n] = accA[t][n] * gt + accB[t][n] * (1.0f - gt); }
#pragma unroll
        for (int t = 0; t < 2; ++t)
#pragma unroll
            for (int n = 0; n < 2; ++n) ex[(t * 2 + n) * 64] = kh ? mix[t][n] : mix[t][2 + n];
        LBAR();
        const LAS f32x4* ey = (const LAS f32x4*)(LW + (wid ^ 4) * 4096) + lane;
#pragma unroll
        for (int t = 0; t < 2; ++t)
#pragma unroll
            for (int n = 0; n < 2; ++n) {
                const f32x4 y = ey[(t * 2 + n) * 64] + (kh ? mix[t][2 + n] : mix[t][n]);
                unsigned short* op = out + (size_t)(rb * 128 + wt * 32 + t * 16 + i16) * DM + cb * 64 + (2 * kh + n) * 16 + 4 * g4;
                *(u32x2*)op = (u32x2){cvtpk(y[0], y[1]), cvtpk(y[2], y[3])};
            }
        STAMP(12);
    }
}

extern "C" void kernel_launch(void* const* d_in, const int* in_sizes, int n_in, void* d_out, int out_size, void* d_ws, size_t ws_size,
                              hipStream_t stream) {
    (void)hipFuncSetAttribute((const void*)fused_kernel, hipFuncAttributeMaxDynamicSharedMemorySize, LDS_BYTES);
    fused_kernel<<<256, 512, LDS_BYTES, stream>>>((const char*)d_in[0], (const char*)d_in[1], (const char*)d_in[2], (const char*)d_in[4], (const char*)d_in[5],
                                                  (const unsigned short*)d_in[6], (char*)d_ws, (unsigned short*)d_out);
}
```

```cpp
#include <hip/hip_runtime.h>
#include <hip/hip_bf16.h>
#include <stdint.h>

#define LAS __attribute__((address_space(3)))
typedef short bf16x8 __attribute__((ext_vector_type(8)));
typedef short s16x4 __attribute__((ext_vector_type(4)));
typedef float f32x16 __attribute__((ext_vector_type(16)));
typedef float f32x4 __attribute__((ext_vector_type(4)));
typedef float f32x2 __attribute__((ext_vector_type(2)));
typedef unsigned u32x4 __attribute__((ext_vector_type(4)));
typedef unsigned u32x2 __attribute__((ext_vector_type(2)));
typedef __bf16 bf16x2_t __attribute__((ext_vector_type(2)));
typedef LAS const char* lds_cptr;
typedef LAS char* lds_ptr;
typedef __attribute__((address_space(1))) unsigned long long gu64;
typedef __attribute__((address_space(1))) unsigned gu32;

#define MFMA32(a, b, c) __builtin_amdgcn_mfma_f32_32x32x16_bf16(a, b, c, 0, 0, 0)
#define MFMA16(a, b, c) __builtin_amdgcn_mfma_f32_16x16x32_bf16(a, b, c, 0, 0, 0)
#define SBAR() __builtin_amdgcn_sched_barrier(0)
#define PIN(x) asm volatile("" : "+v"(x))
#define WAIT_BAR(N) asm volatile("s_waitcnt vmcnt(" #N ")\n\ts_barrier" ::: "memory")
#define WAIT_BAR_L(N) asm volatile("s_waitcnt vmcnt(" #N ") lgkmcnt(0)\n\ts_barrier" ::: "memory")
#define LBAR() asm volatile("s_waitcnt lgkmcnt(0)\n\ts_barrier" ::: "memory")
#define BAR() asm volatile("s_barrier" ::: "memory")
#define RLX_AGENT __ATOMIC_RELAXED, __HIP_MEMORY_SCOPE_AGENT

constexpr int DM = 1024;
constexpr float QSCALE = 0.17677669529663687f * 1.4426950408889634f;

constexpr unsigned WS_KV = 0;
constexpr unsigned WS_O = 16u << 20;
constexpr unsigned WS_G = 24u << 20;
constexpr unsigned WS_EPOCH = WS_G + 65536;
constexpr unsigned WS_FLAG1 = WS_EPOCH + 4096;
constexpr unsigned WS_FLAG2 = WS_FLAG1 + 32768;
constexpr unsigned WS_FLAG3 = WS_FLAG2 + 32768;
constexpr unsigned WS_USED = 32u << 20;
constexpr int LDS_BYTES = 163840;

#define STAMP(i) do {} while (0)

__device__ __forceinline__ unsigned cvtpk(float lo, float hi) {
    f32x2 v = {lo, hi}; bf16x2_t b = __builtin_convertvector(v, bf16x2_t); return __builtin_bit_cast(unsigned, b);
}
__device__ __forceinline__ void glds16(const void* sbase, unsigned voff, unsigned lds_dst) {
    asm volatile("s_mov_b32 m0, %2\n\ts_nop 0\n\tglobal_load_lds_dwordx4 %0, %1" : : "v"(voff), "s"(sbase), "s"(lds_dst) : "memory");
}
__device__ __forceinline__ void touch_lines64(const void* vaddr, unsigned lds_dst) {
    asm volatile("s_mov_b32 m0, %1\n\ts_nop 0\n\tglobal_load_lds_dword %0, off" : : "v"(vaddr), "s"(lds_dst) : "memory");
}
__device__ __forceinline__ void st_sc1(__amdgpu_buffer_rsrc_t rs, unsigned off, u32x4 v) { __builtin_amdgcn_raw_buffer_store_b128(v, rs, (int)off, 0, 16); }
#define LD_AUX 16
__device__ __forceinline__ u32x4 ld_sc1(__amdgpu_buffer_rsrc_t rs, unsigned off) { return __builtin_amdgcn_raw_buffer_load_b128(rs, (int)off, 0, LD_AUX); }
__device__ __forceinline__ void wait16(const char* base, unsigned long long tag, int lane) {
    gu64* f = (gu64*)(base + (lane & 15) * 128);
    for (unsigned spins = 0; spins < (1u << 18); ++spins) {
        const unsigned long long v = __hip_atomic_load(f, RLX_AGENT);
        if (__all(v == tag)) break;
        __builtin_amdgcn_s_sleep(1);
    }
}
__device__ __forceinline__ s16x4 trread(lds_cptr p) {
    typedef short v4i16_t __attribute__((ext_vector_type(4)));
    return __builtin_bit_cast(s16x4, __builtin_amdgcn_ds_read_tr16_b64_v4i16((LAS v4i16_t*)p));
}
__device__ __forceinline__ bf16x8 trfrag(lds_cptr p, int second_off) {
    const s16x4 lo = trread(p), hi = trread(p + second_off);
    return (bf16x8){lo[0], lo[1], lo[2], lo[3], hi[0], hi[1], hi[2], hi[3]};
}
__device__ __forceinline__ u32x4 pack8(const f32x16& a, int s, float sc) {
    u32x4 r;
    r[0] = cvtpk(a[8 * s + 0] * sc, a[8 * s + 1] * sc); r[1] = cvtpk(a[8 * s + 2] * sc, a[8 * s + 3] * sc);
    r[2] = cvtpk(a[8 * s + 4] * sc, a[8 * s + 5] * sc); r[3] = cvtpk(a[8 * s + 6] * sc, a[8 * s + 7] * sc);
    return r;
}
__device__ __forceinline__ u32x4 pack8n(const f32x16& a, int s) {
    u32x4 r;
    r[0] = cvtpk(a[8 * s + 0], a[8 * s + 1]); r[1] = cvtpk(a[8 * s + 2], a[8 * s + 3]);
    r[2] = cvtpk(a[8 * s + 4], a[8 * s + 5]); r[3] = cvtpk(a[8 * s + 6], a[8 * s + 7]);
    return r;
}
__device__ __forceinline__ u32x4 pack44(const f32x4& a, const f32x4& b, float sc) {
    return (u32x4){cvtpk(a[0] * sc, a[1] * sc), cvtpk(a[2] * sc, a[3] * sc), cvtpk(b[0] * sc, b[1] * sc), cvtpk(b[2] * sc, b[3] * sc)};
}
__device__ __forceinline__ u32x4 pack44n(const f32x4& a, const f32x4& b) {
    return (u32x4){cvtpk(a[0], a[1]), cvtpk(a[2], a[3]), cvtpk(b[0], b[1]), cvtpk(b[2], b[3])};
}
#define EXP2(v) __builtin_amdgcn_exp2f(v)
#define LDSV(p) (*(const LAS bf16x8*)(p))
#define LDSW(p) (*(LAS u32x4*)(p))
#define BC8(v) __builtin_bit_cast(bf16x8, v)

__device__ __forceinline__ float halfmax(float m) {
    auto rr = __builtin_amdgcn_permlane32_swap(__float_as_uint(m), __float_as_uint(m), false, false);
    return __builtin_fmaxf(__uint_as_float(rr[0]), __uint_as_float(rr[1]));
}
__device__ __forceinline__ float rowmax16(const f32x16& p) {
    float a = __builtin_fmaxf(p[0], p[1]);
#pragma unroll
    for (int r = 2; r < 16; ++r) a = __builtin_fmaxf(a, p[r]);
    return halfmax(a);
}
template <int MASK> __device__ __forceinline__ void apply_mask(f32x16& s, int r32, int hi) {
#pragma unroll
    for (int r = 0; r < 16; ++r) {
        const int kl = (r & 3) + 8 * (r >> 2) + 4 * hi;
        const bool keep = (MASK == 1) ? (kl <= r32) : (kl > r32);
        if (!keep) s[r] = -INFINITY;
    }
}
__device__ __forceinline__ void attn_slow(__amdgpu_buffer_rsrc_t rs, unsigned kvh, int t_first, int t_last, int t_diag, int t_far, bf16x8 q0, bf16x8 q1, unsigned odst, int lane) {
    const int r32 = lane & 31, hi = lane >> 5;
    f32x16 o = f32x16{}; float m = -INFINITY, l = 0.f;
    for (int t = t_last; t >= t_first; --t) {
        const unsigned p = kvh + (unsigned)t * 4096 + lane * 16;
        const bf16x8 k0 = BC8(ld_sc1(rs, p)), k1 = BC8(ld_sc1(rs, p + 1024)), v0 = BC8(ld_sc1(rs, p + 2048)), v1 = BC8(ld_sc1(rs, p + 3072));
        f32x16 s = MFMA32(k0, q0, f32x16{}); s = MFMA32(k1, q1, s);
        if (t == t_diag) apply_mask<1>(s, r32, hi);
        if (t == t_far) apply_mask<2>(s, r32, hi);
        const float mn = __builtin_fmaxf(m, rowmax16(s));
        const float ms = (mn == -INFINITY) ? 0.f : mn;
        const float alpha = EXP2(m - ms);
        float rs_ = 0.f;
#pragma unroll
        for (int r = 0; r < 16; ++r) { s[r] = EXP2(s[r] - ms); o[r] *= alpha; }
        const u32x4 p0 = pack8n(s, 0), p1 = pack8n(s, 1);
#pragma unroll
        for (int i = 0; i < 4; ++i) rs_ += __uint_as_float(p0[i] << 16) + __uint_as_float(p0[i] & 0xffff0000u) + __uint_as_float(p1[i] << 16) + __uint_as_float(p1[i] & 0xffff0000u);
        l = l * alpha + rs_; m = mn;
        o = MFMA32(v0, BC8(p0), o); o = MFMA32(v1, BC8(p1), o);
    }
    auto rr = __builtin_amdgcn_permlane32_swap(__float_as_uint(l), __float_as_uint(l), false, false);
    l = __uint_as_float(rr[0]) + __uint_as_float(rr[1]);
    const float inv = 1.0f / l;
    st_sc1(rs, odst, pack8(o, 0, inv)); st_sc1(rs, odst + 1024, pack8(o, 1, inv));
}
__device__ __forceinline__ bool finite_state(const f32x16& o, float l) {
    float a = 0.f, b = 0.f;
#pragma unroll
    for (int r = 0; r < 16; r += 2) { a = __builtin_fmaf(o[r], 0.f, a); b = __builtin_fmaf(o[r + 1], 0.f, b); }
    return (l < 1.0e30f) && (a + b == 0.f);
}
struct Fr2 { bf16x8 a, b; };
#define EX4(S, i) do { S[i] = EXP2(S[i]); S[(i) + 1] = EXP2(S[(i) + 1]); S[(i) + 2] = EXP2(S[(i) + 2]); S[(i) + 3] = EXP2(S[(i) + 3]); PIN(S); } while (0)
#define PK4(W, S, i) do { W[0] = cvtpk(S[i], S[(i) + 1]); W[1] = cvtpk(S[(i) + 2], S[(i) + 3]); W[2] = cvtpk(S[(i) + 4], S[(i) + 5]); W[3] = cvtpk(S[(i) + 6], S[(i) + 7]); PIN(W); } while (0)
#define ATT_STEP(SC, SN, CN, Q0, Q1, O, LS, KC, KN, VC, VN, KP, VP, G0) do { \
        SBAR(); \
        SN = MFMA32(KC.a, Q0, CN); PIN(SN); SBAR(); \
        KN.a = LDSV(KP); KN.b = LDSV((KP) + 1024); EX4(SC, 0); SBAR(); \
        O = MFMA32(VC.a, BC8(pw0), O); PIN(O); SBAR(); \
        VN.a = LDSV((VP) + 2048); VN.b = LDSV((VP) + 3072); EX4(SC, 4); SBAR(); \
        SN = MFMA32(KC.b, Q1, SN); PIN(SN); SBAR(); \
        EX4(SC, 8); SBAR(); \
        LS = MFMA16(sel, BC8(pw0), LS); PIN(LS); SBAR(); \
        PK4(pw0, SC, 0); SBAR(); \
        O = MFMA32(VC.b, BC8(pw1), O); PIN(O); SBAR(); \
        EX4(SC, 12); SBAR(); \
        LS = MFMA16(sel, BC8(pw1), LS); PIN(LS); SBAR(); \
        G0; PK4(pw1, SC, 8); SBAR(); \
    } while (0)
#define ATT_DRAIN(O, LS, VC) do { \
        O = MFMA32(VC.a, BC8(pw0), O); O = MFMA32(VC.b, BC8(pw1), O); \
        LS = MFMA16(sel, BC8(pw0), LS); LS = MFMA16(sel, BC8(pw1), LS); \
    } while (0)
#define ZERO_P() do { pw0 = u32x4{}; pw1 = u32x4{}; } while (0)

#define P3BR(c) ((c) >> 2)
constexpr int P1_WR = 49152, P1_WSLOT = 24576, P1_DUMMY = 147456;
constexpr int P2_QX = 0;
constexpr int P2_BOWN = 98304;
struct P1Frags { bf16x8 x[4]; s16x4 wl[6], wh[6]; };
#define WFR(F, n) (bf16x8){F.wl[n][0], F.wl[n][1], F.wl[n][2], F.wl[n][3], F.wh[n][0], F.wh[n][1], F.wh[n][2], F.wh[n][3]}

__global__ __launch_bounds__(512, 2) void fused_kernel(const char* __restrict__ x, const char* __restrict__ wA, const char* __restrict__ woA,
                                                       const char* __restrict__ wB, const char* __restrict__ woB,
                                                       const unsigned short* __restrict__ gate_w, char* __restrict__ ws, unsigned short* __restrict__ out) {
    extern __shared__ __attribute__((aligned(16))) char lds[];
    const int tid = threadIdx.x, lane = tid & 63;
    const int wid = __builtin_amdgcn_readfirstlane(tid >> 6);
    const int xcd = blockIdx.x & 7, jj = blockIdx.x >> 3;
    const int hp = xcd * 2 + (jj & 1), qb = jj >> 1;
    const unsigned lds0 = (unsigned)(uintptr_t)lds;
    const lds_cptr L = (lds_cptr)lds;
    const lds_ptr LW = (lds_ptr)lds;
    const __amdgpu_buffer_rsrc_t rs = __builtin_amdgcn_make_buffer_rsrc((void*)ws, 0, (int)WS_USED, 0x00020000);
    const int i16 = lane & 15, g4 = lane >> 4, q4 = (lane >> 2) & 3, p4 = lane & 3;
    const int r32 = lane & 31, hi = lane >> 5;
    STAMP(0);
    unsigned long long tag;

    const char* xs = x + (size_t)qb * 128 * 2048;
    unsigned vx[2];
#pragma unroll
    for (int p = 0; p < 2; ++p) {
        const int row = 16 * wid + 8 * p + (lane >> 3);
        const int sw = ((row >> 1) & 3) | (((row >> 4) & 1) << 2);
        vx[p] = (unsigned)(row * 2048 + (((lane & 7) ^ sw) * 16));
    }
    const unsigned xdst = lds0 + wid * 2048;
    const int wf_f = (((q4 >> 1) & 1) << 1) | ((g4 & 1) << 2);
    {
        const int wm = wid >> 2, wn = wid & 3, br = wn >> 1, hh = wn & 1;
        const int gtok = qb * 128 + hp * 8 + wid;
        u32x4 ga0, ga1, gg0, gg1;
        { const char* xr = x + (size_t)gtok * 2048; const unsigned go = lane * 32;
          asm volatile("global_load_dwordx4 %0, %4, %5\n\tglobal_load_dwordx4 %1, %4, %5 offset:16\n\tglobal_load_dwordx4 %2, %4, %6\n\tglobal_load_dwordx4 %3, %4, %6 offset:16"
                       : "=&v"(ga0), "=&v"(ga1), "=&v"(gg0), "=&v"(gg1) : "v"(go), "s"(xr), "s"(gate_w) : "memory"); }
        const char* wsrc = (wid < 4 ? wA : wB);
        unsigned vw[3];
#pragma unroll
        for (int i = 0; i < 3; ++i) {
            const int id = wid * 3 + i, sg = id >> 2, rg = id & 3, part = sg % 3;
            const int row = rg * 8 + (lane >> 3);
            const int f = (((row >> 1) & 1) << 1) | (((row >> 3) & 1) << 2);
            vw[i] = (unsigned)(row * 6144 + part * 2048 + hp * 128 + (((lane & 7) ^ f) * 16));
        }
        const unsigned wdst = lds0 + P1_WR + wid * 3072;
#define P1_X(T, slot, p) glds16(xs + (T) * 128, vx[p], xdst + (slot) + (p) * 1024)
#define P1_W(j, i) glds16(wsrc + (size_t)(j) * (32 * 6144), vw[i], wdst + ((j) & 3) * P1_WSLOT + (i) * 1024)
        P1_X(0, 0, 0); P1_X(0, 0, 1); P1_W(0, 0); P1_W(0, 1); P1_W(0, 2);
        P1_W(1, 0); P1_W(1, 1); P1_W(1, 2); P1_X(1, 16384, 0);
        P1_W(2, 0); P1_W(2, 1); P1_W(2, 2); P1_X(1, 16384, 1);
        const char* pfb; unsigned pfstep;
        { const int gw = blockIdx.x * 8 + wid, jl = lane & 3;
          if (lane >= 32) { pfb = ((gw >> 10) ? woB : woA) + (size_t)(gw & 1023) * 2048; pfstep = 128; }
          else if (jl == 0) { pfb = x + (size_t)gw * 2048; pfstep = 128; }
          else { const int id = gw * 3 + jl - 1, bq = id / 3072, rem = id % 3072, row = rem / 48, rest = rem % 48;
                 pfb = (bq ? wB : wA) + (size_t)(row * 6144 + rest * 128); pfstep = 64 * 6144; } }
        if (lane < 48) touch_lines64(pfb + (size_t)(lane < 32 ? 2 + (lane >> 2) : lane - 32) * pfstep, lds0 + P1_DUMMY);
#define P1_PREF(T) do { if (lane < 4) touch_lines64(pfb + (size_t)(T) * pfstep, lds0 + P1_DUMMY); } while (0)
        asm volatile("s_load_dwordx2 %0, %1, 0x0\n\ts_waitcnt lgkmcnt(0)" : "=s"(tag) : "s"(ws + WS_EPOCH) : "memory");
        tag += 1;
        asm volatile("s_waitcnt vmcnt(14)" : "+v"(ga0), "+v"(ga1), "+v"(gg0), "+v"(gg1) :: "memory");
        {
            float s = 0.f;
#pragma unroll
            for (int i = 0; i < 4; ++i) {
                s += __uint_as_float(ga0[i] << 16) * __uint_as_float(gg0[i] << 16) + __uint_as_float(ga0[i] & 0xffff0000u) * __uint_as_float(gg0[i] & 0xffff0000u);
                s += __uint_as_float(ga1[i] << 16) * __uint_as_float(gg1[i] << 16) + __uint_as_float(ga1[i] & 0xffff0000u) * __uint_as_float(gg1[i] & 0xffff0000u);
            }
#pragma unroll
            for (int o = 32; o > 0; o >>= 1) s += __shfl_xor(s, o);
            if (lane == 0) __hip_atomic_store((gu32*)(ws + WS_G) + gtok, __float_as_uint(1.0f / (1.0f + __expf(-s))), RLX_AGENT);
        }
        int xa[2];
        { const int row = wm * 64 + 16 * (i16 >> 3) + (i16 & 7); const int sw = ((row >> 1) & 3) | (((row >> 4) & 1) << 2);
          xa[0] = row * 128 + ((g4 ^ sw) * 16); xa[1] = row * 128 + (((4 + g4) ^ sw) * 16); }
        int wa[2];
#pragma unroll
        for (int np = 0; np < 2; ++np) wa[np] = P1_WR + br * 3 * 4096 + (8 * g4 + q4) * 128 + (((hh * 4 + np * 2 + (p4 >> 1)) ^ wf_f) * 16) + (p4 & 1) * 8;
        f32x4 acc[4][6];
#pragma unroll
        for (int m = 0; m < 4; ++m)
#pragma unroll
            for (int n = 0; n < 6; ++n) acc[m][n] = f32x4{};
#define P1_LX(F, m, xo, kh) F.x[m] = LDSV(L + (xo) + xa[kh] + ((m) >> 1) * 4096 + ((m) & 1) * 1024)
#define P1_LWL(F, n, wo) F.wl[n] = trread(L + (wo) + wa[(n) & 1] + ((n) >> 1) * 4096)
#define P1_LWH(F, n, wo) F.wh[n] = trread(L + (wo) + wa[(n) & 1] + ((n) >> 1) * 4096 + 512)
#define P1_M(F, m, n) do { acc[m][n] = ((n) < 4) ? MFMA16(WFR(F, n), F.x[m], acc[m][n]) : MFMA16(F.x[m], WFR(F, n), acc[m][n]); } while (0)
#define P1_STEP(FC, FN, LD, xo, kh, wo, G0, G1, G2, G3) do { \
            P1_M(FC, 0, 0); if (LD) P1_LX(FN, 0, xo, kh); SBAR();  P1_M(FC, 1, 0); if (LD) P1_LWL(FN, 0, wo); SBAR(); \
            P1_M(FC, 2, 0); if (LD) P1_LWH(FN, 0, wo); SBAR();     P1_M(FC, 3, 0); G0; SBAR(); \
            P1_M(FC, 0, 1); if (LD) P1_LWL(FN, 1, wo); SBAR();     P1_M(FC, 1, 1); if (LD) P1_LWH(FN, 1, wo); SBAR(); \
            P1_M(FC, 2, 1); if (LD) P1_LX(FN, 1, xo, kh); SBAR();  P1_M(FC, 3, 1); if (LD) P1_LWL(FN, 2, wo); SBAR(); \
            P1_M(FC, 0, 2); if (LD) P1_LWH(FN, 2, wo); SBAR();     P1_M(FC, 1, 2); G1; SBAR(); \
            P1_M(FC, 2, 2); if (LD) P1_LWL(FN, 3, wo); SBAR();     P1_M(FC, 3, 2); if (LD) P1_LWH(FN, 3, wo); SBAR(); \
            P1_M(FC, 0, 3); if (LD) P1_LX(FN, 2, xo, kh); SBAR();  P1_M(FC, 1, 3); if (LD) P1_LWL(FN, 4, wo); SBAR(); \
            P1_M(FC, 2, 3); if (LD) P1_LWH(FN, 4, wo); SBAR();     P1_M(FC, 3, 3); G2; SBAR(); \
            P1_M(FC, 0, 4); if (LD) P1_LWL(FN, 5, wo); SBAR();     P1_M(FC, 1, 4); if (LD) P1_LWH(FN, 5, wo); SBAR(); \
            P1_M(FC, 2, 4); if (LD) P1_LX(FN, 3, xo, kh); SBAR();  P1_M(FC, 3, 4); SBAR(); \
            P1_M(FC, 0, 5); G3; SBAR();                            P1_M(FC, 1, 5); SBAR(); \
            P1_M(FC, 2, 5); SBAR();                                P1_M(FC, 3, 5); SBAR(); \
        } while (0)
        WAIT_BAR(6);
        STAMP(1);
        P1Frags FA, FB;
#pragma unroll
        for (int m = 0; m < 4; ++m) P1_LX(FA, m, 0, 0);
#pragma unroll
        for (int n = 0; n < 6; ++n) { P1_LWL(FA, n, 0); P1_LWH(FA, n, 0); }
        int xcur = 0, xnxt = 16384, xfree = 32768;
#define P1_TILE(t, GW0, GX0, GW1, GX1, PF, N0, N1, LASTT) do { \
            const int w1 = ((t) & 1) * 2 * P1_WSLOT + P1_WSLOT, wn0 = (((t) + 1) & 1) * 2 * P1_WSLOT; \
            P1_STEP(FA, FB, true, xcur, 1, w1, if (GW0) P1_W(2 * (t) + 3, 0), if (GW0) P1_W(2 * (t) + 3, 1), if (GW0) P1_W(2 * (t) + 3, 2), if (GX0) P1_X((t) + 2, xfree, 0)); \
            WAIT_BAR(N0); \
            P1_STEP(FB, FA, !(LASTT), xnxt, 0, wn0, if (GW1) P1_W(2 * (t) + 4, 0), if (GW1) P1_W(2 * (t) + 4, 1), if (GW1) P1_W(2 * (t) + 4, 2), if (GX1) P1_X((t) + 2, xfree, 1)); \
            if (PF) P1_PREF((t) + 10 < 16 ? (t) + 10 : 15); \
            if (!(LASTT)) { WAIT_BAR(N1); } \
            { const int tmp = xcur; xcur = xnxt; xnxt = xfree; xfree = tmp; } \
        } while (0)
        for (int t = 0; t < 14; ++t) P1_TILE(t, true, true, true, true, true, 5, 5, false);
        P1_TILE(14, true, false, false, false, false, 4, 0, false);
        P1_TILE(15, false, false, false, false, false, 0, 0, true);
#undef P1_TILE
#undef P1_STEP
#undef P1_M
#undef P1_LX
#undef P1_LWL
#undef P1_LWH
#undef P1_X
#undef P1_W
        STAMP(2);
        LBAR();
        const int h = hp * 2 + hh;
        const int so = (g4 >> 1) * 1024 + (g4 & 1) * 512;
        const int kvoK = so + (16 * (i16 >> 3) + (i16 & 7)) * 16, kvoV = 2048 + so + i16 * 16;
#pragma unroll
        for (int T = 0; T < 2; ++T) {
            const int tl = wm * 2 + T;
            const unsigned kvg = WS_KV + (unsigned)((br * 32 + h) * 64 + qb * 4 + tl) * 4096;
            const lds_ptr kvl = LW + (br ? P2_BOWN + hh * 16384 + tl * 4096 : hh * 49152 + (8 + tl) * 4096);
            const lds_ptr qxl = LW + P2_QX + ((br * 2 + hh) * 4 + tl) * 2048;
#pragma unroll
            for (int mp = 0; mp < 2; ++mp) {
                const u32x4 kf = pack44n(acc[2 * T + mp][2], acc[2 * T + mp][3]);
                st_sc1(rs, kvg + kvoK + mp * 128, kf); LDSW(kvl + kvoK + mp * 128) = kf;
            }
#pragma unroll
            for (int np = 0; np < 2; ++np) {
                const u32x4 vf = pack44n(acc[2 * T][4 + np], acc[2 * T + 1][4 + np]);
                st_sc1(rs, kvg + kvoV + np * 256, vf); LDSW(kvl + kvoV + np * 256) = vf;
            }
#pragma unroll
            for (int mp = 0; mp < 2; ++mp) LDSW(qxl + kvoK + mp * 128) = pack44(acc[2 * T + mp][0], acc[2 * T + mp][1], QSCALE);
        }
        LBAR();
        STAMP(3);
    }

    const int hh = wid >> 2, tt = wid & 3, h = hp * 2 + hh;
    const int qtile = qb * 4 + tt;
    const char* kvA = ws + WS_KV + (size_t)(h * 64 + tt) * 4096;
    const char* kvB = kvA + (size_t)32 * 64 * 4096;
    const unsigned voff = lane * 16;
#define COPY4(src, dst) do { glds16((src), voff, (dst)); glds16((src) + 1024, voff, (dst) + 1024); glds16((src) + 2048, voff, (dst) + 2048); glds16((src) + 3072, voff, (dst) + 3072); } while (0)
    const bf16x8 qa0 = LDSV(L + P2_QX + (hh * 4 + tt) * 2048 + lane * 16), qa1 = LDSV(L + P2_QX + (hh * 4 + tt) * 2048 + 1024 + lane * 16);
    const bf16x8 qb0 = LDSV(L + P2_QX + ((2 + hh) * 4 + tt) * 2048 + lane * 16), qb1 = LDSV(L + P2_QX + ((2 + hh) * 4 + tt) * 2048 + 1024 + lane * 16);

    const int rb = 4 * (xcd >> 1) + (jj & 3), cb = 8 * (xcd & 1) + (jj >> 2);
    const size_t p3off = (size_t)((wid >> 2) * 512 + (wid & 3) * 32) * 2048 + cb * 128;
    const unsigned p3vw = (unsigned)((lane >> 3) * 2048 + (((lane & 7) ^ (((lane >> 4) & 3) << 1)) * 16));
    const unsigned osrc = WS_O + (unsigned)((rb * 4 + (wid & 3)) * 64 + (wid >> 2) * 16) * 2048 + (g4 >> 1) * 1024 + ((g4 & 1) * 32 + i16) * 16;
    u32x4 o0[8], o1[8], o2[8]; float g, g2;
#define P3_LDO(OF, c) do { _Pragma("unroll") for (int i = 0; i < 8; ++i) OF[i] = ld_sc1(rs, osrc + (P3BR(c) * 32 + ((c) & 3) * 4 + (i >> 1)) * 2048 + (i & 1) * 256); } while (0)
#define P3_SLOT(c) (((c) % 5) == 0 ? 65536 : ((c) % 5) == 1 ? 32768 : ((c) % 5) == 2 ? 131072 : ((c) % 5) == 3 ? 98304 : 0)
#define P3_STAGE(c) do { const char* src_ = (((c) >> 2) ? woB : woA) + p3off + (size_t)((c) & 3) * (128 * 2048); const unsigned dst_ = lds0 + P3_SLOT(c) + wid * 4096; \
        glds16(src_, p3vw, dst_); glds16(src_ + 8 * 2048, p3vw, dst_ + 1024); glds16(src_ + 16 * 2048, p3vw, dst_ + 2048); glds16(src_ + 24 * 2048, p3vw, dst_ + 3072); } while (0)
    {
        bf16x8 sel;
        { const short one = (((lane >> 4) & 1) == ((lane >> 2) & 1)) ? (short)0x3F80 : (short)0;
#pragma unroll
          for (int i = 0; i < 8; ++i) sel[i] = one; }
        f32x16 SA, SB, negm, oa = f32x16{}, ob = f32x16{};
        f32x4 la = f32x4{}, lb = f32x4{};
        Fr2 kA, kB, vA, vB; u32x4 pw0, pw1;
        const lds_cptr hb = L + hh * 16384 + lane * 16;
        float mB;
#define A_RUN(pf, pn_, n_) do { \
            int n = (n_); lds_cptr p1 = n == 0 ? (pf) : (pn_); lds_cptr p2 = n <= 1 ? p1 : p1 - 4096; \
            kA.a = LDSV(p1); kA.b = LDSV(p1 + 1024); ZERO_P(); vA = kA; \
            ATT_STEP(SA, SB, negm, qa0, qa1, oa, la, kA, kB, vA, vB, p2, (pf), (void)0); \
            while (n >= 2) { \
                const lds_cptr p3 = n > 2 ? p2 - 4096 : p2; \
                ATT_STEP(SB, SA, negm, qa0, qa1, oa, la, kB, kA, vB, vA, p3, p1, (void)0); \
                const lds_cptr p4 = n > 3 ? p3 - 4096 : p3; \
                ATT_STEP(SA, SB, negm, qa0, qa1, oa, la, kA, kB, vA, vB, p4, p2, (void)0); \
                p1 = p3; p2 = p4; n -= 2; \
            } \
            if (n == 1) { ATT_STEP(SB, SA, negm, qa0, qa1, oa, la, kB, kA, vB, vA, p1, p1, (void)0); ATT_DRAIN(oa, la, vA); } \
            else { ATT_DRAIN(oa, la, vB); } \
        } while (0)
        const lds_cptr ab = L + hh * 49152 + (8 + tt) * 4096 + lane * 16;
        float mA;
        {
            kA.a = LDSV(ab); kA.b = LDSV(ab + 1024);
            SA = MFMA32(kA.a, qa0, f32x16{}); SA = MFMA32(kA.b, qa1, SA);
            apply_mask<1>(SA, r32, hi);
            mA = rowmax16(SA);
#pragma unroll
            for (int r = 0; r < 16; ++r) { SA[r] -= mA; negm[r] = -mA; }
            A_RUN(ab, ab - 4096, tt);
        }
        WAIT_BAR(0);
        if (tid == 0) __hip_atomic_store((gu64*)(ws + WS_FLAG1 + (hp * 16 + qb) * 128), tag, RLX_AGENT);
        STAMP(4);
        const unsigned kvAo = WS_KV + (unsigned)(h * 64 + tt) * 4096 + lane * 16, kvBo = kvAo + 32u * 64 * 4096;
        const int b2 = qb >= 2 ? qb - 2 : qb, b1 = qb >= 1 ? qb - 1 : qb;
        u32x4 st0[4], st1[4];
        {
            const lds_cptr cur = hb + P2_BOWN;
            kA.a = LDSV(cur); kA.b = LDSV(cur + 1024);
            SA = MFMA32(kA.a, qb0, f32x16{}); SA = MFMA32(kA.b, qb1, SA);
            mB = rowmax16(SA);
#pragma unroll
            for (int r = 0; r < 16; ++r) { SA[r] -= mB; negm[r] = -mB; }
            kA.a = LDSV(cur + 4096); kA.b = LDSV(cur + 4096 + 1024);
            ZERO_P(); vA = kA;
            ATT_STEP(SA, SB, negm, qb0, qb1, ob, lb, kA, kB, vA, vB, cur + 2 * 4096, cur, (void)0);
            ATT_STEP(SB, SA, negm, qb0, qb1, ob, lb, kB, kA, vB, vA, cur + 3 * 4096, cur + 1 * 4096, (void)0);
            {
                gu64* f0 = (gu64*)(ws + WS_FLAG1 + (hp * 16 + ((lane & 1) ? b1 : b2)) * 128);
                for (unsigned spins = 0; spins < (1u << 18); ++spins) {
                    const unsigned long long v = __hip_atomic_load(f0, RLX_AGENT);
                    if (__all(v == tag)) break;
                    __builtin_amdgcn_s_sleep(1);
                }
#pragma unroll
                for (int i = 0; i < 4; ++i) st0[i] = ld_sc1(rs, kvAo + b1 * 16384 + i * 1024);
#pragma unroll
                for (int i = 0; i < 4; ++i) st1[i] = ld_sc1(rs, kvAo + b2 * 16384 + i * 1024);
            }
            ATT_STEP(SA, SB, negm, qb0, qb1, ob, lb, kA, kB, vA, vB, cur, cur + 2 * 4096, (void)0);
            ATT_STEP(SB, SA, negm, qb0, qb1, ob, lb, kB, kA, vB, vA, cur, cur + 3 * 4096, (void)0);
            ATT_DRAIN(ob, lb, vA);
        }
        STAMP(5);
        {
            const lds_ptr ad = LW + hh * 49152 + tt * 4096 + lane * 16;
#pragma unroll
            for (int i = 0; i < 4; ++i) LDSW(ad + 4 * 4096 + i * 1024) = st0[i];
#pragma unroll
            for (int i = 0; i < 4; ++i) LDSW(ad + i * 1024) = st1[i];
        }
        wait16(ws + WS_FLAG1 + hp * 2048, tag, lane);
        if (wid == 0) __builtin_amdgcn_fence(__ATOMIC_ACQUIRE, "agent");
#pragma unroll
        for (int i = 0; i < 4; ++i) st0[i] = ld_sc1(rs, kvBo + ((qb + 1) & 15) * 16384 + i * 1024);
#pragma unroll
        for (int i = 0; i < 4; ++i) st1[i] = ld_sc1(rs, kvBo + ((qb + 2) & 15) * 16384 + i * 1024);
        LBAR();
        STAMP(6);
        if (qb >= 1) {
            const bool far = qb >= 2;
            const lds_cptr p7 = L + hh * 49152 + 7 * 4096 + lane * 16;
            const lds_cptr pf = far ? ab - 8 * 4096 : p7;
#pragma unroll
            for (int r = 0; r < 16; ++r) negm[r] = -mA;
            f32x16 c1 = negm;
            if (far) apply_mask<2>(c1, r32, hi);
            kA.a = LDSV(pf); kA.b = LDSV(pf + 1024);
            SA = MFMA32(kA.a, qa0, c1); SA = MFMA32(kA.b, qa1, SA);
            A_RUN(pf, far ? p7 : p7 - 4096, far ? 7 - tt : 3);
        }
#undef A_RUN
#pragma unroll
        for (int i = 0; i < 4; ++i) { LDSW(LW + 131072 + wid * 4096 + i * 1024 + lane * 16) = st0[i]; LDSW(LW + 98304 + wid * 4096 + i * 1024 + lane * 16) = st1[i]; }
        {
            const unsigned oap = WS_O + (unsigned)(qtile * 64 + h) * 2048 + lane * 16;
            if (__all(finite_state(oa, la[0]))) {
                const float inv = __builtin_amdgcn_rcpf(la[0]);
                st_sc1(rs, oap, pack8(oa, 0, inv)); st_sc1(rs, oap + 1024, pack8(oa, 1, inv));
            } else {
                attn_slow(rs, WS_KV + (unsigned)h * (64 * 4096), qtile >= 8 ? qtile - 8 : 0, qtile, qtile, qtile - 8, qa0, qa1, oap, lane);
            }
        }
        WAIT_BAR_L(2);
        STAMP(7);
        P3_STAGE(0);
        COPY4(kvB + ((qb + 3) & 15) * 16384, lds0 + 0 + wid * 4096);
        {
            int so0 = 131072, so1 = 98304, so2 = 0, so3 = 32768;
            lds_cptr cur = hb + so0;
#pragma unroll
            for (int r = 0; r < 16; ++r) negm[r] = -mB;
            kA.a = LDSV(cur); kA.b = LDSV(cur + 1024);
            SA = MFMA32(kA.a, qb0, negm); SA = MFMA32(kA.b, qb1, SA);
            kA.a = LDSV(cur + 4096); kA.b = LDSV(cur + 4096 + 1024);
            ZERO_P(); vA = kA;
            const unsigned long long wo1 = (unsigned long long)(woA + p3off) - ((unsigned long long)12 << 18);
#define P2_BODY(s, PRE) do { \
                const bool kv = (s) < 13; \
                unsigned long long pk_ = (unsigned long long)kvB + (unsigned)(((qb + (s) + 3) & 15) << 14), pw_ = wo1 + ((unsigned long long)(unsigned)(s) << 18); \
                asm volatile("" : "+s"(pk_), "+s"(pw_)); \
                const char* sb_ = (const char*)(kv ? pk_ : pw_); \
                const unsigned sst = kv ? 1024 : 8 * 2048, sv = kv ? voff : p3vw, db_ = lds0 + so3 + wid * 4096; \
                const lds_cptr nxt = hb + so1; \
                PRE; \
                ATT_STEP(SA, SB, negm, qb0, qb1, ob, lb, kA, kB, vA, vB, cur + 2 * 4096, cur, glds16(sb_, sv, db_)); \
                ATT_STEP(SB, SA, negm, qb0, qb1, ob, lb, kB, kA, vB, vA, cur + 3 * 4096, cur + 1 * 4096, glds16(sb_ + sst, sv, db_ + 1024)); \
                ATT_STEP(SA, SB, negm, qb0, qb1, ob, lb, kA, kB, vA, vB, nxt, cur + 2 * 4096, glds16(sb_ + 2 * sst, sv, db_ + 2048)); \
                ATT_STEP(SB, SA, negm, qb0, qb1, ob, lb, kB, kA, vB, vA, nxt + 4096, cur + 3 * 4096, glds16(sb_ + 3 * sst, sv, db_ + 3072)); \
                cur = nxt; { const int t_ = so0; so0 = so1; so1 = so2; so2 = so3; so3 = t_; } \
                WAIT_BAR_L(4); \
            } while (0)
            for (int s = 1; s < 10; ++s) {
                P2_BODY(s, if (__builtin_expect(s == 9 && wid == 0, 0)) wait16(ws + WS_FLAG2 + rb * 2048, tag, lane));
                if (__builtin_expect(s == 1 && wid == 0, 0)) { if (lane == 0) __hip_atomic_store((gu64*)(ws + WS_FLAG2 + (qb * 16 + hp) * 128), tag, RLX_AGENT); }
            }
            if (blockIdx.x == 0 && tid == 0) __hip_atomic_store((gu64*)(ws + WS_EPOCH), tag, RLX_AGENT);
            P2_BODY(10, P3_LDO(o0, 0));
            P2_BODY(11, P3_LDO(o1, 1));
            P2_BODY(12, P3_LDO(o2, 2));
            for (int s = 13; s < 16; ++s) P2_BODY(s, (void)0);
#undef P2_BODY
            ATT_DRAIN(ob, lb, vA);
        }
        STAMP(8);
        g = __uint_as_float(__hip_atomic_load((gu32*)(ws + WS_G) + rb * 128 + (wid & 3) * 32 + i16, RLX_AGENT));
        g2 = __uint_as_float(__hip_atomic_load((gu32*)(ws + WS_G) + rb * 128 + (wid & 3) * 32 + 16 + i16, RLX_AGENT));
        const unsigned obp = WS_O + (unsigned)(qtile * 64 + 32 + h) * 2048 + lane * 16;
        if (__all(finite_state(ob, lb[0]))) {
            const float inv = __builtin_amdgcn_rcpf(lb[0]);
            st_sc1(rs, obp, pack8(ob, 0, inv)); st_sc1(rs, obp + 1024, pack8(ob, 1, inv));
        } else {
            attn_slow(rs, WS_KV + (unsigned)(32 + h) * (64 * 4096), 0, 63, -1, -1, qb0, qb1, obp, lane);
        }
    }

    {
        const int wt = wid & 3, kh = wid >> 2;
        WAIT_BAR_L(4);
        P3_STAGE(4);
        STAMP(9);
        int wa[4];
        { const int f = (2 * (g4 & 1) + (q4 >> 1)) << 1; const int row = 16 * (g4 >> 1) + 4 * (g4 & 1) + q4;
#pragma unroll
          for (int n = 0; n < 4; ++n) wa[n] = kh * 16384 + row * 128 + (((2 * n + (p4 >> 1)) ^ f) * 16) + (p4 & 1) * 8; }
        f32x4 accA[2][4], accB[2][4];
#pragma unroll
        for (int t = 0; t < 2; ++t)
#pragma unroll
            for (int n = 0; n < 4; ++n) { accA[t][n] = f32x4{}; accB[t][n] = f32x4{}; }
        bf16x8 wfA[4], wfB[4];
#define P3_HEAD(ACC, WC, WN, PN, OA, OB) do { \
            _Pragma("unroll") for (int n = 0; n < 4; ++n) { \
                WN[n] = trfrag(L + (PN) + wa[n], 1024); \
                ACC[0][n] = MFMA16(WC[n], BC8(OA), ACC[0][n]); ACC[1][n] = MFMA16(WC[n], BC8(OB), ACC[1][n]); PIN(ACC[0][n]); PIN(ACC[1][n]); SBAR(); } \
        } while (0)
#define P3_COMP(ACC, OF, c, cn) do { \
            P3_HEAD(ACC, wfA, wfB, P3_SLOT(c) + 1 * 4096, OF[0], OF[1]); P3_HEAD(ACC, wfB, wfA, P3_SLOT(c) + 2 * 4096, OF[2], OF[3]); \
            P3_HEAD(ACC, wfA, wfB, P3_SLOT(c) + 3 * 4096, OF[4], OF[5]); P3_HEAD(ACC, wfB, wfA, P3_SLOT(cn), OF[6], OF[7]); \
        } while (0)
#pragma unroll
        for (int n = 0; n < 4; ++n) wfA[n] = trfrag(L + P3_SLOT(0) + wa[n], 1024);
        P3_COMP(accA, o0, 0, 1);
        WAIT_BAR_L(4);
        if (tid == 0) __hip_atomic_store((gu64*)(ws + WS_FLAG3 + (qb * 16 + hp) * 128), tag, RLX_AGENT);
        P3_STAGE(5); P3_LDO(o0, 3);
        P3_COMP(accA, o1, 1, 2);
        LBAR(); P3_STAGE(6);
        P3_COMP(accA, o2, 2, 3);
        WAIT_BAR_L(16);
        P3_STAGE(7);
        P3_HEAD(accA, wfA, wfB, P3_SLOT(3) + 1 * 4096, o0[0], o0[1]); P3_HEAD(accA, wfB, wfA, P3_SLOT(3) + 2 * 4096, o0[2], o0[3]);
        P3_HEAD(accA, wfA, wfB, P3_SLOT(3) + 3 * 4096, o0[4], o0[5]); P3_HEAD(accA, wfB, wfA, P3_SLOT(3), o0[6], o0[7]);
        WAIT_BAR_L(0);
        STAMP(10);
        auto wait_group = [&](unsigned a_) {
            gu64* f3 = (gu64*)(ws + WS_FLAG3 + rb * 2048 + (kh * 8 + a_ * 2 + (lane & 1)) * 128);
            for (unsigned spins = 0; spins < (1u << 18); ++spins) {
                const unsigned long long v = __hip_atomic_load(f3, RLX_AGENT);
                if (__all(v == tag)) break;
                __builtin_amdgcn_s_sleep(1);
            }
        };
#define P3_LDB(OF, a_) do { _Pragma("unroll") for (int i = 0; i < 8; ++i) \
            OF[i] = __builtin_amdgcn_raw_buffer_load_b128(rs, (int)(osrc + (32 + (i >> 1)) * 2048 + (i & 1) * 256), (int)((a_) * 8192), 16); } while (0)
#define P3_SLOTB(a_) ((a_) == 0 ? P3_SLOT(4) : (a_) == 1 ? P3_SLOT(5) : (a_) == 2 ? P3_SLOT(6) : P3_SLOT(7))
#define P3_COMPB(OF, a_) do { const int sl_ = P3_SLOTB(a_); \
            _Pragma("unroll") for (int n = 0; n < 4; ++n) wfA[n] = trfrag(L + sl_ + wa[n], 1024); \
            P3_HEAD(accB, wfA, wfB, sl_ + 1 * 4096, OF[0], OF[1]); P3_HEAD(accB, wfB, wfA, sl_ + 2 * 4096, OF[2], OF[3]); \
            P3_HEAD(accB, wfA, wfB, sl_ + 3 * 4096, OF[4], OF[5]); P3_HEAD(accB, wfB, wfA, sl_, OF[6], OF[7]); \
        } while (0)
        const unsigned a0 = (xcd + 1) & 3, a1 = (xcd + 2) & 3, a2 = (xcd + 3) & 3, a3 = xcd & 3;
        wait_group(a0); P3_LDB(o1, a0);
        wait_group(a1); P3_LDB(o2, a1);
        P3_COMPB(o1, a0);
        wait_group(a2); P3_LDB(o1, a2);
        P3_COMPB(o2, a1);
        wait_group(a3); P3_LDB(o2, a3);
        P3_COMPB(o1, a2);
        P3_COMPB(o2, a3);
#undef P3_LDB
#undef P3_COMPB
#undef P3_LDO
#undef P3_COMP
#undef P3_HEAD
        STAMP(11);
        LBAR();
        LAS f32x4* ex = (LAS f32x4*)(LW + wid * 4096) + lane;
        f32x4 mix[2][4];
#pragma unroll
        for (int t = 0; t < 2; ++t)
#pragma unroll
            for (int n = 0; n < 4; ++n) { const float gt = t ? g2 : g; mix[t][n] = accA[t][n] * gt + accB[t][n] * (1.0f - gt); }
#pragma unroll
        for (int t = 0; t < 2; ++t)
#pragma unroll
            for (int n = 0; n < 2; ++n) ex[(t * 2 + n) * 64] = kh ? mix[t][n] : mix[t][2 + n];
        LBAR();
        const LAS f32x4* ey = (const LAS f32x4*)(LW + (wid ^ 4) * 4096) + lane;
#pragma unroll
        for (int t = 0; t < 2; ++t)
#pragma unroll
            for (int n = 0; n < 2; ++n) {
                const f32x4 y = ey[(t * 2 + n) * 64] + (kh ? mix[t][2 + n] : mix[t][n]);
                unsigned short* op = out + (size_t)(rb * 128 + wt * 32 + t * 16 + i16) * DM + cb * 64 + (2 * kh + n) * 16 + 4 * g4;
                *(u32x2*)op = (u32x2){cvtpk(y[0], y[1]), cvtpk(y[2], y[3])};
            }
        STAMP(12);
    }
}

extern "C" void kernel_launch(void* const* d_in, const int* in_sizes, int n_in, void* d_out, int out_size, void* d_ws, size_t ws_size,
                              hipStream_t stream) {
    (void)hipFuncSetAttribute((const void*)fused_kernel, hipFuncAttributeMaxDynamicSharedMemorySize, LDS_BYTES);
    fused_kernel<<<256, 512, LDS_BYTES, stream>>>((const char*)d_in[0], (const char*)d_in[1], (const char*)d_in[2], (const char*)d_in[4], (const char*)d_in[5],
                                                  (const unsigned short*)d_in[6], (char*)d_ws, (unsigned short*)d_out);
}
```

```cpp
#include <hip/hip_runtime.h>
#include <hip/hip_bf16.h>
#include <stdint.h>

#define LAS __attribute__((address_space(3)))
typedef short bf16x8 __attribute__((ext_vector_type(8)));
typedef short s16x4 __attribute__((ext_vector_type(4)));
typedef float f32x16 __attribute__((ext_vector_type(16)));
typedef float f32x4 __attribute__((ext_vector_type(4)));
typedef float f32x2 __attribute__((ext_vector_type(2)));
typedef unsigned u32x4 __attribute__((ext_vector_type(4)));
typedef unsigned u32x2 __attribute__((ext_vector_type(2)));
typedef __bf16 bf16x2_t __attribute__((ext_vector_type(2)));
typedef LAS const char* lds_cptr;
typedef LAS char* lds_ptr;
typedef __attribute__((address_space(1))) unsigned long long gu64;
typedef __attribute__((address_space(1))) unsigned gu32;

#define MFMA32(a, b, c) __builtin_amdgcn_mfma_f32_32x32x16_bf16(a, b, c, 0, 0, 0)
#define MFMA16(a, b, c) __builtin_amdgcn_mfma_f32_16x16x32_bf16(a, b, c, 0, 0, 0)
#define SBAR() __builtin_amdgcn_sched_barrier(0)
#define PIN(x) asm volatile("" : "+v"(x))
#define WAIT_BAR(N) asm volatile("s_waitcnt vmcnt(" #N ")\n\ts_barrier" ::: "memory")
#define WAIT_BAR_L(N) asm volatile("s_waitcnt vmcnt(" #N ") lgkmcnt(0)\n\ts_barrier" ::: "memory")
#define LBAR() asm volatile("s_waitcnt lgkmcnt(0)\n\ts_barrier" ::: "memory")
#define BAR() asm volatile("s_barrier" ::: "memory")
#define RLX_AGENT __ATOMIC_RELAXED, __HIP_MEMORY_SCOPE_AGENT

constexpr int DM = 1024;
constexpr float QSCALE = 0.17677669529663687f * 1.4426950408889634f;

constexpr unsigned WS_KV = 0;
constexpr unsigned WS_O = 16u << 20;
constexpr unsigned WS_G = 24u << 20;
constexpr unsigned WS_EPOCH = WS_G + 65536;
constexpr unsigned WS_FLAG1 = WS_EPOCH + 4096;
constexpr unsigned WS_FLAG2 = WS_FLAG1 + 32768;
constexpr unsigned WS_FLAG3 = WS_FLAG2 + 32768;
constexpr unsigned WS_USED = 32u << 20;
constexpr int LDS_BYTES = 163840;

#define STAMP(i) do {} while (0)

__device__ __forceinline__ unsigned cvtpk(float lo, float hi) {
    f32x2 v = {lo, hi}; bf16x2_t b = __builtin_convertvector(v, bf16x2_t); return __builtin_bit_cast(unsigned, b);
}
__device__ __forceinline__ void glds16(const void* sbase, unsigned voff, unsigned lds_dst) {
    asm volatile("s_mov_b32 m0, %2\n\ts_nop 0\n\tglobal_load_lds_dwordx4 %0, %1" : : "v"(voff), "s"(sbase), "s"(lds_dst) : "memory");
}
__device__ __forceinline__ void touch_lines64(const void* vaddr, unsigned lds_dst) {
    asm volatile("s_mov_b32 m0, %1\n\ts_nop 0\n\tglobal_load_lds_dword %0, off" : : "v"(vaddr), "s"(lds_dst) : "memory");
}
__device__ __forceinline__ void st_sc1(__amdgpu_buffer_rsrc_t rs, unsigned off, u32x4 v) { __builtin_amdgcn_raw_buffer_store_b128(v, rs, (int)off, 0, 16); }
#define LD_AUX 16
__device__ __forceinline__ u32x4 ld_sc1(__amdgpu_buffer_rsrc_t rs, unsigned off) { return __builtin_amdgcn_raw_buffer_load_b128(rs, (int)off, 0, LD_AUX); }
__device__ __forceinline__ void wait16(const char* base, unsigned long long tag, int lane) {
    gu64* f = (gu64*)(base + (lane & 15) * 128);
    for (unsigned spins = 0; spins < (1u << 18); ++spins) {
        const unsigned long long v = __hip_atomic_load(f, RLX_AGENT);
        if (__all(v == tag)) break;
        __builtin_amdgcn_s_sleep(1);
    }
}
__device__ __forceinline__ s16x4 trread(lds_cptr p) {
    typedef short v4i16_t __attribute__((ext_vector_type(4)));
    return __builtin_bit_cast(s16x4, __builtin_amdgcn_ds_read_tr16_b64_v4i16((LAS v4i16_t*)p));
}
__device__ __forceinline__ bf16x8 trfrag(lds_cptr p, int second_off) {
    const s16x4 lo = trread(p), hi = trread(p + second_off);
    return (bf16x8){lo[0], lo[1], lo[2], lo[3], hi[0], hi[1], hi[2], hi[3]};
}
__device__ __forceinline__ u32x4 pack8(const f32x16& a, int s, float sc) {
    u32x4 r;
    r[0] = cvtpk(a[8 * s + 0] * sc, a[8 * s + 1] * sc); r[1] = cvtpk(a[8 * s + 2] * sc, a[8 * s + 3] * sc);
    r[2] = cvtpk(a[8 * s + 4] * sc, a[8 * s + 5] * sc); r[3] = cvtpk(a[8 * s + 6] * sc, a[8 * s + 7] * sc);
    return r;
}
__device__ __forceinline__ u32x4 pack8n(const f32x16& a, int s) {
    u32x4 r;
    r[0] = cvtpk(a[8 * s + 0], a[8 * s + 1]); r[1] = cvtpk(a[8 * s + 2], a[8 * s + 3]);
    r[2] = cvtpk(a[8 * s + 4], a[8 * s + 5]); r[3] = cvtpk(a[8 * s + 6], a[8 * s + 7]);
    return r;
}
__device__ __forceinline__ u32x4 pack44(const f32x4& a, const f32x4& b, float sc) {
    return (u32x4){cvtpk(a[0] * sc, a[1] * sc), cvtpk(a[2] * sc, a[3] * sc), cvtpk(b[0] * sc, b[1] * sc), cvtpk(b[2] * sc, b[3] * sc)};
}
__device__ __forceinline__ u32x4 pack44n(const f32x4& a, const f32x4& b) {
    return (u32x4){cvtpk(a[0], a[1]), cvtpk(a[2], a[3]), cvtpk(b[0], b[1]), cvtpk(b[2], b[3])};
}
#define EXP2(v) __builtin_amdgcn_exp2f(v)
#define LDSV(p) (*(const LAS bf16x8*)(p))
#define LDSW(p) (*(LAS u32x4*)(p))
#define BC8(v) __builtin_bit_cast(bf16x8, v)

__device__ __forceinline__ float halfmax(float m) {
    auto rr = __builtin_amdgcn_permlane32_swap(__float_as_uint(m), __float_as_uint(m), false, false);
    return __builtin_fmaxf(__uint_as_float(rr[0]), __uint_as_float(rr[1]));
}
__device__ __forceinline__ float rowmax16(const f32x16& p) {
    float a = __builtin_fmaxf(p[0], p[1]);
#pragma unroll
    for (int r = 2; r < 16; ++r) a = __builtin_fmaxf(a, p[r]);
    return halfmax(a);
}
template <int MASK> __device__ __forceinline__ void apply_mask(f32x16& s, int r32, int hi) {
#pragma unroll
    for (int r = 0; r < 16; ++r) {
        const int kl = (r & 3) + 8 * (r >> 2) + 4 * hi;
        const bool keep = (MASK == 1) ? (kl <= r32) : (kl > r32);
        if (!keep) s[r] = -INFINITY;
    }
}
__device__ __forceinline__ void attn_slow(__amdgpu_buffer_rsrc_t rs, unsigned kvh, int t_first, int t_last, int t_diag, int t_far, bf16x8 q0, bf16x8 q1, unsigned odst, int lane) {
    const int r32 = lane & 31, hi = lane >> 5;
    f32x16 o = f32x16{}; float m = -INFINITY, l = 0.f;
    for (int t = t_last; t >= t_first; --t) {
        const unsigned p = kvh + (unsigned)t * 4096 + lane * 16;
        const bf16x8 k0 = BC8(ld_sc1(rs, p)), k1 = BC8(ld_sc1(rs, p + 1024)), v0 = BC8(ld_sc1(rs, p + 2048)), v1 = BC8(ld_sc1(rs, p + 3072));
        f32x16 s = MFMA32(k0, q0, f32x16{}); s = MFMA32(k1, q1, s);
        if (t == t_diag) apply_mask<1>(s, r32, hi);
        if (t == t_far) apply_mask<2>(s, r32, hi);
        const float mn = __builtin_fmaxf(m, rowmax16(s));
        const float ms = (mn == -INFINITY) ? 0.f : mn;
        const float alpha = EXP2(m - ms);
        float rs_ = 0.f;
#pragma unroll
        for (int r = 0; r < 16; ++r) { s[r] = EXP2(s[r] - ms); o[r] *= alpha; }
        const u32x4 p0 = pack8n(s, 0), p1 = pack8n(s, 1);
#pragma unroll
        for (int i = 0; i < 4; ++i) rs_ += __uint_as_float(p0[i] << 16) + __uint_as_float(p0[i] & 0xffff0000u) + __uint_as_float(p1[i] << 16) + __uint_as_float(p1[i] & 0xffff0000u);
        l = l * alpha + rs_; m = mn;
        o = MFMA32(v0, BC8(p0), o); o = MFMA32(v1, BC8(p1), o);
    }
    auto rr = __builtin_amdgcn_permlane32_swap(__float_as_uint(l), __float_as_uint(l), false, false);
    l = __uint_as_float(rr[0]) + __uint_as_float(rr[1]);
    const float inv = 1.0f / l;
    st_sc1(rs, odst, pack8(o, 0, inv)); st_sc1(rs, odst + 1024, pack8(o, 1, inv));
}
__device__ __forceinline__ bool finite_state(const f32x16& o, float l) {
    float a = 0.f, b = 0.f;
#pragma unroll
    for (int r = 0; r < 16; r += 2) { a = __builtin_fmaf(o[r], 0.f, a); b = __builtin_fmaf(o[r + 1], 0.f, b); }
    return (l < 1.0e30f) && (a + b == 0.f);
}
struct Fr2 { bf16x8 a, b; };
#define EX4(S, i) do { S[i] = EXP2(S[i]); S[(i) + 1] = EXP2(S[(i) + 1]); S[(i) + 2] = EXP2(S[(i) + 2]); S[(i) + 3] = EXP2(S[(i) + 3]); PIN(S); } while (0)
#define PK4(W, S, i) do { W[0] = cvtpk(S[i], S[(i) + 1]); W[1] = cvtpk(S[(i) + 2], S[(i) + 3]); W[2] = cvtpk(S[(i) + 4], S[(i) + 5]); W[3] = cvtpk(S[(i) + 6], S[(i) + 7]); PIN(W); } while (0)
#define ATT_STEP(SC, SN, CN, Q0, Q1, O, LS, KC, KN, VC, VN, KP, VP, G0) do { \
        SBAR(); \
        SN = MFMA32(KC.a, Q0, CN); PIN(SN); SBAR(); \
        KN.a = LDSV(KP); KN.b = LDSV((KP) + 1024); EX4(SC, 0); SBAR(); \
        O = MFMA32(VC.a, BC8(pw0), O); PIN(O); SBAR(); \
        VN.a = LDSV((VP) + 2048); VN.b = LDSV((VP) + 3072); EX4(SC, 4); SBAR(); \
        SN = MFMA32(KC.b, Q1, SN); PIN(SN); SBAR(); \
        EX4(SC, 8); SBAR(); \
        LS = MFMA16(sel, BC8(pw0), LS); PIN(LS); SBAR(); \
        PK4(pw0, SC, 0); SBAR(); \
        O = MFMA32(VC.b, BC8(pw1), O); PIN(O); SBAR(); \
        EX4(SC, 12); SBAR(); \
        LS = MFMA16(sel, BC8(pw1), LS); PIN(LS); SBAR(); \
        G0; PK4(pw1, SC, 8); SBAR(); \
    } while (0)
#define ATT_DRAIN(O, LS, VC) do { \
        O = MFMA32(VC.a, BC8(pw0), O); O = MFMA32(VC.b, BC8(pw1), O); \
        LS = MFMA16(sel, BC8(pw0), LS); LS = MFMA16(sel, BC8(pw1), LS); \
    } while (0)
#define ZERO_P() do { pw0 = u32x4{}; pw1 = u32x4{}; } while (0)

#define P3BR(c) ((c) >> 2)
constexpr int P1_WR = 49152, P1_WSLOT = 24576, P1_DUMMY = 147456;
constexpr int P2_QX = 0;
constexpr int P2_BOWN = 98304;
struct P1Frags { bf16x8 x[4]; s16x4 wl[6], wh[6]; };
#define WFR(F, n) (bf16x8){F.wl[n][0], F.wl[n][1], F.wl[n][2], F.wl[n][3], F.wh[n][0], F.wh[n][1], F.wh[n][2], F.wh[n][3]}

__global__ __launch_bounds__(512, 2) void fused_kernel(const char* __restrict__ x, const char* __restrict__ wA, const char* __restrict__ woA,
                                                       const char* __restrict__ wB, const char* __restrict__ woB,
                                                       const unsigned short* __restrict__ gate_w, char* __restrict__ ws, unsigned short* __restrict__ out) {
    extern __shared__ __attribute__((aligned(16))) char lds[];
    const int tid = threadIdx.x, lane = tid & 63;
    const int wid = __builtin_amdgcn_readfirstlane(tid >> 6);
    const int xcd = blockIdx.x & 7, jj = blockIdx.x >> 3;
    const int hp = xcd * 2 + (jj & 1), qb = jj >> 1;
    const unsigned lds0 = (unsigned)(uintptr_t)lds;
    const lds_cptr L = (lds_cptr)lds;
    const lds_ptr LW = (lds_ptr)lds;
    const __amdgpu_buffer_rsrc_t rs = __builtin_amdgcn_make_buffer_rsrc((void*)ws, 0, (int)WS_USED, 0x00020000);
    const int i16 = lane & 15, g4 = lane >> 4, q4 = (lane >> 2) & 3, p4 = lane & 3;
    const int r32 = lane & 31, hi = lane >> 5;
    STAMP(0);
    unsigned long long tag;

    const char* xs = x + (size_t)qb * 128 * 2048;
    unsigned vx[2];
#pragma unroll
    for (int p = 0; p < 2; ++p) {
        const int row = 16 * wid + 8 * p + (lane >> 3);
        const int sw = ((row >> 1) & 3) | (((row >> 4) & 1) << 2);
        vx[p] = (unsigned)(row * 2048 + (((lane & 7) ^ sw) * 16));
    }
    const unsigned xdst = lds0 + wid * 2048;
    const int wf_f = (((q4 >> 1) & 1) << 1) | ((g4 & 1) << 2);
    {
        const int wm = wid >> 2, wn = wid & 3, br = wn >> 1, hh = wn & 1;
        const int gtok = qb * 128 + hp * 8 + wid;
        u32x4 ga0, ga1, gg0, gg1;
        { const char* xr = x + (size_t)gtok * 2048; const unsigned go = lane * 32;
          asm volatile("global_load_dwordx4 %0, %4, %5\n\tglobal_load_dwordx4 %1, %4, %5 offset:16\n\tglobal_load_dwordx4 %2, %4, %6\n\tglobal_load_dwordx4 %3, %4, %6 offset:16"
                       : "=&v"(ga0), "=&v"(ga1), "=&v"(gg0), "=&v"(gg1) : "v"(go), "s"(xr), "s"(gate_w) : "memory"); }
        const char* wsrc = (wid < 4 ? wA : wB);
        unsigned vw[3];
#pragma unroll
        for (int i = 0; i < 3; ++i) {
            const int id = wid * 3 + i, sg = id >> 2, rg = id & 3, part = sg % 3;
            const int row = rg * 8 + (lane >> 3);
            const int f = (((row >> 1) & 1) << 1) | (((row >> 3) & 1) << 2);
            vw[i] = (unsigned)(row * 6144 + part * 2048 + hp * 128 + (((lane & 7) ^ f) * 16));
        }
        const unsigned wdst = lds0 + P1_WR + wid * 3072;
#define P1_X(T, slot, p) glds16(xs + (T) * 128, vx[p], xdst + (slot) + (p) * 1024)
#define P1_W(j, i) glds16(wsrc + (size_t)(j) * (32 * 6144), vw[i], wdst + ((j) & 3) * P1_WSLOT + (i) * 1024)
        P1_X(0, 0, 0); P1_X(0, 0, 1); P1_W(0, 0); P1_W(0, 1); P1_W(0, 2);
        P1_W(1, 0); P1_W(1, 1); P1_W(1, 2); P1_X(1, 16384, 0);
        P1_W(2, 0); P1_W(2, 1); P1_W(2, 2); P1_X(1, 16384, 1);
        const char* pfb; unsigned pfstep;
        { const int gw = blockIdx.x * 8 + wid, jl = lane & 3;
          if (lane >= 32) { pfb = ((gw >> 10) ? woB : woA) + (size_t)(gw & 1023) * 2048; pfstep = 128; }
          else if (jl == 0) { pfb = x + (size_t)gw * 2048; pfstep = 128; }
          else { const int id = gw * 3 + jl - 1, bq = id / 3072, rem = id % 3072, row = rem / 48, rest = rem % 48;
                 pfb = (bq ? wB : wA) + (size_t)(row * 6144 + rest * 128); pfstep = 64 * 6144; } }
        if (lane < 16) touch_lines64(pfb + (size_t)(2 + (lane >> 2)) * pfstep, lds0 + P1_DUMMY);
        const char* pfp = pfb + (size_t)(lane < 32 ? 6 + (lane >> 2) : (lane & 1)) * pfstep;
#define P1_PREF1() do { if (lane < 8) touch_lines64(pfp, lds0 + P1_DUMMY); if (lane < 32) pfp += 2 * (size_t)pfstep; } while (0)
#define P1_PREF2(t) do { if ((lane >> 1) == 16) touch_lines64(pfp, lds0 + P1_DUMMY); if ((t) < 12) pfp += 256; } while (0)
        asm volatile("s_load_dwordx2 %0, %1, 0x0\n\ts_waitcnt lgkmcnt(0)" : "=s"(tag) : "s"(ws + WS_EPOCH) : "memory");
        tag += 1;
        asm volatile("s_waitcnt vmcnt(14)" : "+v"(ga0), "+v"(ga1), "+v"(gg0), "+v"(gg1) :: "memory");
        {
            float s = 0.f;
#pragma unroll
            for (int i = 0; i < 4; ++i) {
                s += __uint_as_float(ga0[i] << 16) * __uint_as_float(gg0[i] << 16) + __uint_as_float(ga0[i] & 0xffff0000u) * __uint_as_float(gg0[i] & 0xffff0000u);
                s += __uint_as_float(ga1[i] << 16) * __uint_as_float(gg1[i] << 16) + __uint_as_float(ga1[i] & 0xffff0000u) * __uint_as_float(gg1[i] & 0xffff0000u);
            }
#pragma unroll
            for (int o = 32; o > 0; o >>= 1) s += __shfl_xor(s, o);
            if (lane == 0) __hip_atomic_store((gu32*)(ws + WS_G) + gtok, __float_as_uint(1.0f / (1.0f + __expf(-s))), RLX_AGENT);
        }
        int xa[2];
        { const int row = wm * 64 + 16 * (i16 >> 3) + (i16 & 7); const int sw = ((row >> 1) & 3) | (((row >> 4) & 1) << 2);
          xa[0] = row * 128 + ((g4 ^ sw) * 16); xa[1] = row * 128 + (((4 + g4) ^ sw) * 16); }
        int wa[2];
#pragma unroll
        for (int np = 0; np < 2; ++np) wa[np] = P1_WR + br * 3 * 4096 + (8 * g4 + q4) * 128 + (((hh * 4 + np * 2 + (p4 >> 1)) ^ wf_f) * 16) + (p4 & 1) * 8;
        f32x4 acc[4][6];
#pragma unroll
        for (int m = 0; m < 4; ++m)
#pragma unroll
            for (int n = 0; n < 6; ++n) acc[m][n] = f32x4{};
#define P1_LX(F, m, xo, kh) F.x[m] = LDSV(L + (xo) + xa[kh] + ((m) >> 1) * 4096 + ((m) & 1) * 1024)
#define P1_LWL(F, n, wo) F.wl[n] = trread(L + (wo) + wa[(n) & 1] + ((n) >> 1) * 4096)
#define P1_LWH(F, n, wo) F.wh[n] = trread(L + (wo) + wa[(n) & 1] + ((n) >> 1) * 4096 + 512)
#define P1_M(F, m, n) do { acc[m][n] = ((n) < 4) ? MFMA16(WFR(F, n), F.x[m], acc[m][n]) : MFMA16(F.x[m], WFR(F, n), acc[m][n]); } while (0)
#define P1_STEP(FC, FN, LD, xo, kh, wo, G0, G1, G2, G3) do { \
            P1_M(FC, 0, 0); if (LD) P1_LX(FN, 0, xo, kh); SBAR();  P1_M(FC, 1, 0); if (LD) P1_LWL(FN, 0, wo); SBAR(); \
            P1_M(FC, 2, 0); if (LD) P1_LWH(FN, 0, wo); SBAR();     P1_M(FC, 3, 0); G0; SBAR(); \
            P1_M(FC, 0, 1); if (LD) P1_LWL(FN, 1, wo); SBAR();     P1_M(FC, 1, 1); if (LD) P1_LWH(FN, 1, wo); SBAR(); \
            P1_M(FC, 2, 1); if (LD) P1_LX(FN, 1, xo, kh); SBAR();  P1_M(FC, 3, 1); if (LD) P1_LWL(FN, 2, wo); SBAR(); \
            P1_M(FC, 0, 2); if (LD) P1_LWH(FN, 2, wo); SBAR();     P1_M(FC, 1, 2); G1; SBAR(); \
            P1_M(FC, 2, 2); if (LD) P1_LWL(FN, 3, wo); SBAR();     P1_M(FC, 3, 2); if (LD) P1_LWH(FN, 3, wo); SBAR(); \
            P1_M(FC, 0, 3); if (LD) P1_LX(FN, 2, xo, kh); SBAR();  P1_M(FC, 1, 3); if (LD) P1_LWL(FN, 4, wo); SBAR(); \
            P1_M(FC, 2, 3); if (LD) P1_LWH(FN, 4, wo); SBAR();     P1_M(FC, 3, 3); G2; SBAR(); \
            P1_M(FC, 0, 4); if (LD) P1_LWL(FN, 5, wo); SBAR();     P1_M(FC, 1, 4); if (LD) P1_LWH(FN, 5, wo); SBAR(); \
            P1_M(FC, 2, 4); if (LD) P1_LX(FN, 3, xo, kh); SBAR();  P1_M(FC, 3, 4); SBAR(); \
            P1_M(FC, 0, 5); G3; SBAR();                            P1_M(FC, 1, 5); SBAR(); \
            P1_M(FC, 2, 5); SBAR();                                P1_M(FC, 3, 5); SBAR(); \
        } while (0)
        WAIT_BAR(6);
        STAMP(1);
        P1Frags FA, FB;
#pragma unroll
        for (int m = 0; m < 4; ++m) P1_LX(FA, m, 0, 0);
#pragma unroll
        for (int n = 0; n < 6; ++n) { P1_LWL(FA, n, 0); P1_LWH(FA, n, 0); }
        int xcur = 0, xnxt = 16384, xfree = 32768;
#define P1_TILE(t, GW0, GX0, GW1, GX1, PF, N0, N1, LASTT) do { \
            const int w1 = ((t) & 1) * 2 * P1_WSLOT + P1_WSLOT, wn0 = (((t) + 1) & 1) * 2 * P1_WSLOT; \
            P1_STEP(FA, FB, true, xcur, 1, w1, if (GW0) P1_W(2 * (t) + 3, 0), if (GW0) P1_W(2 * (t) + 3, 1), if (GW0) P1_W(2 * (t) + 3, 2), if (GX0) P1_X((t) + 2, xfree, 0)); \
            WAIT_BAR(N0); \
            P1_STEP(FB, FA, !(LASTT), xnxt, 0, wn0, if (GW1) P1_W(2 * (t) + 4, 0), if (GW1) P1_W(2 * (t) + 4, 1), if (GW1) P1_W(2 * (t) + 4, 2), if (GX1) P1_X((t) + 2, xfree, 1)); \
            PF; \
            if (!(LASTT)) { WAIT_BAR(N1); } \
            { const int tmp = xcur; xcur = xnxt; xnxt = xfree; xfree = tmp; } \
        } while (0)
#pragma unroll 1
        for (int t = 0; t < 5; ++t) P1_TILE(t, true, true, true, true, P1_PREF1(), 5, 5, false);
#pragma unroll 1
        for (int t = 5; t < 14; ++t) P1_TILE(t, true, true, true, true, P1_PREF2(t), 5, 5, false);
        P1_TILE(14, true, false, false, false, (void)0, 4, 0, false);
        P1_TILE(15, false, false, false, false, (void)0, 0, 0, true);
#undef P1_TILE
#undef P1_STEP
#undef P1_M
#undef P1_LX
#undef P1_LWL
#undef P1_LWH
#undef P1_X
#undef P1_W
        STAMP(2);
        LBAR();
        const int h = hp * 2 + hh;
        const int so = (g4 >> 1) * 1024 + (g4 & 1) * 512;
        const int kvoK = so + (16 * (i16 >> 3) + (i16 & 7)) * 16, kvoV = 2048 + so + i16 * 16;
#pragma unroll
        for (int T = 0; T < 2; ++T) {
            const int tl = wm * 2 + T;
            const unsigned kvg = WS_KV + (unsigned)((br * 32 + h) * 64 + qb * 4 + tl) * 4096;
            const lds_ptr kvl = LW + (br ? P2_BOWN + hh * 16384 + tl * 4096 : hh * 49152 + (8 + tl) * 4096);
            const lds_ptr qxl = LW + P2_QX + ((br * 2 + hh) * 4 + tl) * 2048;
#pragma unroll
            for (int mp = 0; mp < 2; ++mp) {
                const u32x4 kf = pack44n(acc[2 * T + mp][2], acc[2 * T + mp][3]);
                st_sc1(rs, kvg + kvoK + mp * 128, kf); LDSW(kvl + kvoK + mp * 128) = kf;
            }
#pragma unroll
            for (int np = 0; np < 2; ++np) {
                const u32x4 vf = pack44n(acc[2 * T][4 + np], acc[2 * T + 1][4 + np]);
                st_sc1(rs, kvg + kvoV + np * 256, vf); LDSW(kvl + kvoV + np * 256) = vf;
            }
#pragma unroll
            for (int mp = 0; mp < 2; ++mp) LDSW(qxl + kvoK + mp * 128) = pack44(acc[2 * T + mp][0], acc[2 * T + mp][1], QSCALE);
        }
        LBAR();
        STAMP(3);
    }

    const int hh = wid >> 2, tt = wid & 3, h = hp * 2 + hh;
    const int qtile = qb * 4 + tt;
    const char* kvA = ws + WS_KV + (size_t)(h * 64 + tt) * 4096;
    const char* kvB = kvA + (size_t)32 * 64 * 4096;
    const unsigned voff = lane * 16;
#define COPY4(src, dst) do { glds16((src), voff, (dst)); glds16((src) + 1024, voff, (dst) + 1024); glds16((src) + 2048, voff, (dst) + 2048); glds16((src) + 3072, voff, (dst) + 3072); } while (0)
    const bf16x8 qa0 = LDSV(L + P2_QX + (hh * 4 + tt) * 2048 + lane * 16), qa1 = LDSV(L + P2_QX + (hh * 4 + tt) * 2048 + 1024 + lane * 16);
    const bf16x8 qb0 = LDSV(L + P2_QX + ((2 + hh) * 4 + tt) * 2048 + lane * 16), qb1 = LDSV(L + P2_QX + ((2 + hh) * 4 + tt) * 2048 + 1024 + lane * 16);

    const int rb = 4 * (xcd >> 1) + (jj & 3), cb = 8 * (xcd & 1) + (jj >> 2);
    const size_t p3off = (size_t)((wid >> 2) * 512 + (wid & 3) * 32) * 2048 + cb * 128;
    const unsigned p3vw = (unsigned)((lane >> 3) * 2048 + (((lane & 7) ^ (((lane >> 4) & 3) << 1)) * 16));
    const unsigned osrc = WS_O + (unsigned)((rb * 4 + (wid & 3)) * 64 + (wid >> 2) * 16) * 2048 + (g4 >> 1) * 1024 + ((g4 & 1) * 32 + i16) * 16;
    u32x4 o0[8], o1[8], o2[8]; float g, g2;
#define P3_LDO(OF, c) do { _Pragma("unroll") for (int i = 0; i < 8; ++i) OF[i] = ld_sc1(rs, osrc + (P3BR(c) * 32 + ((c) & 3) * 4 + (i >> 1)) * 2048 + (i & 1) * 256); } while (0)
#define P3_SLOT(c) (((c) % 5) == 0 ? 65536 : ((c) % 5) == 1 ? 32768 : ((c) % 5) == 2 ? 131072 : ((c) % 5) == 3 ? 98304 : 0)
#define P3_STAGE(c) do { const char* src_ = (((c) >> 2) ? woB : woA) + p3off + (size_t)((c) & 3) * (128 * 2048); const unsigned dst_ = lds0 + P3_SLOT(c) + wid * 4096; \
        glds16(src_, p3vw, dst_); glds16(src_ + 8 * 2048, p3vw, dst_ + 1024); glds16(src_ + 16 * 2048, p3vw, dst_ + 2048); glds16(src_ + 24 * 2048, p3vw, dst_ + 3072); } while (0)
    {
        bf16x8 sel;
        { const short one = (((lane >> 4) & 1) == ((lane >> 2) & 1)) ? (short)0x3F80 : (short)0;
#pragma unroll
          for (int i = 0; i < 8; ++i) sel[i] = one; }
        f32x16 SA, SB, negm, oa = f32x16{}, ob = f32x16{};
        f32x4 la = f32x4{}, lb = f32x4{};
        Fr2 kA, kB, vA, vB; u32x4 pw0, pw1;
        const lds_cptr hb = L + hh * 16384 + lane * 16;
        float mB;
#define A_RUN(pf, pn_, n_) do { \
            int n = (n_); lds_cptr p1 = n == 0 ? (pf) : (pn_); lds_cptr p2 = n <= 1 ? p1 : p1 - 4096; \
            kA.a = LDSV(p1); kA.b = LDSV(p1 + 1024); ZERO_P(); vA = kA; \
            ATT_STEP(SA, SB, negm, qa0, qa1, oa, la, kA, kB, vA, vB, p2, (pf), (void)0); \
            while (n >= 2) { \
                const lds_cptr p3 = n > 2 ? p2 - 4096 : p2; \
                ATT_STEP(SB, SA, negm, qa0, qa1, oa, la, kB, kA, vB, vA, p3, p1, (void)0); \
                const lds_cptr p4 = n > 3 ? p3 - 4096 : p3; \
                ATT_STEP(SA, SB, negm, qa0, qa1, oa, la, kA, kB, vA, vB, p4, p2, (void)0); \
                p1 = p3; p2 = p4; n -= 2; \
            } \
            if (n == 1) { ATT_STEP(SB, SA, negm, qa0, qa1, oa, la, kB, kA, vB, vA, p1, p1, (void)0); ATT_DRAIN(oa, la, vA); } \
            else { ATT_DRAIN(oa, la, vB); } \
        } while (0)
        const lds_cptr ab = L + hh * 49152 + (8 + tt) * 4096 + lane * 16;
        float mA;
        {
            kA.a = LDSV(ab); kA.b = LDSV(ab + 1024);
            SA = MFMA32(kA.a, qa0, f32x16{}); SA = MFMA32(kA.b, qa1, SA);
            apply_mask<1>(SA, r32, hi);
            mA = rowmax16(SA);
#pragma unroll
            for (int r = 0; r < 16; ++r) { SA[r] -= mA; negm[r] = -mA; }
            A_RUN(ab, ab - 4096, tt);
        }
        WAIT_BAR(0);
        if (tid == 0) __hip_atomic_store((gu64*)(ws + WS_FLAG1 + (hp * 16 + qb) * 128), tag, RLX_AGENT);
        STAMP(4);
        const unsigned kvAo = WS_KV + (unsigned)(h * 64 + tt) * 4096 + lane * 16, kvBo = kvAo + 32u * 64 * 4096;
        const int b2 = qb >= 2 ? qb - 2 : qb, b1 = qb >= 1 ? qb - 1 : qb;
        u32x4 st0[4], st1[4];
        {
            const lds_cptr cur = hb + P2_BOWN;
            kA.a = LDSV(cur); kA.b = LDSV(cur + 1024);
            SA = MFMA32(kA.a, qb0, f32x16{}); SA = MFMA32(kA.b, qb1, SA);
            mB = rowmax16(SA);
#pragma unroll
            for (int r = 0; r < 16; ++r) { SA[r] -= mB; negm[r] = -mB; }
            kA.a = LDSV(cur + 4096); kA.b = LDSV(cur + 4096 + 1024);
            ZERO_P(); vA = kA;
            ATT_STEP(SA, SB, negm, qb0, qb1, ob, lb, kA, kB, vA, vB, cur + 2 * 4096, cur, (void)0);
            ATT_STEP(SB, SA, negm, qb0, qb1, ob, lb, kB, kA, vB, vA, cur + 3 * 4096, cur + 1 * 4096, (void)0);
            {
                gu64* f0 = (gu64*)(ws + WS_FLAG1 + (hp * 16 + ((lane & 1) ? b1 : b2)) * 128);
                for (unsigned spins = 0; spins < (1u << 18); ++spins) {
                    const unsigned long long v = __hip_atomic_load(f0, RLX_AGENT);
                    if (__all(v == tag)) break;
                    __builtin_amdgcn_s_sleep(1);
                }
#pragma unroll
                for (int i = 0; i < 4; ++i) st0[i] = ld_sc1(rs, kvAo + b1 * 16384 + i * 1024);
#pragma unroll
                for (int i = 0; i < 4; ++i) st1[i] = ld_sc1(rs, kvAo + b2 * 16384 + i * 1024);
            }
            ATT_STEP(SA, SB, negm, qb0, qb1, ob, lb, kA, kB, vA, vB, cur, cur + 2 * 4096, (void)0);
            ATT_STEP(SB, SA, negm, qb0, qb1, ob, lb, kB, kA, vB, vA, cur, cur + 3 * 4096, (void)0);
            ATT_DRAIN(ob, lb, vA);
        }
        STAMP(5);
        {
            const lds_ptr ad = LW + hh * 49152 + tt * 4096 + lane * 16;
#pragma unroll
            for (int i = 0; i < 4; ++i) LDSW(ad + 4 * 4096 + i * 1024) = st0[i];
#pragma unroll
            for (int i = 0; i < 4; ++i) LDSW(ad + i * 1024) = st1[i];
        }
        wait16(ws + WS_FLAG1 + hp * 2048, tag, lane);
        if (wid == 0) __builtin_amdgcn_fence(__ATOMIC_ACQUIRE, "agent");
#pragma unroll
        for (int i = 0; i < 4; ++i) st0[i] = ld_sc1(rs, kvBo + ((qb + 1) & 15) * 16384 + i * 1024);
#pragma unroll
        for (int i = 0; i < 4; ++i) st1[i] = ld_sc1(rs, kvBo + ((qb + 2) & 15) * 16384 + i * 1024);
        LBAR();
        STAMP(6);
        if (qb >= 1) {
            const bool far = qb >= 2;
            const lds_cptr p7 = L + hh * 49152 + 7 * 4096 + lane * 16;
            const lds_cptr pf = far ? ab - 8 * 4096 : p7;
#pragma unroll
            for (int r = 0; r < 16; ++r) negm[r] = -mA;
            f32x16 c1 = negm;
            if (far) apply_mask<2>(c1, r32, hi);
            kA.a = LDSV(pf); kA.b = LDSV(pf + 1024);
            SA = MFMA32(kA.a, qa0, c1); SA = MFMA32(kA.b, qa1, SA);
            A_RUN(pf, far ? p7 : p7 - 4096, far ? 7 - tt : 3);
        }
#undef A_RUN
#pragma unroll
        for (int i = 0; i < 4; ++i) { LDSW(LW + 131072 + wid * 4096 + i * 1024 + lane * 16) = st0[i]; LDSW(LW + 98304 + wid * 4096 + i * 1024 + lane * 16) = st1[i]; }
        {
            const unsigned oap = WS_O + (unsigned)(qtile * 64 + h) * 2048 + lane * 16;
            if (__all(finite_state(oa, la[0]))) {
                const float inv = __builtin_amdgcn_rcpf(la[0]);
                st_sc1(rs, oap, pack8(oa, 0, inv)); st_sc1(rs, oap + 1024, pack8(oa, 1, inv));
            } else {
                attn_slow(rs, WS_KV + (unsigned)h * (64 * 4096), qtile >= 8 ? qtile - 8 : 0, qtile, qtile, qtile - 8, qa0, qa1, oap, lane);
            }
        }
        WAIT_BAR_L(2);
        STAMP(7);
        P3_STAGE(0);
        COPY4(kvB + ((qb + 3) & 15) * 16384, lds0 + 0 + wid * 4096);
        {
            int so0 = 131072, so1 = 98304, so2 = 0, so3 = 32768;
            lds_cptr cur = hb + so0;
#pragma unroll
            for (int r = 0; r < 16; ++r) negm[r] = -mB;
            kA.a = LDSV(cur); kA.b = LDSV(cur + 1024);
            SA = MFMA32(kA.a, qb0, negm); SA = MFMA32(kA.b, qb1, SA);
            kA.a = LDSV(cur + 4096); kA.b = LDSV(cur + 4096 + 1024);
            ZERO_P(); vA = kA;
            const unsigned long long wo1 = (unsigned long long)(woA + p3off) - ((unsigned long long)12 << 18);
#define P2_BODY(s, PRE) do { \
                const bool kv = (s) < 13; \
                unsigned long long pk_ = (unsigned long long)kvB + (unsigned)(((qb + (s) + 3) & 15) << 14), pw_ = wo1 + ((unsigned long long)(unsigned)(s) << 18); \
                asm volatile("" : "+s"(pk_), "+s"(pw_)); \
                const char* sb_ = (const char*)(kv ? pk_ : pw_); \
                const unsigned sst = kv ? 1024 : 8 * 2048, sv = kv ? voff : p3vw, db_ = lds0 + so3 + wid * 4096; \
                const lds_cptr nxt = hb + so1; \
                PRE; \
                ATT_STEP(SA, SB, negm, qb0, qb1, ob, lb, kA, kB, vA, vB, cur + 2 * 4096, cur, glds16(sb_, sv, db_)); \
                ATT_STEP(SB, SA, negm, qb0, qb1, ob, lb, kB, kA, vB, vA, cur + 3 * 4096, cur + 1 * 4096, glds16(sb_ + sst, sv, db_ + 1024)); \
                ATT_STEP(SA, SB, negm, qb0, qb1, ob, lb, kA, kB, vA, vB, nxt, cur + 2 * 4096, glds16(sb_ + 2 * sst, sv, db_ + 2048)); \
                ATT_STEP(SB, SA, negm, qb0, qb1, ob, lb, kB, kA, vB, vA, nxt + 4096, cur + 3 * 4096, glds16(sb_ + 3 * sst, sv, db_ + 3072)); \
                cur = nxt; { const int t_ = so0; so0 = so1; so1 = so2; so2 = so3; so3 = t_; } \
                WAIT_BAR_L(4); \
            } while (0)
            for (int s = 1; s < 10; ++s) {
                P2_BODY(s, if (__builtin_expect(s == 9 && wid == 0, 0)) wait16(ws + WS_FLAG2 + rb * 2048, tag, lane));
                if (__builtin_expect(s == 1 && wid == 0, 0)) { if (lane == 0) __hip_atomic_store((gu64*)(ws + WS_FLAG2 + (qb * 16 + hp) * 128), tag, RLX_AGENT); }
            }
            if (blockIdx.x == 0 && tid == 0) __hip_atomic_store((gu64*)(ws + WS_EPOCH), tag, RLX_AGENT);
            P2_BODY(10, P3_LDO(o0, 0));
            P2_BODY(11, P3_LDO(o1, 1));
            P2_BODY(12, P3_LDO(o2, 2));
            for (int s = 13; s < 16; ++s) P2_BODY(s, (void)0);
#undef P2_BODY
            ATT_DRAIN(ob, lb, vA);
        }
        STAMP(8);
        g = __uint_as_float(__hip_atomic_load((gu32*)(ws + WS_G) + rb * 128 + (wid & 3) * 32 + i16, RLX_AGENT));
        g2 = __uint_as_float(__hip_atomic_load((gu32*)(ws + WS_G) + rb * 128 + (wid & 3) * 32 + 16 + i16, RLX_AGENT));
        const unsigned obp = WS_O + (unsigned)(qtile * 64 + 32 + h) * 2048 + lane * 16;
        if (__all(finite_state(ob, lb[0]))) {
            const float inv = __builtin_amdgcn_rcpf(lb[0]);
            st_sc1(rs, obp, pack8(ob, 0, inv)); st_sc1(rs, obp + 1024, pack8(ob, 1, inv));
        } else {
            attn_slow(rs, WS_KV + (unsigned)(32 + h) * (64 * 4096), 0, 63, -1, -1, qb0, qb1, obp, lane);
        }
    }

    {
        const int wt = wid & 3, kh = wid >> 2;
        WAIT_BAR_L(4);
        P3_STAGE(4);
        STAMP(9);
        int wa[4];
        { const int f = (2 * (g4 & 1) + (q4 >> 1)) << 1; const int row = 16 * (g4 >> 1) + 4 * (g4 & 1) + q4;
#pragma unroll
          for (int n = 0; n < 4; ++n) wa[n] = kh * 16384 + row * 128 + (((2 * n + (p4 >> 1)) ^ f) * 16) + (p4 & 1) * 8; }
        f32x4 accA[2][4], accB[2][4];
#pragma unroll
        for (int t = 0; t < 2; ++t)
#pragma unroll
            for (int n = 0; n < 4; ++n) { accA[t][n] = f32x4{}; accB[t][n] = f32x4{}; }
        bf16x8 wfA[4], wfB[4];
#define P3_HEAD(ACC, WC, WN, PN, OA, OB) do { \
            _Pragma("unroll") for (int n = 0; n < 4; ++n) { \
                WN[n] = trfrag(L + (PN) + wa[n], 1024); \
                ACC[0][n] = MFMA16(WC[n], BC8(OA), ACC[0][n]); ACC[1][n] = MFMA16(WC[n], BC8(OB), ACC[1][n]); PIN(ACC[0][n]); PIN(ACC[1][n]); SBAR(); } \
        } while (0)
#define P3_COMP(ACC, OF, c, cn) do { \
            P3_HEAD(ACC, wfA, wfB, P3_SLOT(c) + 1 * 4096, OF[0], OF[1]); P3_HEAD(ACC, wfB, wfA, P3_SLOT(c) + 2 * 4096, OF[2], OF[3]); \
            P3_HEAD(ACC, wfA, wfB, P3_SLOT(c) + 3 * 4096, OF[4], OF[5]); P3_HEAD(ACC, wfB, wfA, P3_SLOT(cn), OF[6], OF[7]); \
        } while (0)
#pragma unroll
        for (int n = 0; n < 4; ++n) wfA[n] = trfrag(L + P3_SLOT(0) + wa[n], 1024);
        P3_COMP(accA, o0, 0, 1);
        WAIT_BAR_L(4);
        if (tid == 0) __hip_atomic_store((gu64*)(ws + WS_FLAG3 + (qb * 16 + hp) * 128), tag, RLX_AGENT);
        P3_STAGE(5); P3_LDO(o0, 3);
        P3_COMP(accA, o1, 1, 2);
        LBAR(); P3_STAGE(6);
        P3_COMP(accA, o2, 2, 3);
        WAIT_BAR_L(16);
        P3_STAGE(7);
        P3_HEAD(accA, wfA, wfB, P3_SLOT(3) + 1 * 4096, o0[0], o0[1]); P3_HEAD(accA, wfB, wfA, P3_SLOT(3) + 2 * 4096, o0[2], o0[3]);
        P3_HEAD(accA, wfA, wfB, P3_SLOT(3) + 3 * 4096, o0[4], o0[5]); P3_HEAD(accA, wfB, wfA, P3_SLOT(3), o0[6], o0[7]);
        WAIT_BAR_L(0);
        STAMP(10);
        auto wait_group = [&](unsigned a_) {
            gu64* f3 = (gu64*)(ws + WS_FLAG3 + rb * 2048 + (kh * 8 + a_ * 2 + (lane & 1)) * 128);
            for (unsigned spins = 0; spins < (1u << 18); ++spins) {
                const unsigned long long v = __hip_atomic_load(f3, RLX_AGENT);
                if (__all(v == tag)) break;
                __builtin_amdgcn_s_sleep(1);
            }
        };
#define P3_LDB(OF, a_) do { _Pragma("unroll") for (int i = 0; i < 8; ++i) \
            OF[i] = __builtin_amdgcn_raw_buffer_load_b128(rs, (int)(osrc + (32 + (i >> 1)) * 2048 + (i & 1) * 256), (int)((a_) * 8192), 16); } while (0)
#define P3_SLOTB(a_) ((a_) == 0 ? P3_SLOT(4) : (a_) == 1 ? P3_SLOT(5) : (a_) == 2 ? P3_SLOT(6) : P3_SLOT(7))
#define P3_COMPB(OF, a_) do { const int sl_ = P3_SLOTB(a_); \
            _Pragma("unroll") for (int n = 0; n < 4; ++n) wfA[n] = trfrag(L + sl_ + wa[n], 1024); \
            P3_HEAD(accB, wfA, wfB, sl_ + 1 * 4096, OF[0], OF[1]); P3_HEAD(accB, wfB, wfA, sl_ + 2 * 4096, OF[2], OF[3]); \
            P3_HEAD(accB, wfA, wfB, sl_ + 3 * 4096, OF[4], OF[5]); P3_HEAD(accB, wfB, wfA, sl_, OF[6], OF[7]); \
        } while (0)
        const unsigned a0 = (xcd + 1) & 3, a1 = (xcd + 2) & 3, a2 = (xcd + 3) & 3, a3 = xcd & 3;
        wait_group(a0); P3_LDB(o1, a0);
        wait_group(a1); P3_LDB(o2, a1);
        P3_COMPB(o1, a0);
        wait_group(a2); P3_LDB(o1, a2);
        P3_COMPB(o2, a1);
        wait_group(a3); P3_LDB(o2, a3);
        P3_COMPB(o1, a2);
        P3_COMPB(o2, a3);
#undef P3_LDB
#undef P3_COMPB
#undef P3_LDO
#undef P3_COMP
#undef P3_HEAD
        STAMP(11);
        LBAR();
        LAS f32x4* ex = (LAS f32x4*)(LW + wid * 4096) + lane;
        f32x4 mix[2][4];
#pragma unroll
        for (int t = 0; t < 2; ++t)
#pragma unroll
            for (int n = 0; n < 4; ++n) { const float gt = t ? g2 : g; mix[t][n] = accA[t][n] * gt + accB[t][n] * (1.0f - gt); }
#pragma unroll
        for (int t = 0; t < 2; ++t)
#pragma unroll
            for (int n = 0; n < 2; ++n) ex[(t * 2 + n) * 64] = kh ? mix[t][n] : mix[t][2 + n];
        LBAR();
        const LAS f32x4* ey = (const LAS f32x4*)(LW + (wid ^ 4) * 4096) + lane;
#pragma unroll
        for (int t = 0; t < 2; ++t)
#pragma unroll
            for (int n = 0; n < 2; ++n) {
                const f32x4 y = ey[(t * 2 + n) * 64] + (kh ? mix[t][2 + n] : mix[t][n]);
                unsigned short* op = out + (size_t)(rb * 128 + wt * 32 + t * 16 + i16) * DM + cb * 64 + (2 * kh + n) * 16 + 4 * g4;
                *(u32x2*)op = (u32x2){cvtpk(y[0], y[1]), cvtpk(y[2], y[3])};
            }
        STAMP(12);
    }
}

extern "C" void kernel_launch(void* const* d_in, const int* in_sizes, int n_in, void* d_out, int out_size, void* d_ws, size_t ws_size,
                              hipStream_t stream) {
    (void)hipFuncSetAttribute((const void*)fused_kernel, hipFuncAttributeMaxDynamicSharedMemorySize, LDS_BYTES);
    fused_kernel<<<256, 512, LDS_BYTES, stream>>>((const char*)d_in[0], (const char*)d_in[1], (const char*)d_in[2], (const char*)d_in[4], (const char*)d_in[5],
                                                  (const unsigned short*)d_in[6], (char*)d_ws, (unsigned short*)d_out);
}
```

```cpp
#include <hip/hip_runtime.h>
#include <hip/hip_bf16.h>
#include <stdint.h>

#define LAS __attribute__((address_space(3)))
typedef short bf16x8 __attribute__((ext_vector_type(8)));
typedef short s16x4 __attribute__((ext_vector_type(4)));
typedef float f32x16 __attribute__((ext_vector_type(16)));
typedef float f32x4 __attribute__((ext_vector_type(4)));
typedef float f32x2 __attribute__((ext_vector_type(2)));
typedef unsigned u32x4 __attribute__((ext_vector_type(4)));
typedef unsigned u32x2 __attribute__((ext_vector_type(2)));
typedef __bf16 bf16x2_t __attribute__((ext_vector_type(2)));
typedef LAS const char* lds_cptr;
typedef LAS char* lds_ptr;
typedef __attribute__((address_space(1))) unsigned long long gu64;
typedef __attribute__((address_space(1))) unsigned gu32;

#define MFMA32(a, b, c) __builtin_amdgcn_mfma_f32_32x32x16_bf16(a, b, c, 0, 0, 0)
#define MFMA16(a, b, c) __builtin_amdgcn_mfma_f32_16x16x32_bf16(a, b, c, 0, 0, 0)
#define SBAR() __builtin_amdgcn_sched_barrier(0)
#define PIN(x) asm volatile("" : "+v"(x))
#define WAIT_BAR(N) asm volatile("s_waitcnt vmcnt(" #N ")\n\ts_barrier" ::: "memory")
#define WAIT_BAR_L(N) asm volatile("s_waitcnt vmcnt(" #N ") lgkmcnt(0)\n\ts_barrier" ::: "memory")
#define LBAR() asm volatile("s_waitcnt lgkmcnt(0)\n\ts_barrier" ::: "memory")
#define BAR() asm volatile("s_barrier" ::: "memory")
#define RLX_AGENT __ATOMIC_RELAXED, __HIP_MEMORY_SCOPE_AGENT

constexpr int DM = 1024;
constexpr float QSCALE = 0.17677669529663687f * 1.4426950408889634f;

constexpr unsigned WS_KV = 0;
constexpr unsigned WS_O = 16u << 20;
constexpr unsigned WS_G = 24u << 20;
constexpr unsigned WS_EPOCH = WS_G + 65536;
constexpr unsigned WS_FLAG1 = WS_EPOCH + 4096;
constexpr unsigned WS_FLAG2 = WS_FLAG1 + 32768;
constexpr unsigned WS_FLAG3 = WS_FLAG2 + 32768;
constexpr unsigned WS_USED = 32u << 20;
constexpr int LDS_BYTES = 163840;


__device__ __forceinline__ unsigned cvtpk(float lo, float hi) {
    f32x2 v = {lo, hi}; bf16x2_t b = __builtin_convertvector(v, bf16x2_t); return __builtin_bit_cast(unsigned, b);
}
__device__ __forceinline__ void glds16(const void* sbase, unsigned voff, unsigned lds_dst) {
    asm volatile("s_mov_b32 m0, %2\n\ts_nop 0\n\tglobal_load_lds_dwordx4 %0, %1" : : "v"(voff), "s"(sbase), "s"(lds_dst) : "memory");
}
__device__ __forceinline__ void touch_lines64(const void* vaddr, unsigned lds_dst) {
    asm volatile("s_mov_b32 m0, %1\n\ts_nop 0\n\tglobal_load_lds_dword %0, off" : : "v"(vaddr), "s"(lds_dst) : "memory");
}
__device__ __forceinline__ void st_sc1(__amdgpu_buffer_rsrc_t rs, unsigned off, u32x4 v) { __builtin_amdgcn_raw_buffer_store_b128(v, rs, (int)off, 0, 16); }
__device__ __forceinline__ u32x4 ld_sc1(__amdgpu_buffer_rsrc_t rs, unsigned off) { return __builtin_amdgcn_raw_buffer_load_b128(rs, (int)off, 0, 16); }
__device__ __forceinline__ void wait16(const char* base, unsigned long long tag, int lane) {
    gu64* f = (gu64*)(base + (lane & 15) * 128);
    for (unsigned spins = 0; spins < (1u << 18); ++spins) {
        const unsigned long long v = __hip_atomic_load(f, RLX_AGENT);
        if (__all(v == tag)) break;
        __builtin_amdgcn_s_sleep(1);
    }
}
__device__ __forceinline__ s16x4 trread(lds_cptr p) {
    typedef short v4i16_t __attribute__((ext_vector_type(4)));
    return __builtin_bit_cast(s16x4, __builtin_amdgcn_ds_read_tr16_b64_v4i16((LAS v4i16_t*)p));
}
__device__ __forceinline__ bf16x8 trfrag(lds_cptr p, int second_off) {
    const s16x4 lo = trread(p), hi = trread(p + second_off);
    return (bf16x8){lo[0], lo[1], lo[2], lo[3], hi[0], hi[1], hi[2], hi[3]};
}
__device__ __forceinline__ u32x4 pack8(const f32x16& a, int s, float sc) {
    u32x4 r;
    r[0] = cvtpk(a[8 * s + 0] * sc, a[8 * s + 1] * sc); r[1] = cvtpk(a[8 * s + 2] * sc, a[8 * s + 3] * sc);
    r[2] = cvtpk(a[8 * s + 4] * sc, a[8 * s + 5] * sc); r[3] = cvtpk(a[8 * s + 6] * sc, a[8 * s + 7] * sc);
    return r;
}
__device__ __forceinline__ u32x4 pack8n(const f32x16& a, int s) {
    u32x4 r;
    r[0] = cvtpk(a[8 * s + 0], a[8 * s + 1]); r[1] = cvtpk(a[8 * s + 2], a[8 * s + 3]);
    r[2] = cvtpk(a[8 * s + 4], a[8 * s + 5]); r[3] = cvtpk(a[8 * s + 6], a[8 * s + 7]);
    return r;
}
__device__ __forceinline__ u32x4 pack44(const f32x4& a, const f32x4& b, float sc) {
    return (u32x4){cvtpk(a[0] * sc, a[1] * sc), cvtpk(a[2] * sc, a[3] * sc), cvtpk(b[0] * sc, b[1] * sc), cvtpk(b[2] * sc, b[3] * sc)};
}
__device__ __forceinline__ u32x4 pack44n(const f32x4& a, const f32x4& b) {
    return (u32x4){cvtpk(a[0], a[1]), cvtpk(a[2], a[3]), cvtpk(b[0], b[1]), cvtpk(b[2], b[3])};
}
#define EXP2(v) __builtin_amdgcn_exp2f(v)
#define LDSV(p) (*(const LAS bf16x8*)(p))
#define LDSW(p) (*(LAS u32x4*)(p))
#define BC8(v) __builtin_bit_cast(bf16x8, v)

__device__ __forceinline__ float halfmax(float m) {
    auto rr = __builtin_amdgcn_permlane32_swap(__float_as_uint(m), __float_as_uint(m), false, false);
    return __builtin_fmaxf(__uint_as_float(rr[0]), __uint_as_float(rr[1]));
}
__device__ __forceinline__ float rowmax16(const f32x16& p) {
    float a = __builtin_fmaxf(p[0], p[1]);
#pragma unroll
    for (int r = 2; r < 16; ++r) a = __builtin_fmaxf(a, p[r]);
    return halfmax(a);
}
template <int MASK> __device__ __forceinline__ void apply_mask(f32x16& s, int r32, int hi) {
#pragma unroll
    for (int r = 0; r < 16; ++r) {
        const int kl = (r & 3) + 8 * (r >> 2) + 4 * hi;
        const bool keep = (MASK == 1) ? (kl <= r32) : (kl > r32);
        if (!keep) s[r] = -INFINITY;
    }
}
__device__ __forceinline__ void attn_slow(__amdgpu_buffer_rsrc_t rs, unsigned kvh, int t_first, int t_last, int t_diag, int t_far, bf16x8 q0, bf16x8 q1, unsigned odst, int lane) {
    const int r32 = lane & 31, hi = lane >> 5;
    f32x16 o = f32x16{}; float m = -INFINITY, l = 0.f;
    for (int t = t_last; t >= t_first; --t) {
        const unsigned p = kvh + (unsigned)t * 4096 + lane * 16;
        const bf16x8 k0 = BC8(ld_sc1(rs, p)), k1 = BC8(ld_sc1(rs, p + 1024)), v0 = BC8(ld_sc1(rs, p + 2048)), v1 = BC8(ld_sc1(rs, p + 3072));
        f32x16 s = MFMA32(k0, q0, f32x16{}); s = MFMA32(k1, q1, s);
        if (t == t_diag) apply_mask<1>(s, r32, hi);
        if (t == t_far) apply_mask<2>(s, r32, hi);
        const float mn = __builtin_fmaxf(m, rowmax16(s));
        const float ms = (mn == -INFINITY) ? 0.f : mn;
        const float alpha = EXP2(m - ms);
        float rs_ = 0.f;
#pragma unroll
        for (int r = 0; r < 16; ++r) { s[r] = EXP2(s[r] - ms); o[r] *= alpha; }
        const u32x4 p0 = pack8n(s, 0), p1 = pack8n(s, 1);
#pragma unroll
        for (int i = 0; i < 4; ++i) rs_ += __uint_as_float(p0[i] << 16) + __uint_as_float(p0[i] & 0xffff0000u) + __uint_as_float(p1[i] << 16) + __uint_as_float(p1[i] & 0xffff0000u);
        l = l * alpha + rs_; m = mn;
        o = MFMA32(v0, BC8(p0), o); o = MFMA32(v1, BC8(p1), o);
    }
    auto rr = __builtin_amdgcn_permlane32_swap(__float_as_uint(l), __float_as_uint(l), false, false);
    l = __uint_as_float(rr[0]) + __uint_as_float(rr[1]);
    const float inv = 1.0f / l;
    st_sc1(rs, odst, pack8(o, 0, inv)); st_sc1(rs, odst + 1024, pack8(o, 1, inv));
}
__device__ __forceinline__ bool finite_state(const f32x16& o, float l) {
    float a = 0.f, b = 0.f;
#pragma unroll
    for (int r = 0; r < 16; r += 2) { a = __builtin_fmaf(o[r], 0.f, a); b = __builtin_fmaf(o[r + 1], 0.f, b); }
    return (l < 1.0e30f) && (a + b == 0.f);
}
struct Fr2 { bf16x8 a, b; };
#define EX4(S, i) do { S[i] = EXP2(S[i]); S[(i) + 1] = EXP2(S[(i) + 1]); S[(i) + 2] = EXP2(S[(i) + 2]); S[(i) + 3] = EXP2(S[(i) + 3]); PIN(S); } while (0)
#define PK4(W, S, i) do { W[0] = cvtpk(S[i], S[(i) + 1]); W[1] = cvtpk(S[(i) + 2], S[(i) + 3]); W[2] = cvtpk(S[(i) + 4], S[(i) + 5]); W[3] = cvtpk(S[(i) + 6], S[(i) + 7]); PIN(W); } while (0)
#define ATT_STEP(SC, SN, CN, Q0, Q1, O, LS, KC, KN, VC, VN, KP, VP, G0) do { \
        SBAR(); \
        SN = MFMA32(KC.a, Q0, CN); PIN(SN); SBAR(); \
        KN.a = LDSV(KP); KN.b = LDSV((KP) + 1024); EX4(SC, 0); SBAR(); \
        O = MFMA32(VC.a, BC8(pw0), O); PIN(O); SBAR(); \
        VN.a = LDSV((VP) + 2048); VN.b = LDSV((VP) + 3072); EX4(SC, 4); SBAR(); \
        SN = MFMA32(KC.b, Q1, SN); PIN(SN); SBAR(); \
        EX4(SC, 8); SBAR(); \
        LS = MFMA16(sel, BC8(pw0), LS); PIN(LS); SBAR(); \
        PK4(pw0, SC, 0); SBAR(); \
        O = MFMA32(VC.b, BC8(pw1), O); PIN(O); SBAR(); \
        EX4(SC, 12); SBAR(); \
        LS = MFMA16(sel, BC8(pw1), LS); PIN(LS); SBAR(); \
        G0; PK4(pw1, SC, 8); SBAR(); \
    } while (0)
#define ATT_DRAIN(O, LS, VC) do { \
        O = MFMA32(VC.a, BC8(pw0), O); O = MFMA32(VC.b, BC8(pw1), O); \
        LS = MFMA16(sel, BC8(pw0), LS); LS = MFMA16(sel, BC8(pw1), LS); \
    } while (0)
#define ZERO_P() do { pw0 = u32x4{}; pw1 = u32x4{}; } while (0)

constexpr int P1_WR = 49152, P1_WSLOT = 24576, P1_DUMMY = 147456;
constexpr int P2_QX = 0;
constexpr int P2_BOWN = 98304;
struct P1Frags { bf16x8 x[4]; s16x4 wl[6], wh[6]; };
#define WFR(F, n) (bf16x8){F.wl[n][0], F.wl[n][1], F.wl[n][2], F.wl[n][3], F.wh[n][0], F.wh[n][1], F.wh[n][2], F.wh[n][3]}

__global__ __launch_bounds__(512, 2) void fused_kernel(const char* __restrict__ x, const char* __restrict__ wA, const char* __restrict__ woA,
                                                       const char* __restrict__ wB, const char* __restrict__ woB,
                                                       const unsigned short* __restrict__ gate_w, char* __restrict__ ws, unsigned short* __restrict__ out) {
    extern __shared__ __attribute__((aligned(16))) char lds[];
    const int tid = threadIdx.x, lane = tid & 63;
    const int wid = __builtin_amdgcn_readfirstlane(tid >> 6);
    const int xcd = blockIdx.x & 7, jj = blockIdx.x >> 3;
    const int hp = xcd * 2 + (jj & 1), qb = jj >> 1;
    const unsigned lds0 = (unsigned)(uintptr_t)lds;
    const lds_cptr L = (lds_cptr)lds;
    const lds_ptr LW = (lds_ptr)lds;
    const __amdgpu_buffer_rsrc_t rs = __builtin_amdgcn_make_buffer_rsrc((void*)ws, 0, (int)WS_USED, 0x00020000);
    const int i16 = lane & 15, g4 = lane >> 4, q4 = (lane >> 2) & 3, p4 = lane & 3;
    const int r32 = lane & 31, hi = lane >> 5;
    unsigned long long tag;

    const char* xs = x + (size_t)qb * 128 * 2048;
    unsigned vx[2];
#pragma unroll
    for (int p = 0; p < 2; ++p) {
        const int row = 16 * wid + 8 * p + (lane >> 3);
        const int sw = ((row >> 1) & 3) | (((row >> 4) & 1) << 2);
        vx[p] = (unsigned)(row * 2048 + (((lane & 7) ^ sw) * 16));
    }
    const unsigned xdst = lds0 + wid * 2048;
    const int wf_f = (((q4 >> 1) & 1) << 1) | ((g4 & 1) << 2);
    {
        const int wm = wid >> 2, wn = wid & 3, br = wn >> 1, hh = wn & 1;
        const int gtok = qb * 128 + hp * 8 + wid;
        u32x4 ga0, ga1, gg0, gg1;
        { const char* xr = x + (size_t)gtok * 2048; const unsigned go = lane * 32;
          asm volatile("global_load_dwordx4 %0, %4, %5\n\tglobal_load_dwordx4 %1, %4, %5 offset:16\n\tglobal_load_dwordx4 %2, %4, %6\n\tglobal_load_dwordx4 %3, %4, %6 offset:16"
                       : "=&v"(ga0), "=&v"(ga1), "=&v"(gg0), "=&v"(gg1) : "v"(go), "s"(xr), "s"(gate_w) : "memory"); }
        const char* wsrc = (wid < 4 ? wA : wB);
        unsigned vw[3];
#pragma unroll
        for (int i = 0; i < 3; ++i) {
            const int id = wid * 3 + i, sg = id >> 2, rg = id & 3, part = sg % 3;
            const int row = rg * 8 + (lane >> 3);
            const int f = (((row >> 1) & 1) << 1) | (((row >> 3) & 1) << 2);
            vw[i] = (unsigned)(row * 6144 + part * 2048 + hp * 128 + (((lane & 7) ^ f) * 16));
        }
        const unsigned wdst = lds0 + P1_WR + wid * 3072;
#define P1_X(T, slot, p) glds16(xs + (T) * 128, vx[p], xdst + (slot) + (p) * 1024)
#define P1_W(j, i) glds16(wsrc + (size_t)(j) * (32 * 6144), vw[i], wdst + ((j) & 3) * P1_WSLOT + (i) * 1024)
        P1_X(0, 0, 0); P1_X(0, 0, 1); P1_W(0, 0); P1_W(0, 1); P1_W(0, 2);
        P1_W(1, 0); P1_W(1, 1); P1_W(1, 2); P1_X(1, 16384, 0);
        P1_W(2, 0); P1_W(2, 1); P1_W(2, 2); P1_X(1, 16384, 1);
        const char* pfb; unsigned pfstep;
        { const int gw = blockIdx.x * 8 + wid, jl = lane & 3;
          if (lane >= 32) { pfb = ((gw >> 10) ? woB : woA) + (size_t)(gw & 1023) * 2048; pfstep = 128; }
          else if (jl == 0) { pfb = x + (size_t)gw * 2048; pfstep = 128; }
          else { const int id = gw * 3 + jl - 1, bq = id / 3072, rem = id % 3072, row = rem / 48, rest = rem % 48;
                 pfb = (bq ? wB : wA) + (size_t)(row * 6144 + rest * 128); pfstep = 64 * 6144; } }
        if (lane < 16) touch_lines64(pfb + (size_t)(2 + (lane >> 2)) * pfstep, lds0 + P1_DUMMY);
        const char* pfp = pfb + (size_t)(lane < 32 ? 6 + (lane >> 2) : (lane & 1)) * pfstep;
#define P1_PREF1() do { if (lane < 8) touch_lines64(pfp, lds0 + P1_DUMMY); if (lane < 32) pfp += 2 * (size_t)pfstep; } while (0)
#define P1_PREF2(t) do { if ((lane >> 1) == 16) touch_lines64(pfp, lds0 + P1_DUMMY); if ((t) < 12) pfp += 256; } while (0)
        asm volatile("s_load_dwordx2 %0, %1, 0x0\n\ts_waitcnt lgkmcnt(0)" : "=s"(tag) : "s"(ws + WS_EPOCH) : "memory");
        tag += 1;
        asm volatile("s_waitcnt vmcnt(14)" : "+v"(ga0), "+v"(ga1), "+v"(gg0), "+v"(gg1) :: "memory");
        {
            float s = 0.f;
#pragma unroll
            for (int i = 0; i < 4; ++i) {
                s += __uint_as_float(ga0[i] << 16) * __uint_as_float(gg0[i] << 16) + __uint_as_float(ga0[i] & 0xffff0000u) * __uint_as_float(gg0[i] & 0xffff0000u);
                s += __uint_as_float(ga1[i] << 16) * __uint_as_float(gg1[i] << 16) + __uint_as_float(ga1[i] & 0xffff0000u) * __uint_as_float(gg1[i] & 0xffff0000u);
            }
#pragma unroll
            for (int o = 32; o > 0; o >>= 1) s += __shfl_xor(s, o);
            if (lane == 0) __hip_atomic_store((gu32*)(ws + WS_G) + gtok, __float_as_uint(1.0f / (1.0f + __expf(-s))), RLX_AGENT);
        }
        int xa[2];
        { const int row = wm * 64 + 16 * (i16 >> 3) + (i16 & 7); const int sw = ((row >> 1) & 3) | (((row >> 4) & 1) << 2);
          xa[0] = row * 128 + ((g4 ^ sw) * 16); xa[1] = row * 128 + (((4 + g4) ^ sw) * 16); }
        int wa[2];
#pragma unroll
        for (int np = 0; np < 2; ++np) wa[np] = P1_WR + br * 3 * 4096 + (8 * g4 + q4) * 128 + (((hh * 4 + np * 2 + (p4 >> 1)) ^ wf_f) * 16) + (p4 & 1) * 8;
        f32x4 acc[4][6];
#pragma unroll
        for (int m = 0; m < 4; ++m)
#pragma unroll
            for (int n = 0; n < 6; ++n) acc[m][n] = f32x4{};
#define P1_LX(F, m, xo, kh) F.x[m] = LDSV(L + (xo) + xa[kh] + ((m) >> 1) * 4096 + ((m) & 1) * 1024)
#define P1_LWL(F, n, wo) F.wl[n] = trread(L + (wo) + wa[(n) & 1] + ((n) >> 1) * 4096)
#define P1_LWH(F, n, wo) F.wh[n] = trread(L + (wo) + wa[(n) & 1] + ((n) >> 1) * 4096 + 512)
#define P1_M(F, m, n) do { acc[m][n] = ((n) < 4) ? MFMA16(WFR(F, n), F.x[m], acc[m][n]) : MFMA16(F.x[m], WFR(F, n), acc[m][n]); } while (0)
#define P1_STEP(FC, FN, LD, xo, kh, wo, G0, G1, G2, G3) do { \
            P1_M(FC, 0, 0); if (LD) P1_LX(FN, 0, xo, kh); SBAR();  P1_M(FC, 1, 0); if (LD) P1_LWL(FN, 0, wo); SBAR(); \
            P1_M(FC, 2, 0); if (LD) P1_LWH(FN, 0, wo); SBAR();     P1_M(FC, 3, 0); G0; SBAR(); \
            P1_M(FC, 0, 1); if (LD) P1_LWL(FN, 1, wo); SBAR();     P1_M(FC, 1, 1); if (LD) P1_LWH(FN, 1, wo); SBAR(); \
            P1_M(FC, 2, 1); if (LD) P1_LX(FN, 1, xo, kh); SBAR();  P1_M(FC, 3, 1); if (LD) P1_LWL(FN, 2, wo); SBAR(); \
            P1_M(FC, 0, 2); if (LD) P1_LWH(FN, 2, wo); SBAR();     P1_M(FC, 1, 2); G1; SBAR(); \
            P1_M(FC, 2, 2); if (LD) P1_LWL(FN, 3, wo); SBAR();     P1_M(FC, 3, 2); if (LD) P1_LWH(FN, 3, wo); SBAR(); \
            P1_M(FC, 0, 3); if (LD) P1_LX(FN, 2, xo, kh); SBAR();  P1_M(FC, 1, 3); if (LD) P1_LWL(FN, 4, wo); SBAR(); \
            P1_M(FC, 2, 3); if (LD) P1_LWH(FN, 4, wo); SBAR();     P1_M(FC, 3, 3); G2; SBAR(); \
            P1_M(FC, 0, 4); if (LD) P1_LWL(FN, 5, wo); SBAR();     P1_M(FC, 1, 4); if (LD) P1_LWH(FN, 5, wo); SBAR(); \
            P1_M(FC, 2, 4); if (LD) P1_LX(FN, 3, xo, kh); SBAR();  P1_M(FC, 3, 4); SBAR(); \
            P1_M(FC, 0, 5); G3; SBAR();                            P1_M(FC, 1, 5); SBAR(); \
            P1_M(FC, 2, 5); SBAR();                                P1_M(FC, 3, 5); SBAR(); \
        } while (0)
        WAIT_BAR(6);
        P1Frags FA, FB;
#pragma unroll
        for (int m = 0; m < 4; ++m) P1_LX(FA, m, 0, 0);
#pragma unroll
        for (int n = 0; n < 6; ++n) { P1_LWL(FA, n, 0); P1_LWH(FA, n, 0); }
        int xcur = 0, xnxt = 16384, xfree = 32768;
#define P1_TILE(t, GW0, GX0, GW1, GX1, PF, N0, N1, LASTT) do { \
            const int w1 = ((t) & 1) * 2 * P1_WSLOT + P1_WSLOT, wn0 = (((t) + 1) & 1) * 2 * P1_WSLOT; \
            P1_STEP(FA, FB, true, xcur, 1, w1, if (GW0) P1_W(2 * (t) + 3, 0), if (GW0) P1_W(2 * (t) + 3, 1), if (GW0) P1_W(2 * (t) + 3, 2), if (GX0) P1_X((t) + 2, xfree, 0)); \
            WAIT_BAR(N0); \
            P1_STEP(FB, FA, !(LASTT), xnxt, 0, wn0, if (GW1) P1_W(2 * (t) + 4, 0), if (GW1) P1_W(2 * (t) + 4, 1), if (GW1) P1_W(2 * (t) + 4, 2), if (GX1) P1_X((t) + 2, xfree, 1)); \
            PF; \
            if (!(LASTT)) { WAIT_BAR(N1); } \
            { const int tmp = xcur; xcur = xnxt; xnxt = xfree; xfree = tmp; } \
        } while (0)
#pragma unroll 1
        for (int t = 0; t < 5; ++t) P1_TILE(t, true, true, true, true, P1_PREF1(), 5, 5, false);
#pragma unroll 1
        for (int t = 5; t < 14; ++t) P1_TILE(t, true, true, true, true, P1_PREF2(t), 5, 5, false);
        P1_TILE(14, true, false, false, false, (void)0, 4, 0, false);
        P1_TILE(15, false, false, false, false, (void)0, 0, 0, true);
#undef P1_TILE
#undef P1_STEP
#undef P1_M
#undef P1_LX
#undef P1_LWL
#undef P1_LWH
#undef P1_X
#undef P1_W
        LBAR();
        const int h = hp * 2 + hh;
        const int so = (g4 >> 1) * 1024 + (g4 & 1) * 512;
        const int kvoK = so + (16 * (i16 >> 3) + (i16 & 7)) * 16, kvoV = 2048 + so + i16 * 16;
#pragma unroll
        for (int T = 0; T < 2; ++T) {
            const int tl = wm * 2 + T;
            const unsigned kvg = WS_KV + (unsigned)((br * 32 + h) * 64 + qb * 4 + tl) * 4096;
            const lds_ptr kvl = LW + (br ? P2_BOWN + hh * 16384 + tl * 4096 : hh * 49152 + (8 + tl) * 4096);
            const lds_ptr qxl = LW + P2_QX + ((br * 2 + hh) * 4 + tl) * 2048;
#pragma unroll
            for (int mp = 0; mp < 2; ++mp) {
                const u32x4 kf = pack44n(acc[2 * T + mp][2], acc[2 * T + mp][3]);
                st_sc1(rs, kvg + kvoK + mp * 128, kf); LDSW(kvl + kvoK + mp * 128) = kf;
            }
#pragma unroll
            for (int np = 0; np < 2; ++np) {
                const u32x4 vf = pack44n(acc[2 * T][4 + np], acc[2 * T + 1][4 + np]);
                st_sc1(rs, kvg + kvoV + np * 256, vf); LDSW(kvl + kvoV + np * 256) = vf;
            }
#pragma unroll
            for (int mp = 0; mp < 2; ++mp) LDSW(qxl + kvoK + mp * 128) = pack44(acc[2 * T + mp][0], acc[2 * T + mp][1], QSCALE);
        }
        LBAR();
    }

    const int hh = wid >> 2, tt = wid & 3, h = hp * 2 + hh;
    const int qtile = qb * 4 + tt;
    const char* kvA = ws + WS_KV + (size_t)(h * 64 + tt) * 4096;
    const char* kvB = kvA + (size_t)32 * 64 * 4096;
    const unsigned voff = lane * 16;
#define COPY4(src, dst) do { glds16((src), voff, (dst)); glds16((src) + 1024, voff, (dst) + 1024); glds16((src) + 2048, voff, (dst) + 2048); glds16((src) + 3072, voff, (dst) + 3072); } while (0)
    const bf16x8 qa0 = LDSV(L + P2_QX + (hh * 4 + tt) * 2048 + lane * 16), qa1 = LDSV(L + P2_QX + (hh * 4 + tt) * 2048 + 1024 + lane * 16);
    const bf16x8 qb0 = LDSV(L + P2_QX + ((2 + hh) * 4 + tt) * 2048 + lane * 16), qb1 = LDSV(L + P2_QX + ((2 + hh) * 4 + tt) * 2048 + 1024 + lane * 16);

    const int rb = 4 * (xcd >> 1) + (jj & 3), cb = 8 * (xcd & 1) + (jj >> 2);
    const size_t p3off = (size_t)((wid >> 2) * 512 + (wid & 3) * 32) * 2048 + cb * 128;
    const unsigned p3vw = (unsigned)((lane >> 3) * 2048 + (((lane & 7) ^ (((lane >> 4) & 3) << 1)) * 16));
    const unsigned osrc = WS_O + (unsigned)((rb * 4 + (wid & 3)) * 64 + (wid >> 2) * 16) * 2048 + (g4 >> 1) * 1024 + ((g4 & 1) * 32 + i16) * 16;
    u32x4 o0[8], o1[8], o2[8]; float g, g2;
#define P3_LDO(OF, c) do { _Pragma("unroll") for (int i = 0; i < 8; ++i) OF[i] = ld_sc1(rs, osrc + (((c) >> 2) * 32 + ((c) & 3) * 4 + (i >> 1)) * 2048 + (i & 1) * 256); } while (0)
#define P3_SLOT(c) (((c) % 5) == 0 ? 65536 : ((c) % 5) == 1 ? 32768 : ((c) % 5) == 2 ? 131072 : ((c) % 5) == 3 ? 98304 : 0)
#define P3_STAGE(c) do { const char* src_ = (((c) >> 2) ? woB : woA) + p3off + (size_t)((c) & 3) * (128 * 2048); const unsigned dst_ = lds0 + P3_SLOT(c) + wid * 4096; \
        glds16(src_, p3vw, dst_); glds16(src_ + 8 * 2048, p3vw, dst_ + 1024); glds16(src_ + 16 * 2048, p3vw, dst_ + 2048); glds16(src_ + 24 * 2048, p3vw, dst_ + 3072); } while (0)
    {
        bf16x8 sel;
        { const short one = (((lane >> 4) & 1) == ((lane >> 2) & 1)) ? (short)0x3F80 : (short)0;
#pragma unroll
          for (int i = 0; i < 8; ++i) sel[i] = one; }
        f32x16 SA, SB, negm, oa = f32x16{}, ob = f32x16{};
        f32x4 la = f32x4{}, lb = f32x4{};
        Fr2 kA, kB, vA, vB; u32x4 pw0, pw1;
        const lds_cptr hb = L + hh * 16384 + lane * 16;
        float mB;
#define A_RUN(pf, pn_, n_) do { \
            int n = (n_); lds_cptr p1 = n == 0 ? (pf) : (pn_); lds_cptr p2 = n <= 1 ? p1 : p1 - 4096; \
            kA.a = LDSV(p1); kA.b = LDSV(p1 + 1024); ZERO_P(); vA = kA; \
            ATT_STEP(SA, SB, negm, qa0, qa1, oa, la, kA, kB, vA, vB, p2, (pf), (void)0); \
            while (n >= 2) { \
                const lds_cptr p3 = n > 2 ? p2 - 4096 : p2; \
                ATT_STEP(SB, SA, negm, qa0, qa1, oa, la, kB, kA, vB, vA, p3, p1, (void)0); \
                const lds_cptr p4 = n > 3 ? p3 - 4096 : p3; \
                ATT_STEP(SA, SB, negm, qa0, qa1, oa, la, kA, kB, vA, vB, p4, p2, (void)0); \
                p1 = p3; p2 = p4; n -= 2; \
            } \
            if (n == 1) { ATT_STEP(SB, SA, negm, qa0, qa1, oa, la, kB, kA, vB, vA, p1, p1, (void)0); ATT_DRAIN(oa, la, vA); } \
            else { ATT_DRAIN(oa, la, vB); } \
        } while (0)
        const lds_cptr ab = L + hh * 49152 + (8 + tt) * 4096 + lane * 16;
        float mA;
        {
            kA.a = LDSV(ab); kA.b = LDSV(ab + 1024);
            SA = MFMA32(kA.a, qa0, f32x16{}); SA = MFMA32(kA.b, qa1, SA);
            apply_mask<1>(SA, r32, hi);
            mA = rowmax16(SA);
#pragma unroll
            for (int r = 0; r < 16; ++r) { SA[r] -= mA; negm[r] = -mA; }
            A_RUN(ab, ab - 4096, tt);
        }
        WAIT_BAR(0);
        if (tid == 0) __hip_atomic_store((gu64*)(ws + WS_FLAG1 + (hp * 16 + qb) * 128), tag, RLX_AGENT);
        const unsigned kvAo = WS_KV + (unsigned)(h * 64 + tt) * 4096 + lane * 16, kvBo = kvAo + 32u * 64 * 4096;
        const int b2 = qb >= 2 ? qb - 2 : qb, b1 = qb >= 1 ? qb - 1 : qb;
        u32x4 st0[4], st1[4];
        {
            const lds_cptr cur = hb + P2_BOWN;
            kA.a = LDSV(cur); kA.b = LDSV(cur + 1024);
            SA = MFMA32(kA.a, qb0, f32x16{}); SA = MFMA32(kA.b, qb1, SA);
            mB = rowmax16(SA);
#pragma unroll
            for (int r = 0; r < 16; ++r) { SA[r] -= mB; negm[r] = -mB; }
            kA.a = LDSV(cur + 4096); kA.b = LDSV(cur + 4096 + 1024);
            ZERO_P(); vA = kA;
            ATT_STEP(SA, SB, negm, qb0, qb1, ob, lb, kA, kB, vA, vB, cur + 2 * 4096, cur, (void)0);
            ATT_STEP(SB, SA, negm, qb0, qb1, ob, lb, kB, kA, vB, vA, cur + 3 * 4096, cur + 1 * 4096, (void)0);
            {
                gu64* f0 = (gu64*)(ws + WS_FLAG1 + (hp * 16 + ((lane & 1) ? b1 : b2)) * 128);
                for (unsigned spins = 0; spins < (1u << 18); ++spins) {
                    const unsigned long long v = __hip_atomic_load(f0, RLX_AGENT);
                    if (__all(v == tag)) break;
                    __builtin_amdgcn_s_sleep(1);
                }
#pragma unroll
                for (int i = 0; i < 4; ++i) st0[i] = ld_sc1(rs, kvAo + b1 * 16384 + i * 1024);
#pragma unroll
                for (int i = 0; i < 4; ++i) st1[i] = ld_sc1(rs, kvAo + b2 * 16384 + i * 1024);
            }
            ATT_STEP(SA, SB, negm, qb0, qb1, ob, lb, kA, kB, vA, vB, cur, cur + 2 * 4096, (void)0);
            ATT_STEP(SB, SA, negm, qb0, qb1, ob, lb, kB, kA, vB, vA, cur, cur + 3 * 4096, (void)0);
            ATT_DRAIN(ob, lb, vA);
        }
        {
            const lds_ptr ad = LW + hh * 49152 + tt * 4096 + lane * 16;
#pragma unroll
            for (int i = 0; i < 4; ++i) LDSW(ad + 4 * 4096 + i * 1024) = st0[i];
#pragma unroll
            for (int i = 0; i < 4; ++i) LDSW(ad + i * 1024) = st1[i];
        }
        wait16(ws + WS_FLAG1 + hp * 2048, tag, lane);
        if (wid == 0) __builtin_amdgcn_fence(__ATOMIC_ACQUIRE, "agent");
#pragma unroll
        for (int i = 0; i < 4; ++i) st0[i] = ld_sc1(rs, kvBo + ((qb + 1) & 15) * 16384 + i * 1024);
#pragma unroll
        for (int i = 0; i < 4; ++i) st1[i] = ld_sc1(rs, kvBo + ((qb + 2) & 15) * 16384 + i * 1024);
        LBAR();
        if (qb >= 1) {
            const bool far = qb >= 2;
            const lds_cptr p7 = L + hh * 49152 + 7 * 4096 + lane * 16;
            const lds_cptr pf = far ? ab - 8 * 4096 : p7;
#pragma unroll
            for (int r = 0; r < 16; ++r) negm[r] = -mA;
            f32x16 c1 = negm;
            if (far) apply_mask<2>(c1, r32, hi);
            kA.a = LDSV(pf); kA.b = LDSV(pf + 1024);
            SA = MFMA32(kA.a, qa0, c1); SA = MFMA32(kA.b, qa1, SA);
            A_RUN(pf, far ? p7 : p7 - 4096, far ? 7 - tt : 3);
        }
#undef A_RUN
#pragma unroll
        for (int i = 0; i < 4; ++i) { LDSW(LW + 131072 + wid * 4096 + i * 1024 + lane * 16) = st0[i]; LDSW(LW + 98304 + wid * 4096 + i * 1024 + lane * 16) = st1[i]; }
        {
            const unsigned oap = WS_O + (unsigned)(qtile * 64 + h) * 2048 + lane * 16;
            if (__all(finite_state(oa, la[0]))) {
                const float inv = __builtin_amdgcn_rcpf(la[0]);
                st_sc1(rs, oap, pack8(oa, 0, inv)); st_sc1(rs, oap + 1024, pack8(oa, 1, inv));
            } else {
                attn_slow(rs, WS_KV + (unsigned)h * (64 * 4096), qtile >= 8 ? qtile - 8 : 0, qtile, qtile, qtile - 8, qa0, qa1, oap, lane);
            }
        }
        WAIT_BAR_L(2);
        P3_STAGE(0);
        COPY4(kvB + ((qb + 3) & 15) * 16384, lds0 + 0 + wid * 4096);
        {
            int so0 = 131072, so1 = 98304, so2 = 0, so3 = 32768;
            lds_cptr cur = hb + so0;
#pragma unroll
            for (int r = 0; r < 16; ++r) negm[r] = -mB;
            kA.a = LDSV(cur); kA.b = LDSV(cur + 1024);
            SA = MFMA32(kA.a, qb0, negm); SA = MFMA32(kA.b, qb1, SA);
            kA.a = LDSV(cur + 4096); kA.b = LDSV(cur + 4096 + 1024);
            ZERO_P(); vA = kA;
            const unsigned long long wo1 = (unsigned long long)(woA + p3off) - ((unsigned long long)12 << 18);
#define P2_BODY(s, PRE) do { \
                const bool kv = (s) < 13; \
                unsigned long long pk_ = (unsigned long long)kvB + (unsigned)(((qb + (s) + 3) & 15) << 14), pw_ = wo1 + ((unsigned long long)(unsigned)(s) << 18); \
                asm volatile("" : "+s"(pk_), "+s"(pw_)); \
                const char* sb_ = (const char*)(kv ? pk_ : pw_); \
                const unsigned sst = kv ? 1024 : 8 * 2048, sv = kv ? voff : p3vw, db_ = lds0 + so3 + wid * 4096; \
                const lds_cptr nxt = hb + so1; \
                PRE; \
                ATT_STEP(SA, SB, negm, qb0, qb1, ob, lb, kA, kB, vA, vB, cur + 2 * 4096, cur, glds16(sb_, sv, db_)); \
                ATT_STEP(SB, SA, negm, qb0, qb1, ob, lb, kB, kA, vB, vA, cur + 3 * 4096, cur + 1 * 4096, glds16(sb_ + sst, sv, db_ + 1024)); \
                ATT_STEP(SA, SB, negm, qb0, qb1, ob, lb, kA, kB, vA, vB, nxt, cur + 2 * 4096, glds16(sb_ + 2 * sst, sv, db_ + 2048)); \
                ATT_STEP(SB, SA, negm, qb0, qb1, ob, lb, kB, kA, vB, vA, nxt + 4096, cur + 3 * 4096, glds16(sb_ + 3 * sst, sv, db_ + 3072)); \
                cur = nxt; { const int t_ = so0; so0 = so1; so1 = so2; so2 = so3; so3 = t_; } \
                WAIT_BAR_L(4); \
            } while (0)
            for (int s = 1; s < 10; ++s) {
                P2_BODY(s, if (__builtin_expect(s == 9 && wid == 0, 0)) wait16(ws + WS_FLAG2 + rb * 2048, tag, lane));
                if (__builtin_expect(s == 1 && wid == 0, 0)) { if (lane == 0) __hip_atomic_store((gu64*)(ws + WS_FLAG2 + (qb * 16 + hp) * 128), tag, RLX_AGENT); }
            }
            if (blockIdx.x == 0 && tid == 0) __hip_atomic_store((gu64*)(ws + WS_EPOCH), tag, RLX_AGENT);
            P2_BODY(10, P3_LDO(o0, 0));
            P2_BODY(11, P3_LDO(o1, 1));
            P2_BODY(12, P3_LDO(o2, 2));
            for (int s = 13; s < 16; ++s) P2_BODY(s, (void)0);
#undef P2_BODY
            ATT_DRAIN(ob, lb, vA);
        }
        g = __uint_as_float(__hip_atomic_load((gu32*)(ws + WS_G) + rb * 128 + (wid & 3) * 32 + i16, RLX_AGENT));
        g2 = __uint_as_float(__hip_atomic_load((gu32*)(ws + WS_G) + rb * 128 + (wid & 3) * 32 + 16 + i16, RLX_AGENT));
        const unsigned obp = WS_O + (unsigned)(qtile * 64 + 32 + h) * 2048 + lane * 16;
        if (__all(finite_state(ob, lb[0]))) {
            const float inv = __builtin_amdgcn_rcpf(lb[0]);
            st_sc1(rs, obp, pack8(ob, 0, inv)); st_sc1(rs, obp + 1024, pack8(ob, 1, inv));
        } else {
            attn_slow(rs, WS_KV + (unsigned)(32 + h) * (64 * 4096), 0, 63, -1, -1, qb0, qb1, obp, lane);
        }
    }

    {
        const int wt = wid & 3, kh = wid >> 2;
        WAIT_BAR_L(4);
        P3_STAGE(4);
        int wa[4];
        { const int f = (2 * (g4 & 1) + (q4 >> 1)) << 1; const int row = 16 * (g4 >> 1) + 4 * (g4 & 1) + q4;
#pragma unroll
          for (int n = 0; n < 4; ++n) wa[n] = kh * 16384 + row * 128 + (((2 * n + (p4 >> 1)) ^ f) * 16) + (p4 & 1) * 8; }
        f32x4 accA[2][4], accB[2][4];
#pragma unroll
        for (int t = 0; t < 2; ++t)
#pragma unroll
            for (int n = 0; n < 4; ++n) { accA[t][n] = f32x4{}; accB[t][n] = f32x4{}; }
        bf16x8 wfA[4], wfB[4];
#define P3_HEAD(ACC, WC, WN, PN, OA, OB) do { \
            _Pragma("unroll") for (int n = 0; n < 4; ++n) { \
                WN[n] = trfrag(L + (PN) + wa[n], 1024); \
                ACC[0][n] = MFMA16(WC[n], BC8(OA), ACC[0][n]); ACC[1][n] = MFMA16(WC[n], BC8(OB), ACC[1][n]); PIN(ACC[0][n]); PIN(ACC[1][n]); SBAR(); } \
        } while (0)
#define P3_COMP(ACC, OF, c, cn) do { \
            P3_HEAD(ACC, wfA, wfB, P3_SLOT(c) + 1 * 4096, OF[0], OF[1]); P3_HEAD(ACC, wfB, wfA, P3_SLOT(c) + 2 * 4096, OF[2], OF[3]); \
            P3_HEAD(ACC, wfA, wfB, P3_SLOT(c) + 3 * 4096, OF[4], OF[5]); P3_HEAD(ACC, wfB, wfA, P3_SLOT(cn), OF[6], OF[7]); \
        } while (0)
#pragma unroll
        for (int n = 0; n < 4; ++n) wfA[n] = trfrag(L + P3_SLOT(0) + wa[n], 1024);
        P3_COMP(accA, o0, 0, 1);
        WAIT_BAR_L(4);
        if (tid == 0) __hip_atomic_store((gu64*)(ws + WS_FLAG3 + (qb * 16 + hp) * 128), tag, RLX_AGENT);
        P3_STAGE(5); P3_LDO(o0, 3);
        P3_COMP(accA, o1, 1, 2);
        LBAR(); P3_STAGE(6);
        P3_COMP(accA, o2, 2, 3);
        WAIT_BAR_L(16);
        P3_STAGE(7);
        P3_HEAD(accA, wfA, wfB, P3_SLOT(3) + 1 * 4096, o0[0], o0[1]); P3_HEAD(accA, wfB, wfA, P3_SLOT(3) + 2 * 4096, o0[2], o0[3]);
        P3_HEAD(accA, wfA, wfB, P3_SLOT(3) + 3 * 4096, o0[4], o0[5]); P3_HEAD(accA, wfB, wfA, P3_SLOT(3), o0[6], o0[7]);
        WAIT_BAR_L(0);
        auto wait_group = [&](unsigned a_) {
            gu64* f3 = (gu64*)(ws + WS_FLAG3 + rb * 2048 + (kh * 8 + a_ * 2 + (lane & 1)) * 128);
            for (unsigned spins = 0; spins < (1u << 18); ++spins) {
                const unsigned long long v = __hip_atomic_load(f3, RLX_AGENT);
                if (__all(v == tag)) break;
                __builtin_amdgcn_s_sleep(1);
            }
        };
#define P3_LDB(OF, a_) do { _Pragma("unroll") for (int i = 0; i < 8; ++i) \
            OF[i] = __builtin_amdgcn_raw_buffer_load_b128(rs, (int)(osrc + (32 + (i >> 1)) * 2048 + (i & 1) * 256), (int)((a_) * 8192), 16); } while (0)
#define P3_SLOTB(a_) ((a_) == 0 ? P3_SLOT(4) : (a_) == 1 ? P3_SLOT(5) : (a_) == 2 ? P3_SLOT(6) : P3_SLOT(7))
#define P3_COMPB(OF, a_) do { const int sl_ = P3_SLOTB(a_); \
            _Pragma("unroll") for (int n = 0; n < 4; ++n) wfA[n] = trfrag(L + sl_ + wa[n], 1024); \
            P3_HEAD(accB, wfA, wfB, sl_ + 1 * 4096, OF[0], OF[1]); P3_HEAD(accB, wfB, wfA, sl_ + 2 * 4096, OF[2], OF[3]); \
            P3_HEAD(accB, wfA, wfB, sl_ + 3 * 4096, OF[4], OF[5]); P3_HEAD(accB, wfB, wfA, sl_, OF[6], OF[7]); \
        } while (0)
        const unsigned a0 = (xcd + 1) & 3, a1 = (xcd + 2) & 3, a2 = (xcd + 3) & 3, a3 = xcd & 3;
        wait_group(a0); P3_LDB(o1, a0);
        wait_group(a1); P3_LDB(o2, a1);
        P3_COMPB(o1, a0);
        wait_group(a2); P3_LDB(o1, a2);
        P3_COMPB(o2, a1);
        wait_group(a3); P3_LDB(o2, a3);
        P3_COMPB(o1, a2);
        P3_COMPB(o2, a3);
#undef P3_LDB
#undef P3_COMPB
#undef P3_LDO
#undef P3_COMP
#undef P3_HEAD
        LBAR();
        LAS f32x4* ex = (LAS f32x4*)(LW + wid * 4096) + lane;
        f32x4 mix[2][4];
#pragma unroll
        for (int t = 0; t < 2; ++t)
#pragma unroll
            for (int n = 0; n < 4; ++n) { const float gt = t ? g2 : g; mix[t][n] = accA[t][n] * gt + accB[t][n] * (1.0f - gt); }
#pragma unroll
        for (int t = 0; t < 2; ++t)
#pragma unroll
            for (int n = 0; n < 2; ++n) ex[(t * 2 + n) * 64] = kh ? mix[t][n] : mix[t][2 + n];
        LBAR();
        const LAS f32x4* ey = (const LAS f32x4*)(LW + (wid ^ 4) * 4096) + lane;
#pragma unroll
        for (int t = 0; t < 2; ++t)
#pragma unroll
            for (int n = 0; n < 2; ++n) {
                const f32x4 y = ey[(t * 2 + n) * 64] + (kh ? mix[t][2 + n] : mix[t][n]);
                unsigned short* op = out + (size_t)(rb * 128 + wt * 32 + t * 16 + i16) * DM + cb * 64 + (2 * kh + n) * 16 + 4 * g4;
                *(u32x2*)op = (u32x2){cvtpk(y[0], y[1]), cvtpk(y[2], y[3])};
            }
    }
}

extern "C" void kernel_launch(void* const* d_in, const int* in_sizes, int n_in, void* d_out, int out_size, void* d_ws, size_t ws_size,
                              hipStream_t stream) {
    (void)hipFuncSetAttribute((const void*)fused_kernel, hipFuncAttributeMaxDynamicSharedMemorySize, LDS_BYTES);
    fused_kernel<<<256, 512, LDS_BYTES, stream>>>((const char*)d_in[0], (const char*)d_in[1], (const char*)d_in[2], (const char*)d_in[4], (const char*)d_in[5],
                                                  (const unsigned short*)d_in[6], (char*)d_ws, (unsigned short*)d_out);
}
```

```cpp
#include <hip/hip_runtime.h>
#include <hip/hip_bf16.h>
#include <stdint.h>

#define LAS __attribute__((address_space(3)))
typedef short bf16x8 __attribute__((ext_vector_type(8)));
typedef short s16x4 __attribute__((ext_vector_type(4)));
typedef float f32x16 __attribute__((ext_vector_type(16)));
typedef float f32x4 __attribute__((ext_vector_type(4)));
typedef float f32x2 __attribute__((ext_vector_type(2)));
typedef unsigned u32x4 __attribute__((ext_vector_type(4)));
typedef unsigned u32x2 __attribute__((ext_vector_type(2)));
typedef __bf16 bf16x2_t __attribute__((ext_vector_type(2)));
typedef LAS const char* lds_cptr;
typedef LAS char* lds_ptr;
typedef __attribute__((address_space(1))) unsigned long long gu64;
typedef __attribute__((address_space(1))) unsigned gu32;

#define MFMA32(a, b, c) __builtin_amdgcn_mfma_f32_32x32x16_bf16(a, b, c, 0, 0, 0)
#define MFMA16(a, b, c) __builtin_amdgcn_mfma_f32_16x16x32_bf16(a, b, c, 0, 0, 0)
#define SBAR() __builtin_amdgcn_sched_barrier(0)
#define PIN(x) asm volatile("" : "+v"(x))
#define WAIT_BAR(N) asm volatile("s_waitcnt vmcnt(" #N ")\n\ts_barrier" ::: "memory")
#define WAIT_BAR_L(N) asm volatile("s_waitcnt vmcnt(" #N ") lgkmcnt(0)\n\ts_barrier" ::: "memory")
#define LBAR() asm volatile("s_waitcnt lgkmcnt(0)\n\ts_barrier" ::: "memory")
#define BAR() asm volatile("s_barrier" ::: "memory")
#define RLX_AGENT __ATOMIC_RELAXED, __HIP_MEMORY_SCOPE_AGENT

constexpr int DM = 1024;
constexpr float QSCALE = 0.17677669529663687f * 1.4426950408889634f;

constexpr unsigned WS_KV = 0;
constexpr unsigned WS_O = 16u << 20;
constexpr unsigned WS_G = 24u << 20;
constexpr unsigned WS_EPOCH = WS_G + 65536;
constexpr unsigned WS_FLAG1 = WS_EPOCH + 4096;
constexpr unsigned WS_FLAG2 = WS_FLAG1 + 32768;
constexpr unsigned WS_FLAG3 = WS_FLAG2 + 32768;
constexpr unsigned WS_USED = 32u << 20;
constexpr int LDS_BYTES = 163840;


__device__ __forceinline__ unsigned cvtpk(float lo, float hi) {
    f32x2 v = {lo, hi}; bf16x2_t b = __builtin_convertvector(v, bf16x2_t); return __builtin_bit_cast(unsigned, b);
}
__device__ __forceinline__ void glds16(const void* sbase, unsigned voff, unsigned lds_dst) {
    asm volatile("s_mov_b32 m0, %2\n\ts_nop 0\n\tglobal_load_lds_dwordx4 %0, %1" : : "v"(voff), "s"(sbase), "s"(lds_dst) : "memory");
}
__device__ __forceinline__ void touch_lines64(const void* vaddr, unsigned lds_dst) {
    asm volatile("s_mov_b32 m0, %1\n\ts_nop 0\n\tglobal_load_lds_dword %0, off" : : "v"(vaddr), "s"(lds_dst) : "memory");
}
__device__ __forceinline__ void st_sc1(__amdgpu_buffer_rsrc_t rs, unsigned off, u32x4 v) { __builtin_amdgcn_raw_buffer_store_b128(v, rs, (int)off, 0, 16); }
__device__ __forceinline__ u32x4 ld_sc1(__amdgpu_buffer_rsrc_t rs, unsigned off) { return __builtin_amdgcn_raw_buffer_load_b128(rs, (int)off, 0, 16); }
__device__ __forceinline__ void wait16(const char* base, unsigned long long tag, int lane) {
    gu64* f = (gu64*)(base + (lane & 15) * 128);
    for (unsigned spins = 0; spins < (1u << 18); ++spins) {
        const unsigned long long v = __hip_atomic_load(f, RLX_AGENT);
        if (__all(v == tag)) break;
        __builtin_amdgcn_s_sleep(1);
    }
}
__device__ __forceinline__ s16x4 trread(lds_cptr p) {
    typedef short v4i16_t __attribute__((ext_vector_type(4)));
    return __builtin_bit_cast(s16x4, __builtin_amdgcn_ds_read_tr16_b64_v4i16((LAS v4i16_t*)p));
}
__device__ __forceinline__ bf16x8 trfrag(lds_cptr p, int second_off) {
    const s16x4 lo = trread(p), hi = trread(p + second_off);
    return (bf16x8){lo[0], lo[1], lo[2], lo[3], hi[0], hi[1], hi[2], hi[3]};
}
__device__ __forceinline__ u32x4 pack8(const f32x16& a, int s, float sc) {
    u32x4 r;
    r[0] = cvtpk(a[8 * s + 0] * sc, a[8 * s + 1] * sc); r[1] = cvtpk(a[8 * s + 2] * sc, a[8 * s + 3] * sc);
    r[2] = cvtpk(a[8 * s + 4] * sc, a[8 * s + 5] * sc); r[3] = cvtpk(a[8 * s + 6] * sc, a[8 * s + 7] * sc);
    return r;
}
__device__ __forceinline__ u32x4 pack8n(const f32x16& a, int s) {
    u32x4 r;
    r[0] = cvtpk(a[8 * s + 0], a[8 * s + 1]); r[1] = cvtpk(a[8 * s + 2], a[8 * s + 3]);
    r[2] = cvtpk(a[8 * s + 4], a[8 * s + 5]); r[3] = cvtpk(a[8 * s + 6], a[8 * s + 7]);
    return r;
}
__device__ __forceinline__ u32x4 pack44(const f32x4& a, const f32x4& b, float sc) {
    return (u32x4){cvtpk(a[0] * sc, a[1] * sc), cvtpk(a[2] * sc, a[3] * sc), cvtpk(b[0] * sc, b[1] * sc), cvtpk(b[2] * sc, b[3] * sc)};
}
__device__ __forceinline__ u32x4 pack44n(const f32x4& a, const f32x4& b) {
    return (u32x4){cvtpk(a[0], a[1]), cvtpk(a[2], a[3]), cvtpk(b[0], b[1]), cvtpk(b[2], b[3])};
}
#define EXP2(v) __builtin_amdgcn_exp2f(v)
#define LDSV(p) (*(const LAS bf16x8*)(p))
#define LDSW(p) (*(LAS u32x4*)(p))
#define BC8(v) __builtin_bit_cast(bf16x8, v)

__device__ __forceinline__ float halfmax(float m) {
    auto rr = __builtin_amdgcn_permlane32_swap(__float_as_uint(m), __float_as_uint(m), false, false);
    return __builtin_fmaxf(__uint_as_float(rr[0]), __uint_as_float(rr[1]));
}
__device__ __forceinline__ float rowmax16(const f32x16& p) {
    float a = __builtin_fmaxf(p[0], p[1]);
#pragma unroll
    for (int r = 2; r < 16; ++r) a = __builtin_fmaxf(a, p[r]);
    return halfmax(a);
}
template <int MASK> __device__ __forceinline__ void apply_mask(f32x16& s, int r32, int hi) {
#pragma unroll
    for (int r = 0; r < 16; ++r) {
        const int kl = (r & 3) + 8 * (r >> 2) + 4 * hi;
        const bool keep = (MASK == 1) ? (kl <= r32) : (kl > r32);
        if (!keep) s[r] = -INFINITY;
    }
}
__device__ __forceinline__ void attn_slow(__amdgpu_buffer_rsrc_t rs, unsigned kvh, int t_first, int t_last, int t_diag, int t_far, bf16x8 q0, bf16x8 q1, unsigned odst, int lane) {
    const int r32 = lane & 31, hi = lane >> 5;
    f32x16 o = f32x16{}; float m = -INFINITY, l = 0.f;
    for (int t = t_last; t >= t_first; --t) {
        const unsigned p = kvh + (unsigned)t * 4096 + lane * 16;
        const bf16x8 k0 = BC8(ld_sc1(rs, p)), k1 = BC8(ld_sc1(rs, p + 1024)), v0 = BC8(ld_sc1(rs, p + 2048)), v1 = BC8(ld_sc1(rs, p + 3072));
        f32x16 s = MFMA32(k0, q0, f32x16{}); s = MFMA32(k1, q1, s);
        if (t == t_diag) apply_mask<1>(s, r32, hi);
        if (t == t_far) apply_mask<2>(s, r32, hi);
        const float mn = __builtin_fmaxf(m, rowmax16(s));
        const float ms = (mn == -INFINITY) ? 0.f : mn;
        const float alpha = EXP2(m - ms);
        float rs_ = 0.f;
#pragma unroll
        for (int r = 0; r < 16; ++r) { s[r] = EXP2(s[r] - ms); o[r] *= alpha; }
        const u32x4 p0 = pack8n(s, 0), p1 = pack8n(s, 1);
#pragma unroll
        for (int i = 0; i < 4; ++i) rs_ += __uint_as_float(p0[i] << 16) + __uint_as_float(p0[i] & 0xffff0000u) + __uint_as_float(p1[i] << 16) + __uint_as_float(p1[i] & 0xffff0000u);
        l = l * alpha + rs_; m = mn;
        o = MFMA32(v0, BC8(p0), o); o = MFMA32(v1, BC8(p1), o);
    }
    auto rr = __builtin_amdgcn_permlane32_swap(__float_as_uint(l), __float_as_uint(l), false, false);
    l = __uint_as_float(rr[0]) + __uint_as_float(rr[1]);
    const float inv = 1.0f / l;
    st_sc1(rs, odst, pack8(o, 0, inv)); st_sc1(rs, odst + 1024, pack8(o, 1, inv));
}
__device__ __forceinline__ bool finite_state(const f32x16& o, float l) {
    float a = 0.f, b = 0.f;
#pragma unroll
    for (int r = 0; r < 16; r += 2) { a = __builtin_fmaf(o[r], 0.f, a); b = __builtin_fmaf(o[r + 1], 0.f, b); }
    return (l < 1.0e30f) && (a + b == 0.f);
}
struct Fr2 { bf16x8 a, b; };
#define EX4(S, i) do { S[i] = EXP2(S[i]); S[(i) + 1] = EXP2(S[(i) + 1]); S[(i) + 2] = EXP2(S[(i) + 2]); S[(i) + 3] = EXP2(S[(i) + 3]); PIN(S); } while (0)
#define PK4(W, S, i) do { W[0] = cvtpk(S[i], S[(i) + 1]); W[1] = cvtpk(S[(i) + 2], S[(i) + 3]); W[2] = cvtpk(S[(i) + 4], S[(i) + 5]); W[3] = cvtpk(S[(i) + 6], S[(i) + 7]); PIN(W); } while (0)
#define ATT_STEP(SC, SN, CN, Q0, Q1, O, LS, KC, KN, VC, VN, KP, VP, G0) do { \
        SBAR(); \
        SN = MFMA32(KC.a, Q0, CN); PIN(SN); SBAR(); \
        KN.a = LDSV(KP); KN.b = LDSV((KP) + 1024); EX4(SC, 0); SBAR(); \
        O = MFMA32(VC.a, BC8(pw0), O); PIN(O); SBAR(); \
        VN.a = LDSV((VP) + 2048); VN.b = LDSV((VP) + 3072); EX4(SC, 4); SBAR(); \
        SN = MFMA32(KC.b, Q1, SN); PIN(SN); SBAR(); \
        EX4(SC, 8); SBAR(); \
        LS = MFMA16(sel, BC8(pw0), LS); PIN(LS); SBAR(); \
        PK4(pw0, SC, 0); SBAR(); \
        O = MFMA32(VC.b, BC8(pw1), O); PIN(O); SBAR(); \
        EX4(SC, 12); SBAR(); \
        LS = MFMA16(sel, BC8(pw1), LS); PIN(LS); SBAR(); \
        G0; PK4(pw1, SC, 8); SBAR(); \
    } while (0)
#define ATT_DRAIN(O, LS, VC) do { \
        O = MFMA32(VC.a, BC8(pw0), O); O = MFMA32(VC.b, BC8(pw1), O); \
        LS = MFMA16(sel, BC8(pw0), LS); LS = MFMA16(sel, BC8(pw1), LS); \
    } while (0)
#define ZERO_P() do { pw0 = u32x4{}; pw1 = u32x4{}; } while (0)

constexpr int P1_WR = 49152, P1_WSLOT = 24576, P1_DUMMY = 147456;
constexpr int P2_QX = 0;
constexpr int P2_BOWN = 98304;
struct P1Frags { bf16x8 x[4]; s16x4 wl[6], wh[6]; };
#define WFR(F, n) (bf16x8){F.wl[n][0], F.wl[n][1], F.wl[n][2], F.wl[n][3], F.wh[n][0], F.wh[n][1], F.wh[n][2], F.wh[n][3]}

__global__ __launch_bounds__(512, 2) void fused_kernel(const char* __restrict__ x, const char* __restrict__ wA, const char* __restrict__ woA,
                                                       const char* __restrict__ wB, const char* __restrict__ woB,
                                                       const unsigned short* __restrict__ gate_w, char* __restrict__ ws, unsigned short* __restrict__ out) {
    extern __shared__ __attribute__((aligned(16))) char lds[];
    const int tid = threadIdx.x, lane = tid & 63;
    const int wid = __builtin_amdgcn_readfirstlane(tid >> 6);
    const int xcd = blockIdx.x & 7, jj = blockIdx.x >> 3;
    const int hp = xcd * 2 + (jj & 1), qb = jj >> 1;
    const unsigned lds0 = (unsigned)(uintptr_t)lds;
    const lds_cptr L = (lds_cptr)lds;
    const lds_ptr LW = (lds_ptr)lds;
    const __amdgpu_buffer_rsrc_t rs = __builtin_amdgcn_make_buffer_rsrc((void*)ws, 0, (int)WS_USED, 0x00020000);
    const int i16 = lane & 15, g4 = lane >> 4, q4 = (lane >> 2) & 3, p4 = lane & 3;
    const int r32 = lane & 31, hi = lane >> 5;
    unsigned long long tag;

    const char* xs = x + (size_t)qb * 128 * 2048;
    unsigned vx[2];
#pragma unroll
    for (int p = 0; p < 2; ++p) {
        const int row = 16 * wid + 8 * p + (lane >> 3);
        const int sw = ((row >> 1) & 3) | (((row >> 4) & 1) << 2);
        vx[p] = (unsigned)(row * 2048 + (((lane & 7) ^ sw) * 16));
    }
    const unsigned xdst = lds0 + wid * 2048;
    const int wf_f = (((q4 >> 1) & 1) << 1) | ((g4 & 1) << 2);
    {
        const int wm = wid >> 2, wn = wid & 3, br = wn >> 1, hh = wn & 1;
        const int gtok = qb * 128 + hp * 8 + wid;
        u32x4 ga0, ga1, gg0, gg1;
        { const char* xr = x + (size_t)gtok * 2048; const unsigned go = lane * 32;
          asm volatile("global_load_dwordx4 %0, %4, %5\n\tglobal_load_dwordx4 %1, %4, %5 offset:16\n\tglobal_load_dwordx4 %2, %4, %6\n\tglobal_load_dwordx4 %3, %4, %6 offset:16"
                       : "=&v"(ga0), "=&v"(ga1), "=&v"(gg0), "=&v"(gg1) : "v"(go), "s"(xr), "s"(gate_w) : "memory"); }
        const char* wsrc = (wid < 4 ? wA : wB);
        unsigned vw[3];
#pragma unroll
        for (int i = 0; i < 3; ++i) {
            const int id = wid * 3 + i, sg = id >> 2, rg = id & 3, part = sg % 3;
            const int row = rg * 8 + (lane >> 3);
            const int f = (((row >> 1) & 1) << 1) | (((row >> 3) & 1) << 2);
            vw[i] = (unsigned)(row * 6144 + part * 2048 + hp * 128 + (((lane & 7) ^ f) * 16));
        }
        const unsigned wdst = lds0 + P1_WR + wid * 3072;
#define P1_X(T, slot, p) glds16(xs + (T) * 128, vx[p], xdst + (slot) + (p) * 1024)
#define P1_W(j, i) glds16(wsrc + (size_t)(j) * (32 * 6144), vw[i], wdst + ((j) & 3) * P1_WSLOT + (i) * 1024)
        P1_X(0, 0, 0); P1_X(0, 0, 1); P1_W(0, 0); P1_W(0, 1); P1_W(0, 2);
        P1_W(1, 0); P1_W(1, 1); P1_W(1, 2); P1_X(1, 16384, 0);
        P1_W(2, 0); P1_W(2, 1); P1_W(2, 2); P1_X(1, 16384, 1);
        const char* pfb; unsigned pfstep;
        { const int gw = blockIdx.x * 8 + wid, jl = lane & 3;
          if (lane >= 32) { pfb = ((gw >> 10) ? woB : woA) + (size_t)(gw & 1023) * 2048; pfstep = 128; }
          else if (jl == 0) { pfb = x + (size_t)gw * 2048; pfstep = 128; }
          else { const int id = gw * 3 + jl - 1, bq = id / 3072, rem = id % 3072, row = rem / 48, rest = rem % 48;
                 pfb = (bq ? wB : wA) + (size_t)(row * 6144 + rest * 128); pfstep = 64 * 6144; } }
        if (lane < 16) touch_lines64(pfb + (size_t)(2 + (lane >> 2)) * pfstep, lds0 + P1_DUMMY);
        const char* pfp = pfb + (size_t)(lane < 32 ? 6 + (lane >> 2) : (lane & 1)) * pfstep;
#define P1_PREF1() do { if (lane < 8) touch_lines64(pfp, lds0 + P1_DUMMY); if (lane < 32) pfp += 2 * (size_t)pfstep; } while (0)
#define P1_PREF2(t) do { if ((lane >> 1) == 16) touch_lines64(pfp, lds0 + P1_DUMMY); if ((t) < 12) pfp += 256; } while (0)
        asm volatile("s_load_dwordx2 %0, %1, 0x0\n\ts_waitcnt lgkmcnt(0)" : "=s"(tag) : "s"(ws + WS_EPOCH) : "memory");
        tag += 1;
        asm volatile("s_waitcnt vmcnt(14)" : "+v"(ga0), "+v"(ga1), "+v"(gg0), "+v"(gg1) :: "memory");
        {
            float s = 0.f;
#pragma unroll
            for (int i = 0; i < 4; ++i) {
                s += __uint_as_float(ga0[i] << 16) * __uint_as_float(gg0[i] << 16) + __uint_as_float(ga0[i] & 0xffff0000u) * __uint_as_float(gg0[i] & 0xffff0000u);
                s += __uint_as_float(ga1[i] << 16) * __uint_as_float(gg1[i] << 16) + __uint_as_float(ga1[i] & 0xffff0000u) * __uint_as_float(gg1[i] & 0xffff0000u);
            }
#pragma unroll
            for (int o = 32; o > 0; o >>= 1) s += __shfl_xor(s, o);
            if (lane == 0) __hip_atomic_store((gu32*)(ws + WS_G) + gtok, __float_as_uint(1.0f / (1.0f + __expf(-s))), RLX_AGENT);
        }
        int xa[2];
        { const int row = wm * 64 + 16 * (i16 >> 3) + (i16 & 7); const int sw = ((row >> 1) & 3) | (((row >> 4) & 1) << 2);
          xa[0] = row * 128 + ((g4 ^ sw) * 16); xa[1] = row * 128 + (((4 + g4) ^ sw) * 16); }
        int wa[2];
#pragma unroll
        for (int np = 0; np < 2; ++np) wa[np] = P1_WR + br * 3 * 4096 + (8 * g4 + q4) * 128 + (((hh * 4 + np * 2 + (p4 >> 1)) ^ wf_f) * 16) + (p4 & 1) * 8;
        f32x4 acc[4][6];
#pragma unroll
        for (int m = 0; m < 4; ++m)
#pragma unroll
            for (int n = 0; n < 6; ++n) acc[m][n] = f32x4{};
#define P1_LX(F, m, xo, kh) F.x[m] = LDSV(L + (xo) + xa[kh] + ((m) >> 1) * 4096 + ((m) & 1) * 1024)
#define P1_LWL(F, n, wo) F.wl[n] = trread(L + (wo) + wa[(n) & 1] + ((n) >> 1) * 4096)
#define P1_LWH(F, n, wo) F.wh[n] = trread(L + (wo) + wa[(n) & 1] + ((n) >> 1) * 4096 + 512)
#define P1_M(F, m, n) do { acc[m][n] = ((n) < 4) ? MFMA16(WFR(F, n), F.x[m], acc[m][n]) : MFMA16(F.x[m], WFR(F, n), acc[m][n]); } while (0)
#define P1_STEP(FC, FN, LD, xo, kh, wo, G0, G1, G2, G3) do { \
            P1_M(FC, 0, 0); if (LD) P1_LX(FN, 0, xo, kh); SBAR();  P1_M(FC, 1, 0); if (LD) P1_LWL(FN, 0, wo); SBAR(); \
            P1_M(FC, 2, 0); if (LD) P1_LWH(FN, 0, wo); SBAR();     P1_M(FC, 3, 0); G0; SBAR(); \
            P1_M(FC, 0, 1); if (LD) P1_LWL(FN, 1, wo); SBAR();     P1_M(FC, 1, 1); if (LD) P1_LWH(FN, 1, wo); SBAR(); \
            P1_M(FC, 2, 1); if (LD) P1_LX(FN, 1, xo, kh); SBAR();  P1_M(FC, 3, 1); if (LD) P1_LWL(FN, 2, wo); SBAR(); \
            P1_M(FC, 0, 2); if (LD) P1_LWH(FN, 2, wo); SBAR();     P1_M(FC, 1, 2); G1; SBAR(); \
            P1_M(FC, 2, 2); if (LD) P1_LWL(FN, 3, wo); SBAR();     P1_M(FC, 3, 2); if (LD) P1_LWH(FN, 3, wo); SBAR(); \
            P1_M(FC, 0, 3); if (LD) P1_LX(FN, 2, xo, kh); SBAR();  P1_M(FC, 1, 3); if (LD) P1_LWL(FN, 4, wo); SBAR(); \
            P1_M(FC, 2, 3); if (LD) P1_LWH(FN, 4, wo); SBAR();     P1_M(FC, 3, 3); G2; SBAR(); \
            P1_M(FC, 0, 4); if (LD) P1_LWL(FN, 5, wo); SBAR();     P1_M(FC, 1, 4); if (LD) P1_LWH(FN, 5, wo); SBAR(); \
            P1_M(FC, 2, 4); if (LD) P1_LX(FN, 3, xo, kh); SBAR();  P1_M(FC, 3, 4); SBAR(); \
            P1_M(FC, 0, 5); G3; SBAR();                            P1_M(FC, 1, 5); SBAR(); \
            P1_M(FC, 2, 5); SBAR();                                P1_M(FC, 3, 5); SBAR(); \
        } while (0)
        WAIT_BAR(6);
        P1Frags FA, FB;
#pragma unroll
        for (int m = 0; m < 4; ++m) P1_LX(FA, m, 0, 0);
#pragma unroll
        for (int n = 0; n < 6; ++n) { P1_LWL(FA, n, 0); P1_LWH(FA, n, 0); }
        int xcur = 0, xnxt = 16384, xfree = 32768;
#define P1_TILE(t, GW0, GX0, GW1, GX1, PF, N0, N1, LASTT) do { \
            const int w1 = ((t) & 1) * 2 * P1_WSLOT + P1_WSLOT, wn0 = (((t) + 1) & 1) * 2 * P1_WSLOT; \
            P1_STEP(FA, FB, true, xcur, 1, w1, if (GW0) P1_W(2 * (t) + 3, 0), if (GW0) P1_W(2 * (t) + 3, 1), if (GW0) P1_W(2 * (t) + 3, 2), if (GX0) P1_X((t) + 2, xfree, 0)); \
            WAIT_BAR(N0); \
            P1_STEP(FB, FA, !(LASTT), xnxt, 0, wn0, if (GW1) P1_W(2 * (t) + 4, 0), if (GW1) P1_W(2 * (t) + 4, 1), if (GW1) P1_W(2 * (t) + 4, 2), if (GX1) P1_X((t) + 2, xfree, 1)); \
            PF; \
            if (!(LASTT)) { WAIT_BAR(N1); } \
            { const int tmp = xcur; xcur = xnxt; xnxt = xfree; xfree = tmp; } \
        } while (0)
#pragma unroll 1
        for (int t = 0; t < 5; ++t) P1_TILE(t, true, true, true, true, P1_PREF1(), 5, 5, false);
#pragma unroll 1
        for (int t = 5; t < 14; ++t) P1_TILE(t, true, true, true, true, P1_PREF2(t), 5, 5, false);
        P1_TILE(14, true, false, false, false, (void)0, 4, 0, false);
        P1_TILE(15, false, false, false, false, (void)0, 0, 0, true);
#undef P1_TILE
#undef P1_STEP
#undef P1_M
#undef P1_LX
#undef P1_LWL
#undef P1_LWH
#undef P1_X
#undef P1_W
        LBAR();
        const int h = hp * 2 + hh;
        const int so = (g4 >> 1) * 1024 + (g4 & 1) * 512;
        const int kvoK = so + (16 * (i16 >> 3) + (i16 & 7)) * 16, kvoV = 2048 + so + i16 * 16;
#pragma unroll
        for (int T = 0; T < 2; ++T) {
            const int tl = wm * 2 + T;
            const unsigned kvg = WS_KV + (unsigned)((br * 32 + h) * 64 + qb * 4 + tl) * 4096;
            const lds_ptr kvl = LW + (br ? P2_BOWN + hh * 16384 + tl * 4096 : hh * 49152 + (8 + tl) * 4096);
            const lds_ptr qxl = LW + P2_QX + ((br * 2 + hh) * 4 + tl) * 2048;
#pragma unroll
            for (int mp = 0; mp < 2; ++mp) {
                const u32x4 kf = pack44n(acc[2 * T + mp][2], acc[2 * T + mp][3]);
                st_sc1(rs, kvg + kvoK + mp * 128, kf); LDSW(kvl + kvoK + mp * 128) = kf;
            }
#pragma unroll
            for (int np = 0; np < 2; ++np) {
                const u32x4 vf = pack44n(acc[2 * T][4 + np], acc[2 * T + 1][4 + np]);
                st_sc1(rs, kvg + kvoV + np * 256, vf); LDSW(kvl + kvoV + np * 256) = vf;
            }
#pragma unroll
            for (int mp = 0; mp < 2; ++mp) LDSW(qxl + kvoK + mp * 128) = pack44(acc[2 * T + mp][0], acc[2 * T + mp][1], QSCALE);
        }
        LBAR();
    }

    const int hh = wid >> 2, tt = wid & 3, h = hp * 2 + hh;
    const int qtile = qb * 4 + tt;
    const char* kvA = ws + WS_KV + (size_t)(h * 64 + tt) * 4096;
    const char* kvB = kvA + (size_t)32 * 64 * 4096;
    const unsigned voff = lane * 16;
#define COPY4(src, dst) do { glds16((src), voff, (dst)); glds16((src) + 1024, voff, (dst) + 1024); glds16((src) + 2048, voff, (dst) + 2048); glds16((src) + 3072, voff, (dst) + 3072); } while (0)
    const bf16x8 qa0 = LDSV(L + P2_QX + (hh * 4 + tt) * 2048 + lane * 16), qa1 = LDSV(L + P2_QX + (hh * 4 + tt) * 2048 + 1024 + lane * 16);
    const bf16x8 qb0 = LDSV(L + P2_QX + ((2 + hh) * 4 + tt) * 2048 + lane * 16), qb1 = LDSV(L + P2_QX + ((2 + hh) * 4 + tt) * 2048 + 1024 + lane * 16);

    const int rb = 4 * (xcd >> 1) + (jj & 3), cb = 8 * (xcd & 1) + (jj >> 2);
    const size_t p3off = (size_t)((wid >> 2) * 512 + (wid & 3) * 32) * 2048 + cb * 128;
    const unsigned p3vw = (unsigned)((lane >> 3) * 2048 + (((lane & 7) ^ (((lane >> 4) & 3) << 1)) * 16));
    const unsigned osrc = WS_O + (unsigned)((rb * 4 + (wid & 3)) * 64 + (wid >> 2) * 16) * 2048 + (g4 >> 1) * 1024 + ((g4 & 1) * 32 + i16) * 16;
    u32x4 o0[8], o1[8], o2[8]; float g, g2;
#define P3_LDO(OF, c) do { _Pragma("unroll") for (int i = 0; i < 8; ++i) OF[i] = ld_sc1(rs, osrc + (((c) >> 2) * 32 + ((c) & 3) * 4 + (i >> 1)) * 2048 + (i & 1) * 256); } while (0)
#define P3_SLOT(c) (((c) % 5) == 0 ? 65536 : ((c) % 5) == 1 ? 32768 : ((c) % 5) == 2 ? 131072 : ((c) % 5) == 3 ? 98304 : 0)
#define P3_STAGE(c) do { const char* src_ = (((c) >> 2) ? woB : woA) + p3off + (size_t)((c) & 3) * (128 * 2048); const unsigned dst_ = lds0 + P3_SLOT(c) + wid * 4096; \
        glds16(src_, p3vw, dst_); glds16(src_ + 8 * 2048, p3vw, dst_ + 1024); glds16(src_ + 16 * 2048, p3vw, dst_ + 2048); glds16(src_ + 24 * 2048, p3vw, dst_ + 3072); } while (0)
    {
        bf16x8 sel;
        { const short one = (((lane >> 4) & 1) == ((lane >> 2) & 1)) ? (short)0x3F80 : (short)0;
#pragma unroll
          for (int i = 0; i < 8; ++i) sel[i] = one; }
        f32x16 SA, SB, negm, oa = f32x16{}, ob = f32x16{};
        f32x4 la = f32x4{}, lb = f32x4{};
        Fr2 kA, kB, vA, vB; u32x4 pw0, pw1;
        const lds_cptr hb = L + hh * 16384 + lane * 16;
        float mB;
#define A_RUN(pf, pn_, n_) do { \
            int n = (n_); lds_cptr p1 = n == 0 ? (pf) : (pn_); lds_cptr p2 = n <= 1 ? p1 : p1 - 4096; \
            kA.a = LDSV(p1); kA.b = LDSV(p1 + 1024); ZERO_P(); vA = kA; \
            ATT_STEP(SA, SB, negm, qa0, qa1, oa, la, kA, kB, vA, vB, p2, (pf), (void)0); \
            while (n >= 2) { \
                const lds_cptr p3 = n > 2 ? p2 - 4096 : p2; \
                ATT_STEP(SB, SA, negm, qa0, qa1, oa, la, kB, kA, vB, vA, p3, p1, (void)0); \
                const lds_cptr p4 = n > 3 ? p3 - 4096 : p3; \
                ATT_STEP(SA, SB, negm, qa0, qa1, oa, la, kA, kB, vA, vB, p4, p2, (void)0); \
                p1 = p3; p2 = p4; n -= 2; \
            } \
            if (n == 1) { ATT_STEP(SB, SA, negm, qa0, qa1, oa, la, kB, kA, vB, vA, p1, p1, (void)0); ATT_DRAIN(oa, la, vA); } \
            else { ATT_DRAIN(oa, la, vB); } \
        } while (0)
        const lds_cptr ab = L + hh * 49152 + (8 + tt) * 4096 + lane * 16;
        float mA;
        {
            kA.a = LDSV(ab); kA.b = LDSV(ab + 1024);
            SA = MFMA32(kA.a, qa0, f32x16{}); SA = MFMA32(kA.b, qa1, SA);
            apply_mask<1>(SA, r32, hi);
            mA = rowmax16(SA);
#pragma unroll
            for (int r = 0; r < 16; ++r) { SA[r] -= mA; negm[r] = -mA; }
            A_RUN(ab, ab - 4096, tt);
        }
        WAIT_BAR(0);
        if (tid == 0) __hip_atomic_store((gu64*)(ws + WS_FLAG1 + (hp * 16 + qb) * 128), tag, RLX_AGENT);
        const unsigned kvAo = WS_KV + (unsigned)(h * 64 + tt) * 4096 + lane * 16, kvBo = kvAo + 32u * 64 * 4096;
        const int b2 = qb >= 2 ? qb - 2 : qb, b1 = qb >= 1 ? qb - 1 : qb;
        u32x4 st0[4], st1[4];
        {
            const lds_cptr cur = hb + P2_BOWN;
            kA.a = LDSV(cur); kA.b = LDSV(cur + 1024);
            SA = MFMA32(kA.a, qb0, f32x16{}); SA = MFMA32(kA.b, qb1, SA);
            mB = rowmax16(SA);
#pragma unroll
            for (int r = 0; r < 16; ++r) { SA[r] -= mB; negm[r] = -mB; }
            kA.a = LDSV(cur + 4096); kA.b = LDSV(cur + 4096 + 1024);
            ZERO_P(); vA = kA;
            ATT_STEP(SA, SB, negm, qb0, qb1, ob, lb, kA, kB, vA, vB, cur + 2 * 4096, cur, (void)0);
            ATT_STEP(SB, SA, negm, qb0, qb1, ob, lb, kB, kA, vB, vA, cur + 3 * 4096, cur + 1 * 4096, (void)0);
            {
                gu64* f0 = (gu64*)(ws + WS_FLAG1 + (hp * 16 + ((lane & 1) ? b1 : b2)) * 128);
                for (unsigned spins = 0; spins < (1u << 18); ++spins) {
                    const unsigned long long v = __hip_atomic_load(f0, RLX_AGENT);
                    if (__all(v == tag)) break;
                    __builtin_amdgcn_s_sleep(1);
                }
#pragma unroll
                for (int i = 0; i < 4; ++i) st0[i] = ld_sc1(rs, kvAo + b1 * 16384 + i * 1024);
#pragma unroll
                for (int i = 0; i < 4; ++i) st1[i] = ld_sc1(rs, kvAo + b2 * 16384 + i * 1024);
            }
            ATT_STEP(SA, SB, negm, qb0, qb1, ob, lb, kA, kB, vA, vB, cur, cur + 2 * 4096, (void)0);
            ATT_STEP(SB, SA, negm, qb0, qb1, ob, lb, kB, kA, vB, vA, cur, cur + 3 * 4096, (void)0);
            ATT_DRAIN(ob, lb, vA);
        }
        {
            const lds_ptr ad = LW + hh * 49152 + tt * 4096 + lane * 16;
#pragma unroll
            for (int i = 0; i < 4; ++i) LDSW(ad + 4 * 4096 + i * 1024) = st0[i];
#pragma unroll
            for (int i = 0; i < 4; ++i) LDSW(ad + i * 1024) = st1[i];
        }
        wait16(ws + WS_FLAG1 + hp * 2048, tag, lane);
        if (wid == 0) __builtin_amdgcn_fence(__ATOMIC_ACQUIRE, "agent");
#pragma unroll
        for (int i = 0; i < 4; ++i) st0[i] = ld_sc1(rs, kvBo + ((qb + 1) & 15) * 16384 + i * 1024);
#pragma unroll
        for (int i = 0; i < 4; ++i) st1[i] = ld_sc1(rs, kvBo + ((qb + 2) & 15) * 16384 + i * 1024);
        LBAR();
        if (qb >= 1) {
            const bool far = qb >= 2;
            const lds_cptr p7 = L + hh * 49152 + 7 * 4096 + lane * 16;
            const lds_cptr pf = far ? ab - 8 * 4096 : p7;
#pragma unroll
            for (int r = 0; r < 16; ++r) negm[r] = -mA;
            f32x16 c1 = negm;
            if (far) apply_mask<2>(c1, r32, hi);
            kA.a = LDSV(pf); kA.b = LDSV(pf + 1024);
            SA = MFMA32(kA.a, qa0, c1); SA = MFMA32(kA.b, qa1, SA);
            A_RUN(pf, far ? p7 : p7 - 4096, far ? 7 - tt : 3);
        }
#undef A_RUN
#pragma unroll
        for (int i = 0; i < 4; ++i) { LDSW(LW + 131072 + wid * 4096 + i * 1024 + lane * 16) = st0[i]; LDSW(LW + 98304 + wid * 4096 + i * 1024 + lane * 16) = st1[i]; }
        {
            const unsigned oap = WS_O + (unsigned)(qtile * 64 + h) * 2048 + lane * 16;
            if (__all(finite_state(oa, la[0]))) {
                const float inv = __builtin_amdgcn_rcpf(la[0]);
                st_sc1(rs, oap, pack8(oa, 0, inv)); st_sc1(rs, oap + 1024, pack8(oa, 1, inv));
            } else {
                attn_slow(rs, WS_KV + (unsigned)h * (64 * 4096), qtile >= 8 ? qtile - 8 : 0, qtile, qtile, qtile - 8, qa0, qa1, oap, lane);
            }
        }
        WAIT_BAR_L(2);
        P3_STAGE(0);
        COPY4(kvB + ((qb + 3) & 15) * 16384, lds0 + 0 + wid * 4096);
        {
            int so0 = 131072, so1 = 98304, so2 = 0, so3 = 32768;
            lds_cptr cur = hb + so0;
#pragma unroll
            for (int r = 0; r < 16; ++r) negm[r] = -mB;
            kA.a = LDSV(cur); kA.b = LDSV(cur + 1024);
            SA = MFMA32(kA.a, qb0, negm); SA = MFMA32(kA.b, qb1, SA);
            kA.a = LDSV(cur + 4096); kA.b = LDSV(cur + 4096 + 1024);
            ZERO_P(); vA = kA;
            const unsigned long long wo1 = (unsigned long long)(woA + p3off) - ((unsigned long long)12 << 18);
#define NOH(k) do {} while (0)
#define P2_BODY(s, PRE, LDH, NW) do { \
                const bool kv = (s) < 13; \
                unsigned long long pk_ = (unsigned long long)kvB + (unsigned)(((qb + (s) + 3) & 15) << 14), pw_ = wo1 + ((unsigned long long)(unsigned)(s) << 18); \
                asm volatile("" : "+s"(pk_), "+s"(pw_)); \
                const char* sb_ = (const char*)(kv ? pk_ : pw_); \
                const unsigned sst = kv ? 1024 : 8 * 2048, sv = kv ? voff : p3vw, db_ = lds0 + so3 + wid * 4096; \
                const lds_cptr nxt = hb + so1; \
                PRE; \
                ATT_STEP(SA, SB, negm, qb0, qb1, ob, lb, kA, kB, vA, vB, cur + 2 * 4096, cur, { glds16(sb_, sv, db_); LDH(0); }); \
                ATT_STEP(SB, SA, negm, qb0, qb1, ob, lb, kB, kA, vB, vA, cur + 3 * 4096, cur + 1 * 4096, { glds16(sb_ + sst, sv, db_ + 1024); LDH(1); }); \
                ATT_STEP(SA, SB, negm, qb0, qb1, ob, lb, kA, kB, vA, vB, nxt, cur + 2 * 4096, { glds16(sb_ + 2 * sst, sv, db_ + 2048); LDH(2); }); \
                ATT_STEP(SB, SA, negm, qb0, qb1, ob, lb, kB, kA, vB, vA, nxt + 4096, cur + 3 * 4096, { LDH(3); glds16(sb_ + 3 * sst, sv, db_ + 3072); }); \
                cur = nxt; { const int t_ = so0; so0 = so1; so1 = so2; so2 = so3; so3 = t_; } \
                WAIT_BAR_L(NW); \
            } while (0)
            for (int s = 1; s < 10; ++s) {
                P2_BODY(s, if (__builtin_expect(s == 9 && wid == 0, 0)) wait16(ws + WS_FLAG2 + rb * 2048, tag, lane), NOH, 4);
                if (__builtin_expect(s == 1 && wid == 0, 0)) { if (lane == 0) __hip_atomic_store((gu64*)(ws + WS_FLAG2 + (qb * 16 + hp) * 128), tag, RLX_AGENT); }
            }
            if (blockIdx.x == 0 && tid == 0) __hip_atomic_store((gu64*)(ws + WS_EPOCH), tag, RLX_AGENT);
#define P3_LD2(OF, c, k) do { OF[2 * (k)] = ld_sc1(rs, osrc + ((c) * 4 + (k)) * 2048); OF[2 * (k) + 1] = ld_sc1(rs, osrc + ((c) * 4 + (k)) * 2048 + 256); } while (0)
#define LH0(k) P3_LD2(o0, 0, k)
#define LH1(k) P3_LD2(o1, 1, k)
#define LH2(k) P3_LD2(o2, 2, k)
            P2_BODY(10, (void)0, LH0, 12);
            P2_BODY(11, (void)0, LH1, 12);
            P2_BODY(12, (void)0, LH2, 12);
            for (int s = 13; s < 16; ++s) P2_BODY(s, (void)0, NOH, 4);
#undef LH0
#undef LH1
#undef LH2
#undef P3_LD2
#undef P2_BODY
            ATT_DRAIN(ob, lb, vA);
        }
        g = __uint_as_float(__hip_atomic_load((gu32*)(ws + WS_G) + rb * 128 + (wid & 3) * 32 + i16, RLX_AGENT));
        g2 = __uint_as_float(__hip_atomic_load((gu32*)(ws + WS_G) + rb * 128 + (wid & 3) * 32 + 16 + i16, RLX_AGENT));
        const unsigned obp = WS_O + (unsigned)(qtile * 64 + 32 + h) * 2048 + lane * 16;
        if (__all(finite_state(ob, lb[0]))) {
            const float inv = __builtin_amdgcn_rcpf(lb[0]);
            st_sc1(rs, obp, pack8(ob, 0, inv)); st_sc1(rs, obp + 1024, pack8(ob, 1, inv));
        } else {
            attn_slow(rs, WS_KV + (unsigned)(32 + h) * (64 * 4096), 0, 63, -1, -1, qb0, qb1, obp, lane);
        }
    }

    {
        const int wt = wid & 3, kh = wid >> 2;
        WAIT_BAR_L(4);
        int wa[4];
        { const int f = (2 * (g4 & 1) + (q4 >> 1)) << 1; const int row = 16 * (g4 >> 1) + 4 * (g4 & 1) + q4;
#pragma unroll
          for (int n = 0; n < 4; ++n) wa[n] = kh * 16384 + row * 128 + (((2 * n + (p4 >> 1)) ^ f) * 16) + (p4 & 1) * 8; }
        f32x4 accA[2][4], accB[2][4];
#pragma unroll
        for (int t = 0; t < 2; ++t)
#pragma unroll
            for (int n = 0; n < 4; ++n) { accA[t][n] = f32x4{}; accB[t][n] = f32x4{}; }
        bf16x8 wfA[4], wfB[4];
#define P3_HEAD(ACC, WC, WN, PN, OA, OB, HK, hb_) do { \
            _Pragma("unroll") for (int n = 0; n < 4; ++n) { \
                WN[n] = trfrag(L + (PN) + wa[n], 1024); \
                ACC[0][n] = MFMA16(WC[n], BC8(OA), ACC[0][n]); ACC[1][n] = MFMA16(WC[n], BC8(OB), ACC[1][n]); PIN(ACC[0][n]); PIN(ACC[1][n]); \
                HK((hb_) + n); SBAR(); } \
        } while (0)
#define P3_COMP(ACC, OF, sc_, sn_, HK) do { \
            P3_HEAD(ACC, wfA, wfB, (sc_) + 1 * 4096, OF[0], OF[1], HK, 0); P3_HEAD(ACC, wfB, wfA, (sc_) + 2 * 4096, OF[2], OF[3], HK, 4); \
            P3_HEAD(ACC, wfA, wfB, (sc_) + 3 * 4096, OF[4], OF[5], HK, 8); P3_HEAD(ACC, wfB, wfA, (sn_), OF[6], OF[7], HK, 12); \
        } while (0)
#define P3_GL(c, j) glds16((((c) >> 2) ? woB : woA) + p3off + (size_t)((c) & 3) * (128 * 2048) + (j) * 8 * 2048, p3vw, lds0 + P3_SLOT(c) + wid * 4096 + (j) * 1024)
#define P3_LA(OF, c, e) OF[e] = ld_sc1(rs, osrc + ((c) * 4 + ((e) >> 1)) * 2048 + ((e) & 1) * 256)
#define P3_LB(OF, a_, e) OF[e] = __builtin_amdgcn_raw_buffer_load_b128(rs, (int)(osrc + (32 + ((e) >> 1)) * 2048 + ((e) & 1) * 256), (int)((a_) * 8192), 16)
#define P3_E(i) (((i) - 4) & 7)
#define P3_ISO(i) ((i) >= 4 && (i) < 12)
        gu64* f3 = (gu64*)(ws + WS_FLAG3 + rb * 2048 + (kh * 8 + (lane & 7)) * 128);
        unsigned rdy = 0;
        auto wait_group = [&](unsigned a_) {
            const unsigned need = 3u << (2 * a_);
            for (unsigned spins = 0; (rdy & need) != need && spins < (1u << 18); ++spins) {
                const unsigned long long v = __hip_atomic_load(f3, RLX_AGENT);
                rdy = (unsigned)__builtin_amdgcn_readfirstlane((int)((unsigned)__ballot(v == tag) & 0xffu));
                if ((rdy & need) != need) __builtin_amdgcn_s_sleep(1);
            }
        };
#define P3_SLOTB(a_) ((a_) == 0 ? P3_SLOT(4) : (a_) == 1 ? P3_SLOT(5) : (a_) == 2 ? P3_SLOT(6) : P3_SLOT(7))
        const unsigned a0 = (xcd + 1) & 3, a1 = (xcd + 2) & 3, a2 = (xcd + 3) & 3, a3 = xcd & 3;
        const int sb0 = P3_SLOTB(a0), sb1 = P3_SLOTB(a1), sb2 = P3_SLOTB(a2), sb3 = P3_SLOTB(a3);
#pragma unroll
        for (int n = 0; n < 4; ++n) wfA[n] = trfrag(L + P3_SLOT(0) + wa[n], 1024);
#define HK(i) do { if ((i) < 4) P3_GL(4, (i) & 3); } while (0)
        P3_COMP(accA, o0, P3_SLOT(0), P3_SLOT(1), HK);
#undef HK
        LBAR();
#define HK(i) do { if ((i) < 4) P3_GL(5, (i) & 3); else if (P3_ISO(i)) P3_LA(o0, 3, P3_E(i)); } while (0)
        P3_COMP(accA, o1, P3_SLOT(1), P3_SLOT(2), HK);
#undef HK
        WAIT_BAR_L(16);
        if (tid == 0) __hip_atomic_store((gu64*)(ws + WS_FLAG3 + (qb * 16 + hp) * 128), tag, RLX_AGENT);
        wait_group(a0);
#define HK(i) do { if ((i) < 4) P3_GL(6, (i) & 3); else if (P3_ISO(i)) P3_LB(o1, a0, P3_E(i)); } while (0)
        P3_COMP(accA, o2, P3_SLOT(2), P3_SLOT(3), HK);
#undef HK
        LBAR();
        wait_group(a1);
#define HK(i) do { if ((i) < 4) P3_GL(7, (i) & 3); else if (P3_ISO(i)) P3_LB(o2, a1, P3_E(i)); } while (0)
        P3_COMP(accA, o0, P3_SLOT(3), P3_SLOT(3), HK);
#undef HK
        WAIT_BAR_L(8);
        wait_group(a2);
#pragma unroll
        for (int n = 0; n < 4; ++n) wfA[n] = trfrag(L + sb0 + wa[n], 1024);
#define HK(i) do { if (P3_ISO(i)) P3_LB(o0, a2, P3_E(i)); } while (0)
        P3_COMP(accB, o1, sb0, sb1, HK);
#undef HK
        wait_group(a3);
#define HK(i) do { if (P3_ISO(i)) P3_LB(o1, a3, P3_E(i)); } while (0)
        P3_COMP(accB, o2, sb1, sb2, HK);
#undef HK
#define HK(i) do {} while (0)
        P3_COMP(accB, o0, sb2, sb3, HK);
        P3_COMP(accB, o1, sb3, sb3, HK);
#undef HK
#undef P3_GL
#undef P3_LA
#undef P3_LB
#undef P3_COMP
#undef P3_HEAD
        LAS f32x4* ex = (LAS f32x4*)(LW + P3_SLOT(3) + wid * 4096) + lane;
        f32x4 mix[2][4];
#pragma unroll
        for (int t = 0; t < 2; ++t)
#pragma unroll
            for (int n = 0; n < 4; ++n) { const float gt = t ? g2 : g; mix[t][n] = accA[t][n] * gt + accB[t][n] * (1.0f - gt); }
#pragma unroll
        for (int t = 0; t < 2; ++t)
#pragma unroll
            for (int n = 0; n < 2; ++n) ex[(t * 2 + n) * 64] = kh ? mix[t][n] : mix[t][2 + n];
        LBAR();
        const LAS f32x4* ey = (const LAS f32x4*)(LW + P3_SLOT(3) + (wid ^ 4) * 4096) + lane;
#pragma unroll
        for (int t = 0; t < 2; ++t)
#pragma unroll
            for (int n = 0; n < 2; ++n) {
                const f32x4 y = ey[(t * 2 + n) * 64] + (kh ? mix[t][2 + n] : mix[t][n]);
                unsigned short* op = out + (size_t)(rb * 128 + wt * 32 + t * 16 + i16) * DM + cb * 64 + (2 * kh + n) * 16 + 4 * g4;
                *(u32x2*)op = (u32x2){cvtpk(y[0], y[1]), cvtpk(y[2], y[3])};
            }
    }
}

extern "C" void kernel_launch(void* const* d_in, const int* in_sizes, int n_in, void* d_out, int out_size, void* d_ws, size_t ws_size,
                              hipStream_t stream) {
    (void)hipFuncSetAttribute((const void*)fused_kernel, hipFuncAttributeMaxDynamicSharedMemorySize, LDS_BYTES);
    fused_kernel<<<256, 512, LDS_BYTES, stream>>>((const char*)d_in[0], (const char*)d_in[1], (const char*)d_in[2], (const char*)d_in[4], (const char*)d_in[5],
                                                  (const unsigned short*)d_in[6], (char*)d_ws, (unsigned short*)d_out);
}
```
